```python
import torch

def _softmax(x, axis=-1):
    return torch.softmax(x, dim=axis)
def _gelu(x):
    return torch.nn.functional.gelu(x, approximate="tanh")
def _silu(x):
    return torch.nn.functional.silu(x)
def _sigmoid(x):
    return torch.sigmoid(x)
def _layernorm(x, axis=-1, eps=1e-5):
    m = x.mean(dim=axis, keepdim=True)
    v = x.var(dim=axis, keepdim=True, unbiased=False)
    return (x - m) / torch.sqrt(v + eps)
def _scatter_add(tgt, idx, src, axis=0):
    return tgt.clone().index_add_(axis, idx, src)
def _segment_sum(src, seg, n):
    out = torch.zeros((n,) + src.shape[1:], dtype=src.dtype)
    return out.index_add_(0, seg, src)
def _pad(x, pw):
    flat = tuple(v for pair in reversed(pw) for v in pair)
    return torch.nn.functional.pad(x, flat)

kernel_name = 'standalone__seq2seq_rnn_t_joint__speech'

def setup_inputs():

    _w = (lambda a: (lambda t: t.to(torch.bfloat16) if t.is_floating_point() else t)(torch.as_tensor(a)))
    B, T, D, K = 2, 1000, 256, 15
    U = 8
    sc = (1.0 / (K * D)) ** 0.5
    sd = (1.0 / D) ** 0.5
    return {
        "x": _w((lambda *s: torch.randn(*s, dtype=torch.bfloat16) * 0.2)(B, T, D) * 0.5),
        "w1": _w((lambda *s: torch.randn(*s, dtype=torch.bfloat16) * 3.2)(K, D, D) * sc),
        "w2": _w((lambda *s: torch.randn(*s, dtype=torch.bfloat16) * 3.2)(K, D, D) * sc),
        "g": _w((lambda *s: torch.randn(*s, dtype=torch.bfloat16) * 3.2)(B, U, D) * 0.5),
        "w_f": _w((lambda *s: torch.randn(*s, dtype=torch.bfloat16) * 3.2)(D, D) * sd),
        "w_g": _w((lambda *s: torch.randn(*s, dtype=torch.bfloat16) * 3.2)(D, D) * sd),
        "w_o": _w((lambda *s: torch.randn(*s, dtype=torch.bfloat16) * 3.2)(D, D) * sd),
    }

def reference(x, w1, w2, g, w_f, w_g, w_o):
    B, T, D = x.shape
    K = w1.shape[0]
    pad = K // 2
    xp = _pad(x, ((0,0),(pad,pad),(0,0)))
    h = torch.zeros_like(x)
    for i in range(K):
        h = h + torch.einsum("btc,cd->btd", xp[:, i:i+T, :], w1[i])
    h = _gelu(h)[:, ::2, :]
    T1 = h.shape[1]
    hp = _pad(h, ((0,0),(pad,pad),(0,0)))
    f = torch.zeros_like(h)
    for i in range(K):
        f = f + torch.einsum("btc,cd->btd", hp[:, i:i+T1, :], w2[i])
    f = _gelu(f)[:, ::2, :]
    fe = torch.einsum("btd,de->bte", f, w_f)
    ge = torch.einsum("bud,de->bue", g, w_g)
    j = torch.tanh(fe[:, :, None, :] + ge[:, None, :, :])
    return torch.einsum("btud,dv->btuv", j, w_o)
```

```cpp
#include <hip/hip_runtime.h>
#include <hip/hip_bf16.h>

typedef unsigned short u16;
__device__ __forceinline__ float bf2f(u16 b) { return __builtin_bit_cast(float, (unsigned)b << 16); }
__device__ __forceinline__ u16 f2bf(float f) { __bf16 b = (__bf16)f; return __builtin_bit_cast(u16, b); }
__device__ __forceinline__ float gelu(float x) {
  float u = 0.7978845608028654f * (x + 0.044715f * x * x * x);
  return 0.5f * x * (1.0f + tanhf(u));
}
__global__ void conv_k(const u16* in, const u16* w, u16* out, int Tin, int Tout) {
  int row = blockIdx.x; int b = row / Tout, t = row % Tout; int d = threadIdx.x;
  float acc = 0.f;
  for (int i = 0; i < 15; ++i) {
    int r = 2 * t + i - 7;
    if (r < 0 || r >= Tin) continue;
    const u16* xr = in + ((size_t)b * Tin + r) * 256;
    const u16* wr = w + (size_t)i * 65536 + d;
    for (int c = 0; c < 256; ++c) acc += bf2f(xr[c]) * bf2f(wr[c * 256]);
  }
  out[(size_t)row * 256 + d] = f2bf(gelu(acc));
}
__global__ void joint_k(const u16* f, const u16* g, const u16* wf, const u16* wg, const u16* wo, u16* out) {
  __shared__ float j[8][256];
  int row = blockIdx.x; int b = row / 250; int e = threadIdx.x;
  float fe = 0.f;
  for (int d = 0; d < 256; ++d) fe += bf2f(f[row * 256 + d]) * bf2f(wf[d * 256 + e]);
  for (int u = 0; u < 8; ++u) {
    float ge = 0.f;
    for (int d = 0; d < 256; ++d) ge += bf2f(g[(b * 8 + u) * 256 + d]) * bf2f(wg[d * 256 + e]);
    j[u][e] = bf2f(f2bf(tanhf(fe + ge)));
  }
  __syncthreads();
  for (int u = 0; u < 8; ++u) {
    float o = 0.f;
    for (int d = 0; d < 256; ++d) o += j[u][d] * bf2f(wo[d * 256 + e]);
    out[((size_t)row * 8 + u) * 256 + e] = f2bf(o);
  }
}

extern "C" void kernel_launch(void* const* d_in, const int* in_sizes, int n_in,
                              void* d_out, int out_size, void* d_ws, size_t ws_size,
                              hipStream_t stream) {
  const u16* x = (const u16*)d_in[0]; const u16* w1 = (const u16*)d_in[1]; const u16* w2 = (const u16*)d_in[2];
  const u16* g = (const u16*)d_in[3]; const u16* wf = (const u16*)d_in[4]; const u16* wg = (const u16*)d_in[5]; const u16* wo = (const u16*)d_in[6];
  u16* h = (u16*)d_ws; u16* f = h + 1000 * 256;
  conv_k<<<1000, 256, 0, stream>>>(x, w1, h, 1000, 500);
  conv_k<<<500, 256, 0, stream>>>(h, w2, f, 500, 250);
  joint_k<<<500, 256, 0, stream>>>(f, g, wf, wg, wo, (u16*)d_out);
}
```

```cpp
#include <hip/hip_runtime.h>
#include <hip/hip_bf16.h>
#include <type_traits>

typedef unsigned u32;
typedef unsigned short u16;
typedef u32 v4u __attribute__((ext_vector_type(4)));
typedef u32 v2u __attribute__((ext_vector_type(2)));
typedef float f32x4 __attribute__((ext_vector_type(4)));
typedef float f32x2 __attribute__((ext_vector_type(2)));
typedef short s16x4 __attribute__((ext_vector_type(4)));
typedef __bf16 bf16x8 __attribute__((ext_vector_type(8)));
typedef __bf16 bf16x2 __attribute__((ext_vector_type(2)));
typedef __amdgpu_buffer_rsrc_t rsrc_t;
#define GAS __attribute__((address_space(1)))
#define LAS __attribute__((address_space(3)))
#define DI __device__ __forceinline__

constexpr u32 WS_EPOCH = 0;
constexpr u32 WS_FLAGS = 4096;
constexpr u32 WS_GE = 65536;
constexpr u32 WS_P1 = 1u << 20;
constexpr u32 WS_P2 = 3u << 20;
constexpr u32 WS_BYTES = 5u << 20;
constexpr u32 OOB = 0x80000000u;
constexpr u32 SENT = 0xFFFFFFFFu;
constexpr u32 SPIN_CAP = 100000;
constexpr u32 LSPIN_CAP = 4000000;
constexpr u32 L_RING = 0;
constexpr u32 L_X = 131072;
constexpr u32 L_F = L_X + 19008;
constexpr u32 L_J = L_F + 1152;
constexpr u32 L_JUNK = L_J + 8192;
constexpr u32 L_HP = L_JUNK + 1024;
constexpr u32 L_CP = L_HP + 16;
constexpr u32 LDS_BYTES = L_CP + 16;
static_assert(LDS_BYTES <= 163840, "lds");
#define CODE_LINES 112
#define LOOK 4
#define SOFF 0
#define CPRIO 1
#define DLY1 0
#define DLY2 0

template <int I, int N, class F> DI void static_for(F&& f) {
  if constexpr (I < N) { f(std::integral_constant<int, I>{}); static_for<I + 1, N>(f); }
}
template <int N> DI void wait_vm() { asm volatile("s_waitcnt vmcnt(%0)" ::"i"(N) : "memory"); }
DI void bar() { asm volatile("s_waitcnt lgkmcnt(0)\n\ts_barrier" ::: "memory"); }
DI void fence_sched() { __builtin_amdgcn_sched_barrier(0); }
DI void mem_order() { asm volatile("" ::: "memory"); }
DI u32 rfl(u32 x) { return __builtin_amdgcn_readfirstlane(x); }
#define WAUX ""
DI void dma16(u32 lds, u32 voff, rsrc_t rs, u32 soff) {
  asm volatile("s_mov_b32 m0, %0\n\ts_nop 0\n\tbuffer_load_dwordx4 %1, %2, %3 offen lds" WAUX ::"s"(lds), "v"(voff), "s"(rs), "s"(soff) : "memory");
}
DI void dma4(u32 lds, u32 voff, rsrc_t rs, u32 soff) {
  asm volatile("s_mov_b32 m0, %0\n\ts_nop 0\n\tbuffer_load_dword %1, %2, %3 offen lds" ::"s"(lds), "v"(voff), "s"(rs), "s"(soff) : "memory");
}
DI rsrc_t mk(const void* p, u32 bytes) { return __builtin_amdgcn_make_buffer_rsrc((void*)p, 0, (int)bytes, 0x00020000); }
DI v4u lds128(u32 a) { return *(const LAS v4u*)(size_t)a; }
DI v2u ldstr(u32 a) { return __builtin_bit_cast(v2u, __builtin_amdgcn_ds_read_tr16_b64_v4i16((LAS s16x4*)(size_t)a)); }
DI v4u ldstr2(u32 a0, u32 a1) { v2u x = ldstr(a0), y = ldstr(a1); return v4u{x.x, x.y, y.x, y.y}; }
DI f32x4 mfma(v4u a, v4u b, f32x4 c) {
  return __builtin_amdgcn_mfma_f32_16x16x32_bf16(__builtin_bit_cast(bf16x8, a), __builtin_bit_cast(bf16x8, b), c, 0, 0, 0);
}
DI u32 pk(float a, float b) { f32x2 f = {a, b}; return __builtin_bit_cast(u32, __builtin_convertvector(f, bf16x2)); }
DI float lo16(u32 x) { return __builtin_bit_cast(float, x << 16); }
DI float hi16(u32 x) { return __builtin_bit_cast(float, x & 0xFFFF0000u); }
DI float gelu(float x) {
  const float c0 = -2.0f * 0.7978845608028654f * 1.4426950408889634f, c1 = c0 * 0.044715f;
  float e = __builtin_amdgcn_exp2f(x * __builtin_fmaf(x * x, c1, c0));
  return x * __builtin_amdgcn_rcpf(1.0f + e);
}
DI float tanh_(float x) {
  float e = __builtin_amdgcn_exp2f(x * 2.8853900817779268f);
  return __builtin_fmaf(-2.0f, __builtin_amdgcn_rcpf(1.0f + e), 1.0f);
}
DI u32 nosent(u32 x) { return min(x, SENT - 1); }
DI v4u pack2(f32x4 a, f32x4 b) { return v4u{nosent(pk(a.x, a.y)), nosent(pk(a.z, a.w)), nosent(pk(b.x, b.y)), nosent(pk(b.z, b.w))}; }
DI void st_sc1(v4u v, rsrc_t rs, u32 off) { __builtin_amdgcn_raw_buffer_store_b128(v, rs, off, 0, 16); }
DI u32 max4(v4u v) { return max(max(v.x, v.y), max(v.z, v.w)); }
DI u32 min4(v4u v) { return min(min(v.x, v.y), min(v.z, v.w)); }
DI u32 prog_read(u32 va) { u32 v = *(const volatile LAS u32*)(size_t)va; mem_order(); return v; }
DI void prog_write(u32 a, u32 v) { mem_order(); *(volatile LAS u32*)(size_t)a = v; mem_order(); }
template <u32 NEED, int SLEEP> DI void prog_ensure(u32 hv, u32 va) {
  u32 cnt;
  asm volatile("v_cmp_gt_u32 vcc, %3, %0\n\ts_cbranch_vccz Ld%=\n\ts_mov_b32 %1, %5\n"
               "Lr%=:\n\ts_sleep %4\n\tds_read_b32 %0, %2\n\ts_waitcnt lgkmcnt(0)\n\tv_cmp_gt_u32 vcc, %3, %0\n\ts_cbranch_vccz Ld%=\n\t"
               "s_sub_u32 %1, %1, 1\n\ts_cbranch_scc0 Lr%=\n"
               "Ld%=:"
               : "+v"(hv), "=&s"(cnt) : "v"(va), "i"(NEED), "i"(SLEEP), "i"(LSPIN_CAP) : "memory", "scc", "vcc");
}
template <u32 NEED, int SLEEP> DI void prog_wait(u32 va) { prog_ensure<NEED, SLEEP>(prog_read(va), va); }
template <int N, u32 STEP, u32 SMASK, bool PUB> DI void rounds(u32& m, u32& so, u32& pv, u32 va, u32 vb, rsrc_t rs, u32 hp) {
  u32 cnt;
  if constexpr (PUB)
    asm volatile("s_mov_b32 %3, %9\n"
               "L%=:\n\ts_mov_b32 m0, %0\n\ts_add_u32 %0, %0, 0x1000\n\tbuffer_load_dwordx4 %4, %6, %1 offen lds" WAUX "\n\t"
               "s_mov_b32 m0, %0\n\ts_add_u32 %0, %0, 0x1000\n\tbuffer_load_dwordx4 %5, %6, %1 offen lds" WAUX "\n\t"
               "s_and_b32 %0, %0, 0x1ffff\n\ts_add_u32 %1, %1, %8\n\ts_and_b32 %1, %1, %10\n\tv_add_u32 %2, 1, %2\n\t"
               "s_waitcnt vmcnt(%11)\n\tds_write_b32 %7, %2\n\ts_sub_u32 %3, %3, 1\n\ts_cbranch_scc0 L%="
               : "+s"(m), "+s"(so), "+v"(pv), "=&s"(cnt)
               : "v"(va), "v"(vb), "s"(rs), "v"(hp), "i"(STEP), "i"(N - 1), "i"(SMASK), "i"(2 * LOOK) : "memory", "scc");
  else
    asm volatile("s_mov_b32 %2, %7\n"
               "L%=:\n\ts_mov_b32 m0, %0\n\ts_add_u32 %0, %0, 0x1000\n\tbuffer_load_dwordx4 %3, %5, %1 offen lds" WAUX "\n\t"
               "s_mov_b32 m0, %0\n\ts_add_u32 %0, %0, 0x1000\n\tbuffer_load_dwordx4 %4, %5, %1 offen lds" WAUX "\n\t"
               "s_and_b32 %0, %0, 0x1ffff\n\ts_add_u32 %1, %1, %6\n\ts_and_b32 %1, %1, %8\n\t"
               "s_sub_u32 %2, %2, 1\n\ts_cbranch_scc0 L%="
               : "+s"(m), "+s"(so), "=&s"(cnt)
               : "v"(va), "v"(vb), "s"(rs), "i"(STEP), "i"(N - 1), "i"(SMASK) : "memory", "scc");
}
DI void delay(int n) { for (int i = 0; i < n; ++i) __builtin_amdgcn_s_sleep(1); }

#define SSTAMP(i)
#define STAMP(i)

struct Args { const u16 *x, *w1, *w2, *gp, *wf, *wg, *wo; u16* out; unsigned char* ws; };

constexpr u32 C_S8 = 1, C_S12 = 2, C_P1 = 3, C_W2A = 4, C_W2B = 5, C_W2C = 6, C_A = 7, C_WFA = 8, C_WFB = 9, C_F = 10;

__global__ void __launch_bounds__(512, 2) rnnt_fused(Args a) {
  __shared__ __attribute__((aligned(16))) unsigned char smem[LDS_BYTES];
  const u32 LB = (u32)(size_t)(LAS unsigned char*)smem;
  STAMP(0);
  u32 tid = threadIdx.x;
  {
    unsigned long long pc;
    asm volatile("s_mov_b32 m0, %2\n\ts_getpc_b64 %0\n\tglobal_load_lds_dword %1, %0" : "=&s"(pc) : "v"(min(tid, (u32)CODE_LINES - 1) << 7), "s"(LB + L_JUNK) : "memory");
    if (tid < 8) *(volatile LAS u32*)(size_t)(LB + L_HP + tid * 4) = 0;
  }
  const u32 wv = rfl(tid >> 6), w4 = wv & 3;
  const u32 bid = blockIdx.x, x8 = bid & 7, j = bid >> 3, mt = j >> 1, mloc = mt & 7, bat = mt >> 3;
  const u32 k1 = x8 >> 1, n1 = 2 * (x8 & 1) + (j & 1);
  const u32 c2 = x8, n2 = j & 1;
  const u32 idx16 = x8 * 2 + (j & 1), pi = mt * 16 + idx16, ploc = pi & 127;
  const bool p3ok = ploc < 125;

  u32 lane = tid & 63;
  const rsrc_t rw1 = mk(a.w1, 15 * 131072);
  u32 xv, w1v;
  {
    const u32 rr8 = lane >> 3;
    xv = (u32)((int)(mloc * 128 + w4 * 8 + rr8) - 7) * 512 + k1 * 128 + (lane & 7) * 16;
    w1v = (k1 * 64 + w4 * 8 + rr8) * 512 + n1 * 128 + ((((lane >> 1) & 3) ^ (((rr8 >> 1) & 1) + 2 * (w4 & 1))) * 32) + (lane & 1) * 16;
    constexpr u32 NT = 15 - LOOK;
    const u32 ln = (mt * 4 + w4) * NT + lane;
    const u32 tv = (LOOK + (ln >> 6)) * 131072 + (k1 * 64 + (ln & 63)) * 512 + n1 * 128;
    if (wv < 4) xv = lane < NT ? tv : OOB;
    asm volatile("" : "+v"(xv), "+v"(w1v));
  }
  bar();
  if (wv < 4) dma4(LB + L_JUNK + 256, xv, rw1, 0);

  if (__builtin_expect(wv >= 4, 1)) {
    const rsrc_t rx = mk(a.x + bat * 256000, 512000);
    static_for<0, 4>([&](auto I) { dma16(LB + L_X + (w4 + 4 * I) * 1056, xv + I * 16384, rx, 0); });
    dma16(LB + L_X + ((w4 & 1) + 16) * 1056, w4 < 2 ? xv + 4 * 16384 : xv + 3 * 16384 + 8192, rx, 0);
    u32 m = LB + L_RING + w4 * 1024, so = 0, pv = 0;
    const u32 hp = LB + L_HP + w4 * 4, cpv = LB + L_CP + (lane & 3) * 4;
    rounds<LOOK - 1, 131072, ~0u, false>(m, so, pv, w1v, w1v + 16384, rw1, hp);
    STAMP(1);
    rounds<16 - LOOK, 131072, ~0u, true>(m, so, pv, w1v, w1v + 16384, rw1, hp);
    STAMP(2);
    asm volatile("" : "+v"(lane));
    const u32 g = lane >> 4;
    const rsrc_t rw2 = mk(a.w2, 15 * 131072);
    const u32 w2v = (c2 * 32 + w4 * 4 + g) * 512 + n2 * 256 + ((((lane >> 1) & 7) ^ (g | (((w4 >> 1) & 1) << 2))) * 32) + (lane & 1) * 16;
    so = 0;
    rounds<1, 131072, ~0u, true>(m, so, pv, w2v, w2v + 8192, rw2, hp);
    prog_wait<C_S8, 1>(cpv);
    rounds<7, 131072, ~0u, true>(m, so, pv, w2v, w2v + 8192, rw2, hp);
    prog_wait<C_S12, 1>(cpv);
    rounds<4, 131072, ~0u, true>(m, so, pv, w2v, w2v + 8192, rw2, hp);
    prog_wait<C_P1, 1>(cpv);
    rounds<3, 131072, ~0u, true>(m, so, pv, w2v, w2v + 8192, rw2, hp);
    STAMP(3);
    const rsrc_t rws = mk(a.ws, WS_BYTES);
    const u32 l15 = lane & 15;
    if (!p3ok) {
      wait_vm<0>();
      prog_write(hp, 31);
      if (ploc >= 126) {
        const u32 et = (bat * 2 + (pi & 1)) * 4 + w4;
        const rsrc_t rwg = mk(a.wg, 131072);
        u32 e[64]; v4u b[8];
        const u32 eo = 8 * g * 512 + (et * 16 + l15) * 2;
        static_for<0, 64>([&](auto J) { e[J] = __builtin_amdgcn_raw_buffer_load_b16(rwg, eo + (J & 7) * 512, (J >> 3) * 16384, 0); });
        static_for<0, 8>([&](auto K) { b[K] = *(const GAS v4u*)(a.gp + l15 * 256 + K * 32 + 8 * g); });
        fence_sched();
        f32x4 d = {0, 0, 0, 0};
        static_for<0, 8>([&](auto K) {
          d = mfma(v4u{e[8 * K] | (e[8 * K + 1] << 16), e[8 * K + 2] | (e[8 * K + 3] << 16), e[8 * K + 4] | (e[8 * K + 5] << 16), e[8 * K + 6] | (e[8 * K + 7] << 16)}, b[K], d);
        });
        v4u o = __builtin_bit_cast(v4u, d);
        st_sc1(v4u{nosent(o.x), nosent(o.y), nosent(o.z), nosent(o.w)}, rws, WS_GE + l15 * 1024 + et * 64 + g * 16);
      } else if (pi == 125 && w4 == 0) {
        u32 epoch;
        asm volatile("s_load_dword %0, %1, 0x0\n\ts_waitcnt lgkmcnt(0)" : "=s"(epoch) : "s"(a.ws) : "memory");
        epoch += 1;
        for (u32 spins = 0; spins < SPIN_CAP; ++spins) {
          v4u f4;
          asm volatile("buffer_load_dwordx4 %0, %1, %2, 0 offen sc1\n\ts_waitcnt vmcnt(0)" : "=v"(f4) : "v"(WS_FLAGS + (lane >> 2) * 128 + (lane & 3) * 16), "s"(rws) : "memory");
          if (__all(f4.x == epoch && f4.y == epoch && f4.z == epoch && f4.w == epoch)) break;
        }
        if (lane == 0) __hip_atomic_store((GAS u32*)(a.ws + WS_EPOCH), epoch, __ATOMIC_RELAXED, __HIP_MEMORY_SCOPE_AGENT);
      }
      return;
    }
    const u32 r2 = 2 * w4 + (lane >> 5);
    const u32 wfv = r2 * 512 + ((((lane >> 1) & 15) ^ r2) * 32) + (lane & 1) * 16;
    const rsrc_t rwf = mk(a.wf, 131072), rwo = mk(a.wo, 131072);
    so = 14 * 8192;
    rounds<1, 8192, 0x1ffff, true>(m, so, pv, wfv, wfv + 4096, rwf, hp);
    prog_wait<C_W2A, 1>(cpv);
    rounds<5, 8192, 0x1ffff, true>(m, so, pv, wfv, wfv + 4096, rwf, hp);
    prog_wait<C_W2B, 1>(cpv);
    rounds<5, 8192, 0x1ffff, true>(m, so, pv, wfv, wfv + 4096, rwf, hp);
    prog_wait<C_W2C, 1>(cpv);
    rounds<5, 8192, 0x1ffff, true>(m, so, pv, wfv, wfv + 4096, rwf, hp);
    wait_vm<0>();
    prog_write(hp, 47);
    STAMP(4);
    m = LB + L_RING + w4 * 1024; so = 0;
    prog_wait<C_WFA, 1>(cpv);
    rounds<8, 8192, 0x1ffff, false>(m, so, pv, wfv, wfv + 4096, rwo, hp);
    prog_wait<C_WFB, 1>(cpv);
    rounds<8, 8192, 0x1ffff, false>(m, so, pv, wfv, wfv + 4096, rwo, hp);
    STAMP(5);
    wait_vm<0>();
    STAMP(6);
  } else {
    __builtin_amdgcn_s_setprio(CPRIO);
    const rsrc_t rws = mk(a.ws, WS_BYTES);
    const u32 p1off = WS_P1 + k1 * 524288 + (2 * n1 + (wv >> 1)) * 65536 + mt * 4096 + (wv & 1) * 2048;
    const u32 p2off = WS_P2 + c2 * 262144 + mt * 16384 + n2 * 8192 + wv * 2048;
    const v4u sv = {SENT, SENT, SENT, SENT};
    u32 epoch;
    st_sc1(sv, rws, p1off + lane * 16);
    st_sc1(sv, rws, p1off + 1024 + lane * 16);
    asm volatile("s_load_dword %0, %1, 0x0" : "=s"(epoch) : "s"(a.ws) : "memory");
    asm volatile("" : "+v"(lane));
    const u32 l15 = lane & 15, g = lane >> 4, q4 = l15 >> 2, p4 = lane & 3;
    const u32 cp = LB + L_CP + wv * 4, cpv = LB + L_CP + p4 * 4, hpv = LB + L_HP + p4 * 4;
    f32x4 acc[4] = {{0, 0, 0, 0}, {0, 0, 0, 0}, {0, 0, 0, 0}, {0, 0, 0, 0}};
    u32 fl;
    const u32 fline = lane < 48 ? min(max(mt + g, 1u) - 1, 15u) : 7 + (lane & 8);
    const u32 foff = WS_FLAGS + fline * 128 + (lane < 48 ? l15 : 8 + (lane & 7)) * 4;
    u32 t2v, t3v, gsv, flv;
    {
      const u32 ln = (mt * 4 + wv) * 15 + lane;
      t2v = lane < 15 ? (ln >> 6) * 131072 + (c2 * 32 + ((ln >> 1) & 31)) * 512 + n2 * 256 + (ln & 1) * 128 : OOB;
      t3v = lane < 16 ? ((mt * 4 + wv) * 16 + lane) * 128 : OOB;
      gsv = ploc >= 126 ? WS_GE + (wv * 4 + g) * 1024 + (bat * 2 + (pi & 1)) * 256 + l15 * 16 : OOB;
      flv = tid == 0 ? WS_FLAGS + mt * 128 + idx16 * 4 : OOB;
    }
    const rsrc_t rw2 = mk(a.w2, 15 * 131072), rw3 = mk((j & 1) ? a.wo : a.wf, 131072);
    asm volatile("s_waitcnt lgkmcnt(0)" : "+s"(epoch), "+v"(t2v), "+v"(t3v), "+v"(gsv), "+v"(flv)::"memory");
    epoch += 1;
    STAMP(1);
    {
      v4u F[2][21];
      v4u Wf[2][2];
      const u32 xr = LB + L_X + l15 * 1056 + g * 16;
      const u32 w1r = LB + L_RING + g * 1024 + q4 * 128 + ((wv ^ ((q4 >> 1) + 2 * (g & 1))) * 32) + p4 * 8;
      prog_wait<1, 0>(hpv);
      STAMP(2);
      static_for<0, 8>([&](auto Q) { F[0][Q] = lds128(xr + Q * 128); F[1][Q] = lds128(xr + Q * 128 + 64); });
      prog_wait<2, 0>(hpv);
      static_for<0, 15>([&](auto I) {
        constexpr int i = I;
        Wf[i & 1][0] = ldstr2(w1r + i * 8192, w1r + i * 8192 + 512);
        Wf[i & 1][1] = ldstr2(w1r + i * 8192 + 4096, w1r + i * 8192 + 4096 + 512);
        if constexpr (i >= 2) {
          F[0][i + 6] = lds128(xr + ((i + 6) >> 3) * 1056 + ((i + 6) & 7) * 128);
          F[1][i + 6] = lds128(xr + ((i + 6) >> 3) * 1056 + ((i + 6) & 7) * 128 + 64);
        }
        fence_sched();
        if constexpr (i >= 1) static_for<0, 4>([&](auto J) { acc[J] = mfma(Wf[(i - 1) & 1][0], F[0][2 * J + i - 1], acc[J]); });
        fence_sched();
        u32 hv = 0, cv = 0;
        if constexpr (i < 14) hv = prog_read(hpv);
        if constexpr (i == 10) cv = prog_read(cpv);
        fence_sched();
        if constexpr (i >= 1) static_for<0, 4>([&](auto J) { acc[J] = mfma(Wf[(i - 1) & 1][1], F[1][2 * J + i - 1], acc[J]); });
        fence_sched();
        if constexpr (i == 2) {
          st_sc1(sv, rws, p2off + lane * 16);
          st_sc1(sv, rws, p2off + 1024 + lane * 16);
          st_sc1(sv, rws, gsv);
        }
        if constexpr (i == 4) dma4(LB + L_JUNK, t2v, rw2, 0);
        if constexpr (i == 9) dma4(LB + L_JUNK, t3v, rw3, 0);
        if constexpr (i == 8) { wait_vm<0>(); prog_write(cp, C_S8); }
        if constexpr (i == 10) {
          prog_ensure<C_S8, 0>(cv, cpv);
          __builtin_amdgcn_raw_buffer_store_b32(epoch, rws, flv, 0, 16);
        }
        if constexpr (i == 12) prog_write(cp, C_S12);
        if constexpr (i < 14) prog_ensure<i + 3, 0>(hv, hpv);
        fence_sched();
        SSTAMP(i + 1);
      });
      static_for<0, 2>([&](auto C) { static_for<0, 4>([&](auto J) { acc[J] = mfma(Wf[0][C], F[C][2 * J + 14], acc[J]); }); });
    }
    STAMP(3);
    st_sc1(pack2(acc[0], acc[1]), rws, p1off + l15 * 64 + g * 16);
    st_sc1(pack2(acc[2], acc[3]), rws, p1off + 1024 + l15 * 64 + g * 16);
    prog_write(cp, C_P1);
    asm volatile("buffer_load_dword %0, %1, %2, 0 offen sc1" : "=v"(fl) : "v"(foff), "s"(rws) : "memory");
    STAMP(4);
    v4u W2[2][15];
    const u32 w2r = LB + L_RING + (8 * g + q4) * 256 + (((2 * wv) ^ (q4 | ((g & 1) << 2))) * 32) + p4 * 8;
    auto rdw2 = [&](auto T) {
      constexpr u32 s = (T + 15) & 15;
      W2[0][T] = ldstr2(w2r + s * 8192, w2r + s * 8192 + 1024);
      W2[1][T] = ldstr2((w2r ^ 32) + s * 8192, (w2r ^ 32) + s * 8192 + 1024);
    };
    auto lds_done = [&]() { asm volatile("s_waitcnt lgkmcnt(0)" ::: "memory"); };
    prog_wait<21, 0>(hpv);
    static_for<0, 5>(rdw2);
    lds_done(); prog_write(cp, C_W2A);
    v4u hp[4]; u32 hh[4];
    u32 rho, rhoh, h1off, h1offh;
    {
      const u32 mm = lane >> 2;
      rho = 8 + 4 * mm + 2 * (wv & 1);
      h1off = mloc * 64 + rho - 8 < 500 ? WS_P1 + c2 * 65536 + mt * 4096 + wv * 1024 + lane * 16 : OOB;
      rho = LB + L_X + rho * 64 + (rho >> 2) * 32 + (wv >> 1) * 32 + p4 * 8;
      const u32 nx = lane >> 5, mh = (lane >> 4) & 1, dw = lane & 3;
      rhoh = 72 * nx + 4 * mh + 2 * (wv & 1) + (dw >> 1);
      const int hrow = (int)(mloc * 64 + rhoh) - 8;
      h1offh = (hrow >= 0 && hrow < 500) ? WS_P1 + c2 * 65536 + (mt + 2 * nx - 1) * 4096 + wv * 1024 + (nx ? 0 : 896) + (lane & 31) * 4 : OOB;
      rhoh = LB + L_X + rhoh * 64 + (rhoh >> 2) * 32 + (wv >> 1) * 32 + q4 * 8 + (dw & 1) * 4;
    }
    asm volatile("s_waitcnt vmcnt(0)" : "+v"(fl)::"memory");
    for (u32 spins = 0; !__all(fl == epoch) && spins < SPIN_CAP; ++spins)
      asm volatile("buffer_load_dword %0, %1, %2, 0 offen sc1\n\ts_waitcnt vmcnt(0)" : "=v"(fl) : "v"(foff), "s"(rws) : "memory");
    STAMP(5);
    delay(DLY1);
    auto h1_load = [&]() {
      static_for<0, 4>([&](auto K) { hp[K] = __builtin_amdgcn_raw_buffer_load_b128(rws, h1off, K * 524288, 16); });
      static_for<0, 4>([&](auto K) { hh[K] = __builtin_amdgcn_raw_buffer_load_b32(rws, h1offh, K * 524288, 16); });
      mem_order();
    };
    h1_load();
    prog_wait<26, 0>(hpv);
    static_for<5, 10>(rdw2);
    lds_done(); prog_write(cp, C_W2B);
    prog_wait<31, 0>(hpv);
    static_for<10, 15>(rdw2);
    lds_done(); prog_write(cp, C_W2C);
    STAMP(6);
    for (u32 spins = 0;; ++spins) {
      const u32 mx = max(max(max(max4(hp[0]), max4(hp[1])), max(max4(hp[2]), max4(hp[3]))), max(max(hh[0], hh[1]), max(hh[2], hh[3])));
      if (__all(mx != SENT) || spins >= SPIN_CAP) break;
      h1_load();
    }
    STAMP(7);
    {
      float v[8];
      static_for<0, 4>([&](auto D) {
        v[2 * D] = gelu((lo16(hp[0][+D]) + lo16(hp[1][+D])) + (lo16(hp[2][+D]) + lo16(hp[3][+D])));
        v[2 * D + 1] = gelu((hi16(hp[0][+D]) + hi16(hp[1][+D])) + (hi16(hp[2][+D]) + hi16(hp[3][+D])));
      });
      const float u0 = gelu((lo16(hh[0]) + lo16(hh[1])) + (lo16(hh[2]) + lo16(hh[3])));
      const float u1 = gelu((hi16(hh[0]) + hi16(hh[1])) + (hi16(hh[2]) + hi16(hh[3])));
      *(LAS v2u*)(size_t)(rho) = v2u{pk(v[0], v[1]), pk(v[2], v[3])};
      *(LAS v2u*)(size_t)(rho + 64) = v2u{pk(v[4], v[5]), pk(v[6], v[7])};
      *(LAS u32*)(size_t)(rhoh) = pk(u0, u1);
    }
    prog_write(cp, C_A);
    STAMP(8);
    prog_wait<C_A, 0>(cpv);
    STAMP(9);
    {
      static_for<0, 4>([&](auto J) { acc[J] = f32x4{0, 0, 0, 0}; });
      const u32 ar = LB + L_X + l15 * 288 + g * 16;
      v4u A[17];
      static_for<0, 17>([&](auto Q) { A[Q] = lds128(ar + ((Q + 1) >> 2) * 288 + ((Q + 1) & 3) * 64); });
      static_for<0, 15>([&](auto T) {
        acc[0] = mfma(W2[0][T], A[T], acc[0]);
        acc[2] = mfma(W2[1][T], A[T], acc[2]);
        acc[1] = mfma(W2[0][T], A[T + 2], acc[1]);
        acc[3] = mfma(W2[1][T], A[T + 2], acc[3]);
      });
      st_sc1(pack2(acc[0], acc[1]), rws, p2off + l15 * 64 + g * 16);
      st_sc1(pack2(acc[2], acc[3]), rws, p2off + 1024 + l15 * 64 + g * 16);
    }
    STAMP(10);
    if (!p3ok) return;
    v4u WF[8][4];
    const u32 wr = LB + L_RING + (4 * g + q4) * 512 + (((4 * wv) ^ ((4 * g + q4) & 7)) * 32) + p4 * 8;
    auto rdwf = [&](auto K) { static_for<0, 4>([&](auto T) { WF[K][T] = ldstr2((wr ^ (T * 32)) + K * 16384, (wr ^ (T * 32)) + K * 16384 + 8192); }); };
    prog_wait<41, 0>(hpv);
    static_for<0, 4>(rdwf);
    lds_done(); prog_write(cp, C_WFA);
    prog_wait<47, 0>(hpv);
    static_for<4, 8>(rdwf);
    lds_done(); prog_write(cp, C_WFB);
    STAMP(11);
    delay(DLY2);
    u32 fp[8];
    v4u ge[4];
    {
      const u32 h2off = WS_P2 + mt * 16384 + (tid >> 4) * 1024 + idx16 * 64 + l15 * 4;
      const u32 geoff = WS_GE + (bat * 8 + (lane & 7)) * 1024 + wv * 256 + g * 16;
      for (u32 spins = 0;; ++spins) {
        mem_order();
        static_for<0, 8>([&](auto K) { fp[K] = __builtin_amdgcn_raw_buffer_load_b32(rws, h2off, K * 262144, 16); });
        static_for<0, 4>([&](auto T) { ge[T] = __builtin_amdgcn_raw_buffer_load_b128(rws, geoff + T * 64, 0, 16); });
        u32 mx = max(max(max4(ge[0]), max4(ge[1])), max(max4(ge[2]), max4(ge[3])));
        static_for<0, 8>([&](auto K) { mx = max(mx, fp[K]); });
        if (__all(mx != SENT) || spins >= SPIN_CAP) break;
      }
      STAMP(12);
      float s0 = 0.f, s1 = 0.f;
      static_for<0, 8>([&](auto K) { s0 += lo16(fp[K]); s1 += hi16(fp[K]); });
      const u32 pos = (tid >> 5) * 32 + q4 * 8 + ((tid >> 4) & 1) * 4 + (tid & 1) * 2;
      *(LAS u32*)(size_t)(LB + L_F + ((tid >> 1) & 1) * 576 + pos * 2) = pk(gelu(s0), gelu(s1));
    }
    prog_write(cp, C_F);
    prog_wait<C_F, 0>(cpv);
    {
      f32x4 d[4];
      static_for<0, 4>([&](auto T) { d[T] = __builtin_bit_cast(f32x4, ge[T]); });
      const u32 fr = LB + L_F + (l15 >> 3) * 576 + g * 16;
      v4u b[8];
      static_for<0, 8>([&](auto K) { b[K] = lds128(fr + K * 64); });
      static_for<0, 2>([&](auto H) {
        static_for<0, 8>([&](auto K) { d[2 * H] = mfma(WF[K][2 * H], b[K], d[2 * H]); d[2 * H + 1] = mfma(WF[K][2 * H + 1], b[K], d[2 * H + 1]); });
      });
      static_for<0, 2>([&](auto H) {
        const f32x4 d0 = d[2 * H], d1 = d[2 * H + 1];
        v4u jf = {pk(tanh_(d0.x), tanh_(d0.y)), pk(tanh_(d0.z), tanh_(d0.w)), pk(tanh_(d1.x), tanh_(d1.y)), pk(tanh_(d1.z), tanh_(d1.w))};
        *(LAS v4u*)(size_t)(LB + L_J + (2 * wv + H) * 1024 + lane * 16) = jf;
      });
    }
    STAMP(13);
  }
  bar();
  {
    asm volatile("" : "+v"(lane));
    const u32 l15 = lane & 15, g = lane >> 4, q4 = l15 >> 2, p4 = lane & 3;
    const u32 wr0 = LB + L_RING + (4 * g + q4) * 512 + (((2 * wv) ^ ((4 * g + q4) & 7)) * 32) + p4 * 8;
    const u32 wr1 = wr0 ^ 32;
    f32x4 d0 = {0, 0, 0, 0}, d1 = {0, 0, 0, 0};
    static_for<0, 8>([&](auto K) {
      const v4u b = lds128(LB + L_J + K * 1024 + lane * 16);
      d0 = mfma(ldstr2(wr0 + K * 16384, wr0 + K * 16384 + 8192), b, d0);
      d1 = mfma(ldstr2(wr1 + K * 16384, wr1 + K * 16384 + 8192), b, d1);
    });
    u16* op = a.out + ((bat * 250 + 2 * ploc) * 8 + l15) * 256 + wv * 32 + 4 * g;
    *(GAS v2u*)(op) = v2u{pk(d0.x, d0.y), pk(d0.z, d0.w)};
    *(GAS v2u*)(op + 16) = v2u{pk(d1.x, d1.y), pk(d1.z, d1.w)};
  }
}

extern "C" void kernel_launch(void* const* d_in, const int* in_sizes, int n_in, void* d_out, int out_size, void* d_ws, size_t ws_size,
                              hipStream_t stream) {
  Args a{(const u16*)d_in[0], (const u16*)d_in[1], (const u16*)d_in[2], (const u16*)d_in[3], (const u16*)d_in[4],
         (const u16*)d_in[5], (const u16*)d_in[6], (u16*)d_out, (unsigned char*)d_ws};
  rnnt_fused<<<256, 512, 0, stream>>>(a);
}
```

```cpp
#include <hip/hip_runtime.h>
#include <hip/hip_bf16.h>
#include <type_traits>
#include <cstdio>

typedef unsigned u32;
typedef unsigned short u16;
typedef u32 v4u __attribute__((ext_vector_type(4)));
typedef u32 v2u __attribute__((ext_vector_type(2)));
typedef float f32x4 __attribute__((ext_vector_type(4)));
typedef float f32x2 __attribute__((ext_vector_type(2)));
typedef short s16x4 __attribute__((ext_vector_type(4)));
typedef __bf16 bf16x8 __attribute__((ext_vector_type(8)));
typedef __bf16 bf16x2 __attribute__((ext_vector_type(2)));
typedef __amdgpu_buffer_rsrc_t rsrc_t;
#define GAS __attribute__((address_space(1)))
#define LAS __attribute__((address_space(3)))
#define DI __device__ __forceinline__

constexpr u32 WS_EPOCH = 0;
constexpr u32 WS_FLAGS = 4096;
constexpr u32 WS_GE = 65536;
constexpr u32 WS_P1 = 1u << 20;
constexpr u32 WS_P2 = 3u << 20;
constexpr u32 WS_BYTES = 5u << 20;
constexpr u32 OOB = 0x80000000u;
constexpr u32 SENT = 0xFFFFFFFFu;
constexpr u32 SPIN_CAP = 100000;
constexpr u32 LSPIN_CAP = 4000000;
constexpr u32 L_RING = 0;
constexpr u32 L_X = 131072;
constexpr u32 L_F = L_X + 19008;
constexpr u32 L_J = L_F + 1152;
constexpr u32 L_JUNK = L_J + 8192;
constexpr u32 L_HP = L_JUNK + 1024;
constexpr u32 L_CP = L_HP + 16;
constexpr u32 LDS_BYTES = L_CP + 16;
static_assert(LDS_BYTES <= 163840, "lds");
#define CODE_LINES 112
#define LOOK 4
#define SOFF 0
#define CPRIO 1
#define DLY1 0
#define DLY2 0

template <int I, int N, class F> DI void static_for(F&& f) {
  if constexpr (I < N) { f(std::integral_constant<int, I>{}); static_for<I + 1, N>(f); }
}
template <int N> DI void wait_vm() { asm volatile("s_waitcnt vmcnt(%0)" ::"i"(N) : "memory"); }
DI void bar() { asm volatile("s_waitcnt lgkmcnt(0)\n\ts_barrier" ::: "memory"); }
DI void fence_sched() { __builtin_amdgcn_sched_barrier(0); }
DI void mem_order() { asm volatile("" ::: "memory"); }
DI u32 rfl(u32 x) { return __builtin_amdgcn_readfirstlane(x); }
#define WAUX ""
DI void dma16(u32 lds, u32 voff, rsrc_t rs, u32 soff) {
  asm volatile("s_mov_b32 m0, %0\n\ts_nop 0\n\tbuffer_load_dwordx4 %1, %2, %3 offen lds" WAUX ::"s"(lds), "v"(voff), "s"(rs), "s"(soff) : "memory");
}
DI void dma4(u32 lds, u32 voff, rsrc_t rs, u32 soff) {
  asm volatile("s_mov_b32 m0, %0\n\ts_nop 0\n\tbuffer_load_dword %1, %2, %3 offen lds" ::"s"(lds), "v"(voff), "s"(rs), "s"(soff) : "memory");
}
DI rsrc_t mk(const void* p, u32 bytes) { return __builtin_amdgcn_make_buffer_rsrc((void*)p, 0, (int)bytes, 0x00020000); }
DI v4u lds128(u32 a) { return *(const LAS v4u*)(size_t)a; }
DI v2u ldstr(u32 a) { return __builtin_bit_cast(v2u, __builtin_amdgcn_ds_read_tr16_b64_v4i16((LAS s16x4*)(size_t)a)); }
DI v4u ldstr2(u32 a0, u32 a1) { v2u x = ldstr(a0), y = ldstr(a1); return v4u{x.x, x.y, y.x, y.y}; }
DI f32x4 mfma(v4u a, v4u b, f32x4 c) {
  return __builtin_amdgcn_mfma_f32_16x16x32_bf16(__builtin_bit_cast(bf16x8, a), __builtin_bit_cast(bf16x8, b), c, 0, 0, 0);
}
DI u32 pk(float a, float b) { f32x2 f = {a, b}; return __builtin_bit_cast(u32, __builtin_convertvector(f, bf16x2)); }
DI float lo16(u32 x) { return __builtin_bit_cast(float, x << 16); }
DI float hi16(u32 x) { return __builtin_bit_cast(float, x & 0xFFFF0000u); }
DI float gelu(float x) {
  const float c0 = -2.0f * 0.7978845608028654f * 1.4426950408889634f, c1 = c0 * 0.044715f;
  float e = __builtin_amdgcn_exp2f(x * __builtin_fmaf(x * x, c1, c0));
  return x * __builtin_amdgcn_rcpf(1.0f + e);
}
DI float tanh_(float x) {
  float e = __builtin_amdgcn_exp2f(x * 2.8853900817779268f);
  return __builtin_fmaf(-2.0f, __builtin_amdgcn_rcpf(1.0f + e), 1.0f);
}
DI u32 nosent(u32 x) { return min(x, SENT - 1); }
DI v4u pack2(f32x4 a, f32x4 b) { return v4u{nosent(pk(a.x, a.y)), nosent(pk(a.z, a.w)), nosent(pk(b.x, b.y)), nosent(pk(b.z, b.w))}; }
DI void st_sc1(v4u v, rsrc_t rs, u32 off) { __builtin_amdgcn_raw_buffer_store_b128(v, rs, off, 0, 16); }
DI u32 max4(v4u v) { return max(max(v.x, v.y), max(v.z, v.w)); }
DI u32 min4(v4u v) { return min(min(v.x, v.y), min(v.z, v.w)); }
DI u32 prog_read(u32 va) { u32 v = *(const volatile LAS u32*)(size_t)va; mem_order(); return v; }
DI void prog_write(u32 a, u32 v) { mem_order(); *(volatile LAS u32*)(size_t)a = v; mem_order(); }
template <u32 NEED, int SLEEP> DI void prog_ensure(u32 hv, u32 va) {
  u32 cnt;
  asm volatile("v_cmp_gt_u32 vcc, %3, %0\n\ts_cbranch_vccz Ld%=\n\ts_mov_b32 %1, %5\n"
               "Lr%=:\n\ts_sleep %4\n\tds_read_b32 %0, %2\n\ts_waitcnt lgkmcnt(0)\n\tv_cmp_gt_u32 vcc, %3, %0\n\ts_cbranch_vccz Ld%=\n\t"
               "s_sub_u32 %1, %1, 1\n\ts_cbranch_scc0 Lr%=\n"
               "Ld%=:"
               : "+v"(hv), "=&s"(cnt) : "v"(va), "i"(NEED), "i"(SLEEP), "i"(LSPIN_CAP) : "memory", "scc", "vcc");
}
template <u32 NEED, int SLEEP> DI void prog_wait(u32 va) { prog_ensure<NEED, SLEEP>(prog_read(va), va); }
template <int N, u32 STEP, u32 SMASK, bool PUB> DI void rounds(u32& m, u32& so, u32& pv, u32 va, u32 vb, rsrc_t rs, u32 hp) {
  u32 cnt;
  if constexpr (PUB)
    asm volatile("s_mov_b32 %3, %9\n"
               "L%=:\n\ts_mov_b32 m0, %0\n\ts_add_u32 %0, %0, 0x1000\n\tbuffer_load_dwordx4 %4, %6, %1 offen lds" WAUX "\n\t"
               "s_mov_b32 m0, %0\n\ts_add_u32 %0, %0, 0x1000\n\tbuffer_load_dwordx4 %5, %6, %1 offen lds" WAUX "\n\t"
               "s_and_b32 %0, %0, 0x1ffff\n\ts_add_u32 %1, %1, %8\n\ts_and_b32 %1, %1, %10\n\tv_add_u32 %2, 1, %2\n\t"
               "s_waitcnt vmcnt(%11)\n\tds_write_b32 %7, %2\n\ts_sub_u32 %3, %3, 1\n\ts_cbranch_scc0 L%="
               : "+s"(m), "+s"(so), "+v"(pv), "=&s"(cnt)
               : "v"(va), "v"(vb), "s"(rs), "v"(hp), "i"(STEP), "i"(N - 1), "i"(SMASK), "i"(2 * LOOK) : "memory", "scc");
  else
    asm volatile("s_mov_b32 %2, %7\n"
               "L%=:\n\ts_mov_b32 m0, %0\n\ts_add_u32 %0, %0, 0x1000\n\tbuffer_load_dwordx4 %3, %5, %1 offen lds" WAUX "\n\t"
               "s_mov_b32 m0, %0\n\ts_add_u32 %0, %0, 0x1000\n\tbuffer_load_dwordx4 %4, %5, %1 offen lds" WAUX "\n\t"
               "s_and_b32 %0, %0, 0x1ffff\n\ts_add_u32 %1, %1, %6\n\ts_and_b32 %1, %1, %8\n\t"
               "s_sub_u32 %2, %2, 1\n\ts_cbranch_scc0 L%="
               : "+s"(m), "+s"(so), "=&s"(cnt)
               : "v"(va), "v"(vb), "s"(rs), "i"(STEP), "i"(N - 1), "i"(SMASK) : "memory", "scc");
}
DI void delay(int n) { for (int i = 0; i < n; ++i) __builtin_amdgcn_s_sleep(1); }

#define SSTAMP(i)
#define STAMP(i)

struct Args { const u16 *x, *w1, *w2, *gp, *wf, *wg, *wo; u16* out; unsigned char* ws; };

constexpr u32 C_S8 = 1, C_S12 = 2, C_P1 = 3, C_W2A = 4, C_W2B = 5, C_W2C = 6, C_A = 7, C_WFA = 8, C_WFB = 9, C_F = 10;

__global__ void __launch_bounds__(512, 2) rnnt_fused(Args a) {
  __shared__ __attribute__((aligned(16))) unsigned char smem[LDS_BYTES];
  const u32 LB = (u32)(size_t)(LAS unsigned char*)smem;
  const unsigned long long rtp = __builtin_amdgcn_s_memrealtime();
  u32 tid = threadIdx.x;
  {
    unsigned long long pc;
    asm volatile("s_mov_b32 m0, %2\n\ts_getpc_b64 %0\n\tglobal_load_lds_dword %1, %0" : "=&s"(pc) : "v"(min(tid, (u32)CODE_LINES - 1) << 7), "s"(LB + L_JUNK) : "memory");
    if (tid < 8) *(volatile LAS u32*)(size_t)(LB + L_HP + tid * 4) = 0;
  }
  const u32 wv = rfl(tid >> 6), w4 = wv & 3;
  const u32 bid = blockIdx.x, x8 = bid & 7, j = bid >> 3, mt = j >> 1, mloc = mt & 7, bat = mt >> 3;
  const u32 k1 = x8 >> 1, n1 = 2 * (x8 & 1) + (j & 1);
  const u32 c2 = x8, n2 = j & 1;
  const u32 idx16 = x8 * 2 + (j & 1), pi = mt * 16 + idx16, ploc = pi & 127;
  const bool p3ok = ploc < 125;

  u32 lane = tid & 63;
  const rsrc_t rw1 = mk(a.w1, 15 * 131072);
  u32 xv, w1v;
  {
    const u32 rr8 = lane >> 3;
    xv = (u32)((int)(mloc * 128 + w4 * 8 + rr8) - 7) * 512 + k1 * 128 + (lane & 7) * 16;
    w1v = (k1 * 64 + w4 * 8 + rr8) * 512 + n1 * 128 + ((((lane >> 1) & 3) ^ (((rr8 >> 1) & 1) + 2 * (w4 & 1))) * 32) + (lane & 1) * 16;
    constexpr u32 NT = 15 - LOOK;
    const u32 ln = (mt * 4 + w4) * NT + lane;
    const u32 tv = (LOOK + (ln >> 6)) * 131072 + (k1 * 64 + (ln & 63)) * 512 + n1 * 128;
    if (wv < 4) xv = lane < NT ? tv : OOB;
    asm volatile("" : "+v"(xv), "+v"(w1v));
  }
  bar();
  if (wv < 4) dma4(LB + L_JUNK + 256, xv, rw1, 0);

  if (__builtin_expect(wv >= 4, 1)) {
    const rsrc_t rx = mk(a.x + bat * 256000, 512000);
    static_for<0, 4>([&](auto I) { dma16(LB + L_X + (w4 + 4 * I) * 1056, xv + I * 16384, rx, 0); });
    dma16(LB + L_X + ((w4 & 1) + 16) * 1056, w4 < 2 ? xv + 4 * 16384 : xv + 3 * 16384 + 8192, rx, 0);
    u32 m = LB + L_RING + w4 * 1024, so = 0, pv = 0;
    const u32 hp = LB + L_HP + w4 * 4, cpv = LB + L_CP + (lane & 3) * 4;
    rounds<LOOK - 1, 131072, ~0u, false>(m, so, pv, w1v, w1v + 16384, rw1, hp);
    STAMP(1);
    rounds<16 - LOOK, 131072, ~0u, true>(m, so, pv, w1v, w1v + 16384, rw1, hp);
    STAMP(2);
    asm volatile("" : "+v"(lane));
    const u32 g = lane >> 4;
    const rsrc_t rw2 = mk(a.w2, 15 * 131072);
    const u32 w2v = (c2 * 32 + w4 * 4 + g) * 512 + n2 * 256 + ((((lane >> 1) & 7) ^ (g | (((w4 >> 1) & 1) << 2))) * 32) + (lane & 1) * 16;
    so = 0;
    rounds<1, 131072, ~0u, true>(m, so, pv, w2v, w2v + 8192, rw2, hp);
    prog_wait<C_S8, 1>(cpv);
    rounds<7, 131072, ~0u, true>(m, so, pv, w2v, w2v + 8192, rw2, hp);
    prog_wait<C_S12, 1>(cpv);
    rounds<4, 131072, ~0u, true>(m, so, pv, w2v, w2v + 8192, rw2, hp);
    prog_wait<C_P1, 1>(cpv);
    rounds<3, 131072, ~0u, true>(m, so, pv, w2v, w2v + 8192, rw2, hp);
    STAMP(3);
    const rsrc_t rws = mk(a.ws, WS_BYTES);
    const u32 l15 = lane & 15;
    if (!p3ok) {
      wait_vm<0>();
      prog_write(hp, 31);
      if (ploc >= 126) {
        const u32 et = (bat * 2 + (pi & 1)) * 4 + w4;
        const rsrc_t rwg = mk(a.wg, 131072);
        u32 e[64]; v4u b[8];
        const u32 eo = 8 * g * 512 + (et * 16 + l15) * 2;
        static_for<0, 64>([&](auto J) { e[J] = __builtin_amdgcn_raw_buffer_load_b16(rwg, eo + (J & 7) * 512, (J >> 3) * 16384, 0); });
        static_for<0, 8>([&](auto K) { b[K] = *(const GAS v4u*)(a.gp + l15 * 256 + K * 32 + 8 * g); });
        fence_sched();
        f32x4 d = {0, 0, 0, 0};
        static_for<0, 8>([&](auto K) {
          d = mfma(v4u{e[8 * K] | (e[8 * K + 1] << 16), e[8 * K + 2] | (e[8 * K + 3] << 16), e[8 * K + 4] | (e[8 * K + 5] << 16), e[8 * K + 6] | (e[8 * K + 7] << 16)}, b[K], d);
        });
        v4u o = __builtin_bit_cast(v4u, d);
        st_sc1(v4u{nosent(o.x), nosent(o.y), nosent(o.z), nosent(o.w)}, rws, WS_GE + l15 * 1024 + et * 64 + g * 16);
      } else if (pi == 125 && w4 == 0) {
        u32 epoch;
        asm volatile("s_load_dword %0, %1, 0x0\n\ts_waitcnt lgkmcnt(0)" : "=s"(epoch) : "s"(a.ws) : "memory");
        epoch += 1;
        for (u32 spins = 0; spins < SPIN_CAP; ++spins) {
          v4u f4;
          asm volatile("buffer_load_dwordx4 %0, %1, %2, 0 offen sc1\n\ts_waitcnt vmcnt(0)" : "=v"(f4) : "v"(WS_FLAGS + (lane >> 2) * 128 + (lane & 3) * 16), "s"(rws) : "memory");
          if (__all(f4.x == epoch && f4.y == epoch && f4.z == epoch && f4.w == epoch)) break;
        }
        if (lane == 0) __hip_atomic_store((GAS u32*)(a.ws + WS_EPOCH), epoch, __ATOMIC_RELAXED, __HIP_MEMORY_SCOPE_AGENT);
      }
      return;
    }
    const u32 r2 = 2 * w4 + (lane >> 5);
    const u32 wfv = r2 * 512 + ((((lane >> 1) & 15) ^ r2) * 32) + (lane & 1) * 16;
    const rsrc_t rwf = mk(a.wf, 131072), rwo = mk(a.wo, 131072);
    so = 14 * 8192;
    rounds<1, 8192, 0x1ffff, true>(m, so, pv, wfv, wfv + 4096, rwf, hp);
    prog_wait<C_W2A, 1>(cpv);
    rounds<5, 8192, 0x1ffff, true>(m, so, pv, wfv, wfv + 4096, rwf, hp);
    prog_wait<C_W2B, 1>(cpv);
    rounds<5, 8192, 0x1ffff, true>(m, so, pv, wfv, wfv + 4096, rwf, hp);
    prog_wait<C_W2C, 1>(cpv);
    rounds<5, 8192, 0x1ffff, true>(m, so, pv, wfv, wfv + 4096, rwf, hp);
    wait_vm<0>();
    prog_write(hp, 47);
    STAMP(4);
    m = LB + L_RING + w4 * 1024; so = 0;
    prog_wait<C_WFA, 1>(cpv);
    rounds<8, 8192, 0x1ffff, false>(m, so, pv, wfv, wfv + 4096, rwo, hp);
    prog_wait<C_WFB, 1>(cpv);
    rounds<8, 8192, 0x1ffff, false>(m, so, pv, wfv, wfv + 4096, rwo, hp);
    STAMP(5);
    wait_vm<0>();
    STAMP(6);
  } else {
    __builtin_amdgcn_s_setprio(CPRIO);
    const rsrc_t rws = mk(a.ws, WS_BYTES);
    const u32 p1off = WS_P1 + k1 * 524288 + (2 * n1 + (wv >> 1)) * 65536 + mt * 4096 + (wv & 1) * 2048;
    const u32 p2off = WS_P2 + c2 * 262144 + mt * 16384 + n2 * 8192 + wv * 2048;
    const v4u sv = {SENT, SENT, SENT, SENT};
    u32 epoch;
    st_sc1(sv, rws, p1off + lane * 16);
    st_sc1(sv, rws, p1off + 1024 + lane * 16);
    asm volatile("s_load_dword %0, %1, 0x0" : "=s"(epoch) : "s"(a.ws) : "memory");
    asm volatile("" : "+v"(lane));
    const u32 l15 = lane & 15, g = lane >> 4, q4 = l15 >> 2, p4 = lane & 3;
    const u32 cp = LB + L_CP + wv * 4, cpv = LB + L_CP + p4 * 4, hpv = LB + L_HP + p4 * 4;
    f32x4 acc[4] = {{0, 0, 0, 0}, {0, 0, 0, 0}, {0, 0, 0, 0}, {0, 0, 0, 0}};
    u32 fl;
    const u32 fline = lane < 48 ? min(max(mt + g, 1u) - 1, 15u) : 7 + (lane & 8);
    const u32 foff = WS_FLAGS + fline * 128 + (lane < 48 ? l15 : 8 + (lane & 7)) * 4;
    u32 t2v, t3v, gsv, flv;
    {
      const u32 ln = (mt * 4 + wv) * 15 + lane;
      t2v = lane < 15 ? (ln >> 6) * 131072 + (c2 * 32 + ((ln >> 1) & 31)) * 512 + n2 * 256 + (ln & 1) * 128 : OOB;
      t3v = lane < 16 ? ((mt * 4 + wv) * 16 + lane) * 128 : OOB;
      gsv = ploc >= 126 ? WS_GE + (wv * 4 + g) * 1024 + (bat * 2 + (pi & 1)) * 256 + l15 * 16 : OOB;
      flv = tid == 0 ? WS_FLAGS + mt * 128 + idx16 * 4 : OOB;
    }
    const rsrc_t rw2 = mk(a.w2, 15 * 131072), rw3 = mk((j & 1) ? a.wo : a.wf, 131072);
    asm volatile("s_waitcnt lgkmcnt(0)" : "+s"(epoch), "+v"(t2v), "+v"(t3v), "+v"(gsv), "+v"(flv)::"memory");
    epoch += 1;
    STAMP(1);
    {
      v4u F[2][21];
      v4u Wf[2][2];
      const u32 xr = LB + L_X + l15 * 1056 + g * 16;
      const u32 w1r = LB + L_RING + g * 1024 + q4 * 128 + ((wv ^ ((q4 >> 1) + 2 * (g & 1))) * 32) + p4 * 8;
      prog_wait<1, 0>(hpv);
      STAMP(2);
      static_for<0, 8>([&](auto Q) { F[0][Q] = lds128(xr + Q * 128); F[1][Q] = lds128(xr + Q * 128 + 64); });
      prog_wait<2, 0>(hpv);
      static_for<0, 15>([&](auto I) {
        constexpr int i = I;
        Wf[i & 1][0] = ldstr2(w1r + i * 8192, w1r + i * 8192 + 512);
        Wf[i & 1][1] = ldstr2(w1r + i * 8192 + 4096, w1r + i * 8192 + 4096 + 512);
        if constexpr (i >= 2) {
          F[0][i + 6] = lds128(xr + ((i + 6) >> 3) * 1056 + ((i + 6) & 7) * 128);
          F[1][i + 6] = lds128(xr + ((i + 6) >> 3) * 1056 + ((i + 6) & 7) * 128 + 64);
        }
        fence_sched();
        if constexpr (i >= 1) static_for<0, 4>([&](auto J) { acc[J] = mfma(Wf[(i - 1) & 1][0], F[0][2 * J + i - 1], acc[J]); });
        fence_sched();
        u32 hv = 0, cv = 0;
        if constexpr (i < 14) hv = prog_read(hpv);
        if constexpr (i == 10) cv = prog_read(cpv);
        fence_sched();
        if constexpr (i >= 1) static_for<0, 4>([&](auto J) { acc[J] = mfma(Wf[(i - 1) & 1][1], F[1][2 * J + i - 1], acc[J]); });
        fence_sched();
        if constexpr (i == 2) {
          st_sc1(sv, rws, p2off + lane * 16);
          st_sc1(sv, rws, p2off + 1024 + lane * 16);
          st_sc1(sv, rws, gsv);
        }
        if constexpr (i == 4) dma4(LB + L_JUNK, t2v, rw2, 0);
        if constexpr (i == 9) dma4(LB + L_JUNK, t3v, rw3, 0);
        if constexpr (i == 8) { wait_vm<0>(); prog_write(cp, C_S8); }
        if constexpr (i == 10) {
          prog_ensure<C_S8, 0>(cv, cpv);
          __builtin_amdgcn_raw_buffer_store_b32(epoch, rws, flv, 0, 16);
        }
        if constexpr (i == 12) prog_write(cp, C_S12);
        if constexpr (i < 14) prog_ensure<i + 3, 0>(hv, hpv);
        fence_sched();
        SSTAMP(i + 1);
      });
      static_for<0, 2>([&](auto C) { static_for<0, 4>([&](auto J) { acc[J] = mfma(Wf[0][C], F[C][2 * J + 14], acc[J]); }); });
    }
    STAMP(3);
    st_sc1(pack2(acc[0], acc[1]), rws, p1off + l15 * 64 + g * 16);
    st_sc1(pack2(acc[2], acc[3]), rws, p1off + 1024 + l15 * 64 + g * 16);
    prog_write(cp, C_P1);
    asm volatile("buffer_load_dword %0, %1, %2, 0 offen sc1" : "=v"(fl) : "v"(foff), "s"(rws) : "memory");
    STAMP(4);
    v4u W2[2][15];
    const u32 w2r = LB + L_RING + (8 * g + q4) * 256 + (((2 * wv) ^ (q4 | ((g & 1) << 2))) * 32) + p4 * 8;
    auto rdw2 = [&](auto T) {
      constexpr u32 s = (T + 15) & 15;
      W2[0][T] = ldstr2(w2r + s * 8192, w2r + s * 8192 + 1024);
      W2[1][T] = ldstr2((w2r ^ 32) + s * 8192, (w2r ^ 32) + s * 8192 + 1024);
    };
    auto lds_done = [&]() { asm volatile("s_waitcnt lgkmcnt(0)" ::: "memory"); };
    prog_wait<21, 0>(hpv);
    static_for<0, 5>(rdw2);
    lds_done(); prog_write(cp, C_W2A);
    v4u hp[4]; u32 hh[4];
    u32 rho, rhoh, h1off, h1offh;
    {
      const u32 mm = lane >> 2;
      rho = 8 + 4 * mm + 2 * (wv & 1);
      h1off = mloc * 64 + rho - 8 < 500 ? WS_P1 + c2 * 65536 + mt * 4096 + wv * 1024 + lane * 16 : OOB;
      rho = LB + L_X + rho * 64 + (rho >> 2) * 32 + (wv >> 1) * 32 + p4 * 8;
      const u32 nx = lane >> 5, mh = (lane >> 4) & 1, dw = lane & 3;
      rhoh = 72 * nx + 4 * mh + 2 * (wv & 1) + (dw >> 1);
      const int hrow = (int)(mloc * 64 + rhoh) - 8;
      h1offh = (hrow >= 0 && hrow < 500) ? WS_P1 + c2 * 65536 + (mt + 2 * nx - 1) * 4096 + wv * 1024 + (nx ? 0 : 896) + (lane & 31) * 4 : OOB;
      rhoh = LB + L_X + rhoh * 64 + (rhoh >> 2) * 32 + (wv >> 1) * 32 + q4 * 8 + (dw & 1) * 4;
    }
    asm volatile("s_waitcnt vmcnt(0)" : "+v"(fl)::"memory");
    for (u32 spins = 0; !__all(fl == epoch) && spins < SPIN_CAP; ++spins)
      asm volatile("buffer_load_dword %0, %1, %2, 0 offen sc1\n\ts_waitcnt vmcnt(0)" : "=v"(fl) : "v"(foff), "s"(rws) : "memory");
    STAMP(5);
    delay(DLY1);
    auto h1_load = [&]() {
      static_for<0, 4>([&](auto K) { hp[K] = __builtin_amdgcn_raw_buffer_load_b128(rws, h1off, K * 524288, 16); });
      static_for<0, 4>([&](auto K) { hh[K] = __builtin_amdgcn_raw_buffer_load_b32(rws, h1offh, K * 524288, 16); });
      mem_order();
    };
    h1_load();
    prog_wait<26, 0>(hpv);
    static_for<5, 10>(rdw2);
    lds_done(); prog_write(cp, C_W2B);
    prog_wait<31, 0>(hpv);
    static_for<10, 15>(rdw2);
    lds_done(); prog_write(cp, C_W2C);
    STAMP(6);
    for (u32 spins = 0;; ++spins) {
      const u32 mx = max(max(max(max4(hp[0]), max4(hp[1])), max(max4(hp[2]), max4(hp[3]))), max(max(hh[0], hh[1]), max(hh[2], hh[3])));
      if (__all(mx != SENT) || spins >= SPIN_CAP) break;
      h1_load();
    }
    STAMP(7);
    {
      float v[8];
      static_for<0, 4>([&](auto D) {
        v[2 * D] = gelu((lo16(hp[0][+D]) + lo16(hp[1][+D])) + (lo16(hp[2][+D]) + lo16(hp[3][+D])));
        v[2 * D + 1] = gelu((hi16(hp[0][+D]) + hi16(hp[1][+D])) + (hi16(hp[2][+D]) + hi16(hp[3][+D])));
      });
      const float u0 = gelu((lo16(hh[0]) + lo16(hh[1])) + (lo16(hh[2]) + lo16(hh[3])));
      const float u1 = gelu((hi16(hh[0]) + hi16(hh[1])) + (hi16(hh[2]) + hi16(hh[3])));
      *(LAS v2u*)(size_t)(rho) = v2u{pk(v[0], v[1]), pk(v[2], v[3])};
      *(LAS v2u*)(size_t)(rho + 64) = v2u{pk(v[4], v[5]), pk(v[6], v[7])};
      *(LAS u32*)(size_t)(rhoh) = pk(u0, u1);
    }
    prog_write(cp, C_A);
    STAMP(8);
    prog_wait<C_A, 0>(cpv);
    STAMP(9);
    {
      static_for<0, 4>([&](auto J) { acc[J] = f32x4{0, 0, 0, 0}; });
      const u32 ar = LB + L_X + l15 * 288 + g * 16;
      v4u A[17];
      static_for<0, 17>([&](auto Q) { A[Q] = lds128(ar + ((Q + 1) >> 2) * 288 + ((Q + 1) & 3) * 64); });
      static_for<0, 15>([&](auto T) {
        acc[0] = mfma(W2[0][T], A[T], acc[0]);
        acc[2] = mfma(W2[1][T], A[T], acc[2]);
        acc[1] = mfma(W2[0][T], A[T + 2], acc[1]);
        acc[3] = mfma(W2[1][T], A[T + 2], acc[3]);
      });
      st_sc1(pack2(acc[0], acc[1]), rws, p2off + l15 * 64 + g * 16);
      st_sc1(pack2(acc[2], acc[3]), rws, p2off + 1024 + l15 * 64 + g * 16);
    }
    STAMP(10);
    if (!p3ok) return;
    v4u WF[8][4];
    const u32 wr = LB + L_RING + (4 * g + q4) * 512 + (((4 * wv) ^ ((4 * g + q4) & 7)) * 32) + p4 * 8;
    auto rdwf = [&](auto K) { static_for<0, 4>([&](auto T) { WF[K][T] = ldstr2((wr ^ (T * 32)) + K * 16384, (wr ^ (T * 32)) + K * 16384 + 8192); }); };
    prog_wait<41, 0>(hpv);
    static_for<0, 4>(rdwf);
    lds_done(); prog_write(cp, C_WFA);
    prog_wait<47, 0>(hpv);
    static_for<4, 8>(rdwf);
    lds_done(); prog_write(cp, C_WFB);
    STAMP(11);
    delay(DLY2);
    u32 fp[8];
    v4u ge[4];
    {
      const u32 h2off = WS_P2 + mt * 16384 + (tid >> 4) * 1024 + idx16 * 64 + l15 * 4;
      const u32 geoff = WS_GE + (bat * 8 + (lane & 7)) * 1024 + wv * 256 + g * 16;
      for (u32 spins = 0;; ++spins) {
        mem_order();
        static_for<0, 8>([&](auto K) { fp[K] = __builtin_amdgcn_raw_buffer_load_b32(rws, h2off, K * 262144, 16); });
        static_for<0, 4>([&](auto T) { ge[T] = __builtin_amdgcn_raw_buffer_load_b128(rws, geoff + T * 64, 0, 16); });
        u32 mx = max(max(max4(ge[0]), max4(ge[1])), max(max4(ge[2]), max4(ge[3])));
        static_for<0, 8>([&](auto K) { mx = max(mx, fp[K]); });
        if (__all(mx != SENT) || spins >= SPIN_CAP) break;
      }
      STAMP(12);
      float s0 = 0.f, s1 = 0.f;
      static_for<0, 8>([&](auto K) { s0 += lo16(fp[K]); s1 += hi16(fp[K]); });
      const u32 pos = (tid >> 5) * 32 + q4 * 8 + ((tid >> 4) & 1) * 4 + (tid & 1) * 2;
      *(LAS u32*)(size_t)(LB + L_F + ((tid >> 1) & 1) * 576 + pos * 2) = pk(gelu(s0), gelu(s1));
    }
    prog_write(cp, C_F);
    prog_wait<C_F, 0>(cpv);
    {
      f32x4 d[4];
      static_for<0, 4>([&](auto T) { d[T] = __builtin_bit_cast(f32x4, ge[T]); });
      const u32 fr = LB + L_F + (l15 >> 3) * 576 + g * 16;
      v4u b[8];
      static_for<0, 8>([&](auto K) { b[K] = lds128(fr + K * 64); });
      static_for<0, 2>([&](auto H) {
        static_for<0, 8>([&](auto K) { d[2 * H] = mfma(WF[K][2 * H], b[K], d[2 * H]); d[2 * H + 1] = mfma(WF[K][2 * H + 1], b[K], d[2 * H + 1]); });
      });
      static_for<0, 2>([&](auto H) {
        const f32x4 d0 = d[2 * H], d1 = d[2 * H + 1];
        v4u jf = {pk(tanh_(d0.x), tanh_(d0.y)), pk(tanh_(d0.z), tanh_(d0.w)), pk(tanh_(d1.x), tanh_(d1.y)), pk(tanh_(d1.z), tanh_(d1.w))};
        *(LAS v4u*)(size_t)(LB + L_J + (2 * wv + H) * 1024 + lane * 16) = jf;
      });
    }
    STAMP(13);
  }
  bar();
  {
    asm volatile("" : "+v"(lane));
    const u32 l15 = lane & 15, g = lane >> 4, q4 = l15 >> 2, p4 = lane & 3;
    const u32 wr0 = LB + L_RING + (4 * g + q4) * 512 + (((2 * wv) ^ ((4 * g + q4) & 7)) * 32) + p4 * 8;
    const u32 wr1 = wr0 ^ 32;
    f32x4 d0 = {0, 0, 0, 0}, d1 = {0, 0, 0, 0};
    static_for<0, 8>([&](auto K) {
      const v4u b = lds128(LB + L_J + K * 1024 + lane * 16);
      d0 = mfma(ldstr2(wr0 + K * 16384, wr0 + K * 16384 + 8192), b, d0);
      d1 = mfma(ldstr2(wr1 + K * 16384, wr1 + K * 16384 + 8192), b, d1);
    });
    u16* op = a.out + ((bat * 250 + 2 * ploc) * 8 + l15) * 256 + wv * 32 + 4 * g;
    *(GAS v2u*)(op) = v2u{pk(d0.x, d0.y), pk(d0.z, d0.w)};
    *(GAS v2u*)(op + 16) = v2u{pk(d1.x, d1.y), pk(d1.z, d1.w)};
    while (__builtin_amdgcn_s_memrealtime() < rtp + 3000) __builtin_amdgcn_s_sleep(1);
  }
}

extern "C" void kernel_launch(void* const* d_in, const int* in_sizes, int n_in, void* d_out, int out_size, void* d_ws, size_t ws_size,
                              hipStream_t stream) {
  Args a{(const u16*)d_in[0], (const u16*)d_in[1], (const u16*)d_in[2], (const u16*)d_in[3], (const u16*)d_in[4],
         (const u16*)d_in[5], (const u16*)d_in[6], (u16*)d_out, (unsigned char*)d_ws};
  for (int i = 0; i < 7; ++i) { hipPointerAttribute_t at; hipError_t e = hipPointerGetAttributes(&at, d_in[i]); fprintf(stderr, "in%d %p type %d flags %u err %d\n", i, d_in[i], (int)at.type, at.allocationFlags, (int)e); }
  { hipPointerAttribute_t at; hipError_t e = hipPointerGetAttributes(&at, d_ws); fprintf(stderr, "ws %p type %d flags %u err %d out %p\n", d_ws, (int)at.type, at.allocationFlags, (int)e, d_out); }
  rnnt_fused<<<256, 512, 0, stream>>>(a);
}
```

```cpp
#include <hip/hip_runtime.h>
#include <hip/hip_bf16.h>
#include <type_traits>

typedef unsigned u32;
typedef unsigned short u16;
typedef u32 v4u __attribute__((ext_vector_type(4)));
typedef u32 v2u __attribute__((ext_vector_type(2)));
typedef float f32x4 __attribute__((ext_vector_type(4)));
typedef float f32x2 __attribute__((ext_vector_type(2)));
typedef short s16x4 __attribute__((ext_vector_type(4)));
typedef __bf16 bf16x8 __attribute__((ext_vector_type(8)));
typedef __bf16 bf16x2 __attribute__((ext_vector_type(2)));
typedef __amdgpu_buffer_rsrc_t rsrc_t;
#define GAS __attribute__((address_space(1)))
#define LAS __attribute__((address_space(3)))
#define DI __device__ __forceinline__

constexpr u32 WS_EPOCH = 0;
constexpr u32 WS_FLAGS = 4096;
constexpr u32 WS_GE = 65536;
constexpr u32 WS_P1 = 1u << 20;
constexpr u32 WS_P2 = 3u << 20;
constexpr u32 WS_BYTES = 5u << 20;
constexpr u32 OOB = 0x80000000u;
constexpr u32 SENT = 0xFFFFFFFFu;
constexpr u32 SPIN_CAP = 100000;
constexpr u32 LSPIN_CAP = 4000000;
constexpr u32 L_RING = 0;
constexpr u32 L_X = 131072;
constexpr u32 L_F = L_X + 19008;
constexpr u32 L_J = L_F + 1152;
constexpr u32 L_JUNK = L_J + 8192;
constexpr u32 L_HP = L_JUNK + 1024;
constexpr u32 L_CP = L_HP + 16;
constexpr u32 LDS_BYTES = L_CP + 16;
static_assert(LDS_BYTES <= 163840, "lds");
#define CODE_LINES 112
#define LOOK 4
#define SOFF 0
#define CPRIO 1
#define DLY1 0
#define DLY2 0

template <int I, int N, class F> DI void static_for(F&& f) {
  if constexpr (I < N) { f(std::integral_constant<int, I>{}); static_for<I + 1, N>(f); }
}
template <int N> DI void wait_vm() { asm volatile("s_waitcnt vmcnt(%0)" ::"i"(N) : "memory"); }
DI void bar() { asm volatile("s_waitcnt lgkmcnt(0)\n\ts_barrier" ::: "memory"); }
DI void fence_sched() { __builtin_amdgcn_sched_barrier(0); }
DI void mem_order() { asm volatile("" ::: "memory"); }
DI u32 rfl(u32 x) { return __builtin_amdgcn_readfirstlane(x); }
#define WAUX ""
DI void dma16(u32 lds, u32 voff, rsrc_t rs, u32 soff) {
  asm volatile("s_mov_b32 m0, %0\n\ts_nop 0\n\tbuffer_load_dwordx4 %1, %2, %3 offen lds" WAUX ::"s"(lds), "v"(voff), "s"(rs), "s"(soff) : "memory");
}
DI void dma4(u32 lds, u32 voff, rsrc_t rs, u32 soff) {
  asm volatile("s_mov_b32 m0, %0\n\ts_nop 0\n\tbuffer_load_dword %1, %2, %3 offen lds" ::"s"(lds), "v"(voff), "s"(rs), "s"(soff) : "memory");
}
DI rsrc_t mk(const void* p, u32 bytes) { return __builtin_amdgcn_make_buffer_rsrc((void*)p, 0, (int)bytes, 0x00020000); }
DI v4u lds128(u32 a) { return *(const LAS v4u*)(size_t)a; }
DI v2u ldstr(u32 a) { return __builtin_bit_cast(v2u, __builtin_amdgcn_ds_read_tr16_b64_v4i16((LAS s16x4*)(size_t)a)); }
DI v4u ldstr2(u32 a0, u32 a1) { v2u x = ldstr(a0), y = ldstr(a1); return v4u{x.x, x.y, y.x, y.y}; }
DI f32x4 mfma(v4u a, v4u b, f32x4 c) {
  return __builtin_amdgcn_mfma_f32_16x16x32_bf16(__builtin_bit_cast(bf16x8, a), __builtin_bit_cast(bf16x8, b), c, 0, 0, 0);
}
DI u32 pk(float a, float b) { f32x2 f = {a, b}; return __builtin_bit_cast(u32, __builtin_convertvector(f, bf16x2)); }
DI float lo16(u32 x) { return __builtin_bit_cast(float, x << 16); }
DI float hi16(u32 x) { return __builtin_bit_cast(float, x & 0xFFFF0000u); }
DI float gelu(float x) {
  const float c0 = -2.0f * 0.7978845608028654f * 1.4426950408889634f, c1 = c0 * 0.044715f;
  float e = __builtin_amdgcn_exp2f(x * __builtin_fmaf(x * x, c1, c0));
  return x * __builtin_amdgcn_rcpf(1.0f + e);
}
DI float tanh_(float x) {
  float e = __builtin_amdgcn_exp2f(x * 2.8853900817779268f);
  return __builtin_fmaf(-2.0f, __builtin_amdgcn_rcpf(1.0f + e), 1.0f);
}
DI u32 nosent(u32 x) { return min(x, SENT - 1); }
DI v4u pack2(f32x4 a, f32x4 b) { return v4u{nosent(pk(a.x, a.y)), nosent(pk(a.z, a.w)), nosent(pk(b.x, b.y)), nosent(pk(b.z, b.w))}; }
DI void st_sc1(v4u v, rsrc_t rs, u32 off) { __builtin_amdgcn_raw_buffer_store_b128(v, rs, off, 0, 16); }
DI u32 max4(v4u v) { return max(max(v.x, v.y), max(v.z, v.w)); }
DI u32 min4(v4u v) { return min(min(v.x, v.y), min(v.z, v.w)); }
DI u32 prog_read(u32 va) { u32 v = *(const volatile LAS u32*)(size_t)va; mem_order(); return v; }
DI void prog_write(u32 a, u32 v) { mem_order(); *(volatile LAS u32*)(size_t)a = v; mem_order(); }
template <u32 NEED, int SLEEP> DI void prog_ensure(u32 hv, u32 va) {
  u32 cnt;
  asm volatile("v_cmp_gt_u32 vcc, %3, %0\n\ts_cbranch_vccz Ld%=\n\ts_mov_b32 %1, %5\n"
               "Lr%=:\n\ts_sleep %4\n\tds_read_b32 %0, %2\n\ts_waitcnt lgkmcnt(0)\n\tv_cmp_gt_u32 vcc, %3, %0\n\ts_cbranch_vccz Ld%=\n\t"
               "s_sub_u32 %1, %1, 1\n\ts_cbranch_scc0 Lr%=\n"
               "Ld%=:"
               : "+v"(hv), "=&s"(cnt) : "v"(va), "i"(NEED), "i"(SLEEP), "i"(LSPIN_CAP) : "memory", "scc", "vcc");
}
template <u32 NEED, int SLEEP> DI void prog_wait(u32 va) { prog_ensure<NEED, SLEEP>(prog_read(va), va); }
template <int N, u32 STEP, u32 SMASK, bool PUB> DI void rounds(u32& m, u32& so, u32& pv, u32 va, u32 vb, rsrc_t rs, u32 hp) {
  u32 cnt;
  if constexpr (PUB)
    asm volatile("s_mov_b32 %3, %9\n"
               "L%=:\n\ts_mov_b32 m0, %0\n\ts_add_u32 %0, %0, 0x1000\n\tbuffer_load_dwordx4 %4, %6, %1 offen lds" WAUX "\n\t"
               "s_mov_b32 m0, %0\n\ts_add_u32 %0, %0, 0x1000\n\tbuffer_load_dwordx4 %5, %6, %1 offen lds" WAUX "\n\t"
               "s_and_b32 %0, %0, 0x1ffff\n\ts_add_u32 %1, %1, %8\n\ts_and_b32 %1, %1, %10\n\tv_add_u32 %2, 1, %2\n\t"
               "s_waitcnt vmcnt(%11)\n\tds_write_b32 %7, %2\n\ts_sub_u32 %3, %3, 1\n\ts_cbranch_scc0 L%="
               : "+s"(m), "+s"(so), "+v"(pv), "=&s"(cnt)
               : "v"(va), "v"(vb), "s"(rs), "v"(hp), "i"(STEP), "i"(N - 1), "i"(SMASK), "i"(2 * LOOK) : "memory", "scc");
  else
    asm volatile("s_mov_b32 %2, %7\n"
               "L%=:\n\ts_mov_b32 m0, %0\n\ts_add_u32 %0, %0, 0x1000\n\tbuffer_load_dwordx4 %3, %5, %1 offen lds" WAUX "\n\t"
               "s_mov_b32 m0, %0\n\ts_add_u32 %0, %0, 0x1000\n\tbuffer_load_dwordx4 %4, %5, %1 offen lds" WAUX "\n\t"
               "s_and_b32 %0, %0, 0x1ffff\n\ts_add_u32 %1, %1, %6\n\ts_and_b32 %1, %1, %8\n\t"
               "s_sub_u32 %2, %2, 1\n\ts_cbranch_scc0 L%="
               : "+s"(m), "+s"(so), "=&s"(cnt)
               : "v"(va), "v"(vb), "s"(rs), "i"(STEP), "i"(N - 1), "i"(SMASK) : "memory", "scc");
}
DI void delay(int n) { for (int i = 0; i < n; ++i) __builtin_amdgcn_s_sleep(1); }

#define SSTAMP(i)
#define STAMP(i)

struct Args { const u16 *x, *w1, *w2, *gp, *wf, *wg, *wo; u16* out; unsigned char* ws; };

constexpr u32 C_S8 = 1, C_S12 = 2, C_P1 = 3, C_W2A = 4, C_W2B = 5, C_W2C = 6, C_A = 7, C_WFA = 8, C_WFB = 9, C_F = 10;

__global__ void __launch_bounds__(512, 2) rnnt_fused(Args a) {
  __shared__ __attribute__((aligned(16))) unsigned char smem[LDS_BYTES];
  const u32 LB = (u32)(size_t)(LAS unsigned char*)smem;
  const unsigned long long rtp = __builtin_amdgcn_s_memrealtime();
  u32 tid = threadIdx.x;
  {
    unsigned long long pc;
    asm volatile("s_mov_b32 m0, %2\n\ts_getpc_b64 %0\n\tglobal_load_lds_dword %1, %0" : "=&s"(pc) : "v"(min(tid, (u32)CODE_LINES - 1) << 7), "s"(LB + L_JUNK) : "memory");
    if (tid < 8) *(volatile LAS u32*)(size_t)(LB + L_HP + tid * 4) = 0;
  }
  const u32 wv = rfl(tid >> 6), w4 = wv & 3;
  const u32 bid = blockIdx.x, x8 = bid & 7, j = bid >> 3, mt = j >> 1, mloc = mt & 7, bat = mt >> 3;
  const u32 k1 = x8 >> 1, n1 = 2 * (x8 & 1) + (j & 1);
  const u32 c2 = x8, n2 = j & 1;
  const u32 idx16 = x8 * 2 + (j & 1), pi = mt * 16 + idx16, ploc = pi & 127;
  const bool p3ok = ploc < 125;

  u32 lane = tid & 63;
  const rsrc_t rw1 = mk(a.w1, 15 * 131072);
  u32 xv, w1v;
  {
    const u32 rr8 = lane >> 3;
    xv = (u32)((int)(mloc * 128 + w4 * 8 + rr8) - 7) * 512 + k1 * 128 + (lane & 7) * 16;
    w1v = (k1 * 64 + w4 * 8 + rr8) * 512 + n1 * 128 + ((((lane >> 1) & 3) ^ (((rr8 >> 1) & 1) + 2 * (w4 & 1))) * 32) + (lane & 1) * 16;
    constexpr u32 NT = 15 - LOOK;
    const u32 ln = (mt * 4 + w4) * NT + lane;
    const u32 tv = (LOOK + (ln >> 6)) * 131072 + (k1 * 64 + (ln & 63)) * 512 + n1 * 128;
    if (wv < 4) xv = lane < NT ? tv : OOB;
    asm volatile("" : "+v"(xv), "+v"(w1v));
  }
  bar();
  if (wv < 4) dma4(LB + L_JUNK + 256, xv, rw1, 0);

  if (__builtin_expect(wv >= 4, 1)) {
    const rsrc_t rx = mk(a.x + bat * 256000, 512000);
    static_for<0, 4>([&](auto I) { dma16(LB + L_X + (w4 + 4 * I) * 1056, xv + I * 16384, rx, 0); });
    dma16(LB + L_X + ((w4 & 1) + 16) * 1056, w4 < 2 ? xv + 4 * 16384 : xv + 3 * 16384 + 8192, rx, 0);
    u32 m = LB + L_RING + w4 * 1024, so = 0, pv = 0;
    const u32 hp = LB + L_HP + w4 * 4, cpv = LB + L_CP + (lane & 3) * 4;
    rounds<LOOK - 1, 131072, ~0u, false>(m, so, pv, w1v, w1v + 16384, rw1, hp);
    STAMP(1);
    rounds<16 - LOOK, 131072, ~0u, true>(m, so, pv, w1v, w1v + 16384, rw1, hp);
    STAMP(2);
    asm volatile("" : "+v"(lane));
    const u32 g = lane >> 4;
    const rsrc_t rw2 = mk(a.w2, 15 * 131072);
    const u32 w2v = (c2 * 32 + w4 * 4 + g) * 512 + n2 * 256 + ((((lane >> 1) & 7) ^ (g | (((w4 >> 1) & 1) << 2))) * 32) + (lane & 1) * 16;
    so = 0;
    rounds<1, 131072, ~0u, true>(m, so, pv, w2v, w2v + 8192, rw2, hp);
    prog_wait<C_S8, 1>(cpv);
    rounds<7, 131072, ~0u, true>(m, so, pv, w2v, w2v + 8192, rw2, hp);
    prog_wait<C_S12, 1>(cpv);
    rounds<4, 131072, ~0u, true>(m, so, pv, w2v, w2v + 8192, rw2, hp);
    prog_wait<C_P1, 1>(cpv);
    rounds<3, 131072, ~0u, true>(m, so, pv, w2v, w2v + 8192, rw2, hp);
    STAMP(3);
    const rsrc_t rws = mk(a.ws, WS_BYTES);
    const u32 l15 = lane & 15;
    if (!p3ok) {
      wait_vm<0>();
      prog_write(hp, 31);
      if (ploc >= 126) {
        const u32 et = (bat * 2 + (pi & 1)) * 4 + w4;
        const rsrc_t rwg = mk(a.wg, 131072);
        u32 e[64]; v4u b[8];
        const u32 eo = 8 * g * 512 + (et * 16 + l15) * 2;
        static_for<0, 64>([&](auto J) { e[J] = __builtin_amdgcn_raw_buffer_load_b16(rwg, eo + (J & 7) * 512, (J >> 3) * 16384, 0); });
        static_for<0, 8>([&](auto K) { b[K] = *(const GAS v4u*)(a.gp + l15 * 256 + K * 32 + 8 * g); });
        fence_sched();
        f32x4 d = {0, 0, 0, 0};
        static_for<0, 8>([&](auto K) {
          d = mfma(v4u{e[8 * K] | (e[8 * K + 1] << 16), e[8 * K + 2] | (e[8 * K + 3] << 16), e[8 * K + 4] | (e[8 * K + 5] << 16), e[8 * K + 6] | (e[8 * K + 7] << 16)}, b[K], d);
        });
        v4u o = __builtin_bit_cast(v4u, d);
        st_sc1(v4u{nosent(o.x), nosent(o.y), nosent(o.z), nosent(o.w)}, rws, WS_GE + l15 * 1024 + et * 64 + g * 16);
      } else if (pi == 125 && w4 == 0) {
        u32 epoch;
        asm volatile("s_load_dword %0, %1, 0x0\n\ts_waitcnt lgkmcnt(0)" : "=s"(epoch) : "s"(a.ws) : "memory");
        epoch += 1;
        for (u32 spins = 0; spins < SPIN_CAP; ++spins) {
          v4u f4;
          asm volatile("buffer_load_dwordx4 %0, %1, %2, 0 offen sc1\n\ts_waitcnt vmcnt(0)" : "=v"(f4) : "v"(WS_FLAGS + (lane >> 2) * 128 + (lane & 3) * 16), "s"(rws) : "memory");
          if (__all(f4.x == epoch && f4.y == epoch && f4.z == epoch && f4.w == epoch)) break;
        }
        if (lane == 0) __hip_atomic_store((GAS u32*)(a.ws + WS_EPOCH), epoch, __ATOMIC_RELAXED, __HIP_MEMORY_SCOPE_AGENT);
        {
          v4u t4 = __builtin_amdgcn_raw_buffer_load_b128(rws, 32768 + lane * 16, 0, 16);
          const u32 base = (u32)rtp - 100000;
          u32 mn = min(min(t4.x - base, t4.y - base), min(t4.z - base, t4.w - base));
          u32 mx = max(max(t4.x - base, t4.y - base), max(t4.z - base, t4.w - base));
          for (int o = 32; o; o >>= 1) { mn = min(mn, (u32)__shfl_xor((int)mn, o)); mx = max(mx, (u32)__shfl_xor((int)mx, o)); }
          mn = rfl(mn); mx = rfl(mx);
          const u32 dl = mn + 3000;
          for (u32 s = 0; s < 1000000 && (u32)__builtin_amdgcn_s_memrealtime() - base < dl; ++s) __builtin_amdgcn_s_sleep(1);
        }
      }
      return;
    }
    const u32 r2 = 2 * w4 + (lane >> 5);
    const u32 wfv = r2 * 512 + ((((lane >> 1) & 15) ^ r2) * 32) + (lane & 1) * 16;
    const rsrc_t rwf = mk(a.wf, 131072), rwo = mk(a.wo, 131072);
    so = 14 * 8192;
    rounds<1, 8192, 0x1ffff, true>(m, so, pv, wfv, wfv + 4096, rwf, hp);
    prog_wait<C_W2A, 1>(cpv);
    rounds<5, 8192, 0x1ffff, true>(m, so, pv, wfv, wfv + 4096, rwf, hp);
    prog_wait<C_W2B, 1>(cpv);
    rounds<5, 8192, 0x1ffff, true>(m, so, pv, wfv, wfv + 4096, rwf, hp);
    prog_wait<C_W2C, 1>(cpv);
    rounds<5, 8192, 0x1ffff, true>(m, so, pv, wfv, wfv + 4096, rwf, hp);
    wait_vm<0>();
    prog_write(hp, 47);
    STAMP(4);
    m = LB + L_RING + w4 * 1024; so = 0;
    prog_wait<C_WFA, 1>(cpv);
    rounds<8, 8192, 0x1ffff, false>(m, so, pv, wfv, wfv + 4096, rwo, hp);
    prog_wait<C_WFB, 1>(cpv);
    rounds<8, 8192, 0x1ffff, false>(m, so, pv, wfv, wfv + 4096, rwo, hp);
    STAMP(5);
    wait_vm<0>();
    STAMP(6);
  } else {
    __builtin_amdgcn_s_setprio(CPRIO);
    const rsrc_t rws = mk(a.ws, WS_BYTES);
    const u32 p1off = WS_P1 + k1 * 524288 + (2 * n1 + (wv >> 1)) * 65536 + mt * 4096 + (wv & 1) * 2048;
    const u32 p2off = WS_P2 + c2 * 262144 + mt * 16384 + n2 * 8192 + wv * 2048;
    const v4u sv = {SENT, SENT, SENT, SENT};
    u32 epoch;
    __builtin_amdgcn_raw_buffer_store_b32((u32)rtp, rws, tid == 0 ? 32768 + bid * 4 : OOB, 0, 16);
    st_sc1(sv, rws, p1off + lane * 16);
    st_sc1(sv, rws, p1off + 1024 + lane * 16);
    asm volatile("s_load_dword %0, %1, 0x0" : "=s"(epoch) : "s"(a.ws) : "memory");
    asm volatile("" : "+v"(lane));
    const u32 l15 = lane & 15, g = lane >> 4, q4 = l15 >> 2, p4 = lane & 3;
    const u32 cp = LB + L_CP + wv * 4, cpv = LB + L_CP + p4 * 4, hpv = LB + L_HP + p4 * 4;
    f32x4 acc[4] = {{0, 0, 0, 0}, {0, 0, 0, 0}, {0, 0, 0, 0}, {0, 0, 0, 0}};
    u32 fl;
    const u32 fline = lane < 48 ? min(max(mt + g, 1u) - 1, 15u) : 7 + (lane & 8);
    const u32 foff = WS_FLAGS + fline * 128 + (lane < 48 ? l15 : 8 + (lane & 7)) * 4;
    u32 t2v, t3v, gsv, flv;
    {
      const u32 ln = (mt * 4 + wv) * 15 + lane;
      t2v = lane < 15 ? (ln >> 6) * 131072 + (c2 * 32 + ((ln >> 1) & 31)) * 512 + n2 * 256 + (ln & 1) * 128 : OOB;
      t3v = lane < 16 ? ((mt * 4 + wv) * 16 + lane) * 128 : OOB;
      gsv = ploc >= 126 ? WS_GE + (wv * 4 + g) * 1024 + (bat * 2 + (pi & 1)) * 256 + l15 * 16 : OOB;
      flv = tid == 0 ? WS_FLAGS + mt * 128 + idx16 * 4 : OOB;
    }
    const rsrc_t rw2 = mk(a.w2, 15 * 131072), rw3 = mk((j & 1) ? a.wo : a.wf, 131072);
    asm volatile("s_waitcnt lgkmcnt(0)" : "+s"(epoch), "+v"(t2v), "+v"(t3v), "+v"(gsv), "+v"(flv)::"memory");
    epoch += 1;
    STAMP(1);
    {
      v4u F[2][21];
      v4u Wf[2][2];
      const u32 xr = LB + L_X + l15 * 1056 + g * 16;
      const u32 w1r = LB + L_RING + g * 1024 + q4 * 128 + ((wv ^ ((q4 >> 1) + 2 * (g & 1))) * 32) + p4 * 8;
      prog_wait<1, 0>(hpv);
      STAMP(2);
      static_for<0, 8>([&](auto Q) { F[0][Q] = lds128(xr + Q * 128); F[1][Q] = lds128(xr + Q * 128 + 64); });
      prog_wait<2, 0>(hpv);
      static_for<0, 15>([&](auto I) {
        constexpr int i = I;
        Wf[i & 1][0] = ldstr2(w1r + i * 8192, w1r + i * 8192 + 512);
        Wf[i & 1][1] = ldstr2(w1r + i * 8192 + 4096, w1r + i * 8192 + 4096 + 512);
        if constexpr (i >= 2) {
          F[0][i + 6] = lds128(xr + ((i + 6) >> 3) * 1056 + ((i + 6) & 7) * 128);
          F[1][i + 6] = lds128(xr + ((i + 6) >> 3) * 1056 + ((i + 6) & 7) * 128 + 64);
        }
        fence_sched();
        if constexpr (i >= 1) static_for<0, 4>([&](auto J) { acc[J] = mfma(Wf[(i - 1) & 1][0], F[0][2 * J + i - 1], acc[J]); });
        fence_sched();
        u32 hv = 0, cv = 0;
        if constexpr (i < 14) hv = prog_read(hpv);
        if constexpr (i == 10) cv = prog_read(cpv);
        fence_sched();
        if constexpr (i >= 1) static_for<0, 4>([&](auto J) { acc[J] = mfma(Wf[(i - 1) & 1][1], F[1][2 * J + i - 1], acc[J]); });
        fence_sched();
        if constexpr (i == 2) {
          st_sc1(sv, rws, p2off + lane * 16);
          st_sc1(sv, rws, p2off + 1024 + lane * 16);
          st_sc1(sv, rws, gsv);
        }
        if constexpr (i == 4) dma4(LB + L_JUNK, t2v, rw2, 0);
        if constexpr (i == 9) dma4(LB + L_JUNK, t3v, rw3, 0);
        if constexpr (i == 8) { wait_vm<0>(); prog_write(cp, C_S8); }
        if constexpr (i == 10) {
          prog_ensure<C_S8, 0>(cv, cpv);
          __builtin_amdgcn_raw_buffer_store_b32(epoch, rws, flv, 0, 16);
        }
        if constexpr (i == 12) prog_write(cp, C_S12);
        if constexpr (i < 14) prog_ensure<i + 3, 0>(hv, hpv);
        fence_sched();
        SSTAMP(i + 1);
      });
      static_for<0, 2>([&](auto C) { static_for<0, 4>([&](auto J) { acc[J] = mfma(Wf[0][C], F[C][2 * J + 14], acc[J]); }); });
    }
    STAMP(3);
    st_sc1(pack2(acc[0], acc[1]), rws, p1off + l15 * 64 + g * 16);
    st_sc1(pack2(acc[2], acc[3]), rws, p1off + 1024 + l15 * 64 + g * 16);
    prog_write(cp, C_P1);
    asm volatile("buffer_load_dword %0, %1, %2, 0 offen sc1" : "=v"(fl) : "v"(foff), "s"(rws) : "memory");
    STAMP(4);
    v4u W2[2][15];
    const u32 w2r = LB + L_RING + (8 * g + q4) * 256 + (((2 * wv) ^ (q4 | ((g & 1) << 2))) * 32) + p4 * 8;
    auto rdw2 = [&](auto T) {
      constexpr u32 s = (T + 15) & 15;
      W2[0][T] = ldstr2(w2r + s * 8192, w2r + s * 8192 + 1024);
      W2[1][T] = ldstr2((w2r ^ 32) + s * 8192, (w2r ^ 32) + s * 8192 + 1024);
    };
    auto lds_done = [&]() { asm volatile("s_waitcnt lgkmcnt(0)" ::: "memory"); };
    prog_wait<21, 0>(hpv);
    static_for<0, 5>(rdw2);
    lds_done(); prog_write(cp, C_W2A);
    v4u hp[4]; u32 hh[4];
    u32 rho, rhoh, h1off, h1offh;
    {
      const u32 mm = lane >> 2;
      rho = 8 + 4 * mm + 2 * (wv & 1);
      h1off = mloc * 64 + rho - 8 < 500 ? WS_P1 + c2 * 65536 + mt * 4096 + wv * 1024 + lane * 16 : OOB;
      rho = LB + L_X + rho * 64 + (rho >> 2) * 32 + (wv >> 1) * 32 + p4 * 8;
      const u32 nx = lane >> 5, mh = (lane >> 4) & 1, dw = lane & 3;
      rhoh = 72 * nx + 4 * mh + 2 * (wv & 1) + (dw >> 1);
      const int hrow = (int)(mloc * 64 + rhoh) - 8;
      h1offh = (hrow >= 0 && hrow < 500) ? WS_P1 + c2 * 65536 + (mt + 2 * nx - 1) * 4096 + wv * 1024 + (nx ? 0 : 896) + (lane & 31) * 4 : OOB;
      rhoh = LB + L_X + rhoh * 64 + (rhoh >> 2) * 32 + (wv >> 1) * 32 + q4 * 8 + (dw & 1) * 4;
    }
    asm volatile("s_waitcnt vmcnt(0)" : "+v"(fl)::"memory");
    for (u32 spins = 0; !__all(fl == epoch) && spins < SPIN_CAP; ++spins)
      asm volatile("buffer_load_dword %0, %1, %2, 0 offen sc1\n\ts_waitcnt vmcnt(0)" : "=v"(fl) : "v"(foff), "s"(rws) : "memory");
    STAMP(5);
    delay(DLY1);
    auto h1_load = [&]() {
      static_for<0, 4>([&](auto K) { hp[K] = __builtin_amdgcn_raw_buffer_load_b128(rws, h1off, K * 524288, 16); });
      static_for<0, 4>([&](auto K) { hh[K] = __builtin_amdgcn_raw_buffer_load_b32(rws, h1offh, K * 524288, 16); });
      mem_order();
    };
    h1_load();
    prog_wait<26, 0>(hpv);
    static_for<5, 10>(rdw2);
    lds_done(); prog_write(cp, C_W2B);
    prog_wait<31, 0>(hpv);
    static_for<10, 15>(rdw2);
    lds_done(); prog_write(cp, C_W2C);
    STAMP(6);
    for (u32 spins = 0;; ++spins) {
      const u32 mx = max(max(max(max4(hp[0]), max4(hp[1])), max(max4(hp[2]), max4(hp[3]))), max(max(hh[0], hh[1]), max(hh[2], hh[3])));
      if (__all(mx != SENT) || spins >= SPIN_CAP) break;
      h1_load();
    }
    STAMP(7);
    {
      float v[8];
      static_for<0, 4>([&](auto D) {
        v[2 * D] = gelu((lo16(hp[0][+D]) + lo16(hp[1][+D])) + (lo16(hp[2][+D]) + lo16(hp[3][+D])));
        v[2 * D + 1] = gelu((hi16(hp[0][+D]) + hi16(hp[1][+D])) + (hi16(hp[2][+D]) + hi16(hp[3][+D])));
      });
      const float u0 = gelu((lo16(hh[0]) + lo16(hh[1])) + (lo16(hh[2]) + lo16(hh[3])));
      const float u1 = gelu((hi16(hh[0]) + hi16(hh[1])) + (hi16(hh[2]) + hi16(hh[3])));
      *(LAS v2u*)(size_t)(rho) = v2u{pk(v[0], v[1]), pk(v[2], v[3])};
      *(LAS v2u*)(size_t)(rho + 64) = v2u{pk(v[4], v[5]), pk(v[6], v[7])};
      *(LAS u32*)(size_t)(rhoh) = pk(u0, u1);
    }
    prog_write(cp, C_A);
    STAMP(8);
    prog_wait<C_A, 0>(cpv);
    STAMP(9);
    {
      static_for<0, 4>([&](auto J) { acc[J] = f32x4{0, 0, 0, 0}; });
      const u32 ar = LB + L_X + l15 * 288 + g * 16;
      v4u A[17];
      static_for<0, 17>([&](auto Q) { A[Q] = lds128(ar + ((Q + 1) >> 2) * 288 + ((Q + 1) & 3) * 64); });
      static_for<0, 15>([&](auto T) {
        acc[0] = mfma(W2[0][T], A[T], acc[0]);
        acc[2] = mfma(W2[1][T], A[T], acc[2]);
        acc[1] = mfma(W2[0][T], A[T + 2], acc[1]);
        acc[3] = mfma(W2[1][T], A[T + 2], acc[3]);
      });
      st_sc1(pack2(acc[0], acc[1]), rws, p2off + l15 * 64 + g * 16);
      st_sc1(pack2(acc[2], acc[3]), rws, p2off + 1024 + l15 * 64 + g * 16);
    }
    STAMP(10);
    if (!p3ok) return;
    v4u WF[8][4];
    const u32 wr = LB + L_RING + (4 * g + q4) * 512 + (((4 * wv) ^ ((4 * g + q4) & 7)) * 32) + p4 * 8;
    auto rdwf = [&](auto K) { static_for<0, 4>([&](auto T) { WF[K][T] = ldstr2((wr ^ (T * 32)) + K * 16384, (wr ^ (T * 32)) + K * 16384 + 8192); }); };
    prog_wait<41, 0>(hpv);
    static_for<0, 4>(rdwf);
    lds_done(); prog_write(cp, C_WFA);
    prog_wait<47, 0>(hpv);
    static_for<4, 8>(rdwf);
    lds_done(); prog_write(cp, C_WFB);
    STAMP(11);
    delay(DLY2);
    u32 fp[8];
    v4u ge[4];
    {
      const u32 h2off = WS_P2 + mt * 16384 + (tid >> 4) * 1024 + idx16 * 64 + l15 * 4;
      const u32 geoff = WS_GE + (bat * 8 + (lane & 7)) * 1024 + wv * 256 + g * 16;
      for (u32 spins = 0;; ++spins) {
        mem_order();
        static_for<0, 8>([&](auto K) { fp[K] = __builtin_amdgcn_raw_buffer_load_b32(rws, h2off, K * 262144, 16); });
        static_for<0, 4>([&](auto T) { ge[T] = __builtin_amdgcn_raw_buffer_load_b128(rws, geoff + T * 64, 0, 16); });
        u32 mx = max(max(max4(ge[0]), max4(ge[1])), max(max4(ge[2]), max4(ge[3])));
        static_for<0, 8>([&](auto K) { mx = max(mx, fp[K]); });
        if (__all(mx != SENT) || spins >= SPIN_CAP) break;
      }
      STAMP(12);
      float s0 = 0.f, s1 = 0.f;
      static_for<0, 8>([&](auto K) { s0 += lo16(fp[K]); s1 += hi16(fp[K]); });
      const u32 pos = (tid >> 5) * 32 + q4 * 8 + ((tid >> 4) & 1) * 4 + (tid & 1) * 2;
      *(LAS u32*)(size_t)(LB + L_F + ((tid >> 1) & 1) * 576 + pos * 2) = pk(gelu(s0), gelu(s1));
    }
    prog_write(cp, C_F);
    prog_wait<C_F, 0>(cpv);
    {
      f32x4 d[4];
      static_for<0, 4>([&](auto T) { d[T] = __builtin_bit_cast(f32x4, ge[T]); });
      const u32 fr = LB + L_F + (l15 >> 3) * 576 + g * 16;
      v4u b[8];
      static_for<0, 8>([&](auto K) { b[K] = lds128(fr + K * 64); });
      static_for<0, 2>([&](auto H) {
        static_for<0, 8>([&](auto K) { d[2 * H] = mfma(WF[K][2 * H], b[K], d[2 * H]); d[2 * H + 1] = mfma(WF[K][2 * H + 1], b[K], d[2 * H + 1]); });
      });
      static_for<0, 2>([&](auto H) {
        const f32x4 d0 = d[2 * H], d1 = d[2 * H + 1];
        v4u jf = {pk(tanh_(d0.x), tanh_(d0.y)), pk(tanh_(d0.z), tanh_(d0.w)), pk(tanh_(d1.x), tanh_(d1.y)), pk(tanh_(d1.z), tanh_(d1.w))};
        *(LAS v4u*)(size_t)(LB + L_J + (2 * wv + H) * 1024 + lane * 16) = jf;
      });
    }
    STAMP(13);
  }
  bar();
  {
    asm volatile("" : "+v"(lane));
    const u32 l15 = lane & 15, g = lane >> 4, q4 = l15 >> 2, p4 = lane & 3;
    const u32 wr0 = LB + L_RING + (4 * g + q4) * 512 + (((2 * wv) ^ ((4 * g + q4) & 7)) * 32) + p4 * 8;
    const u32 wr1 = wr0 ^ 32;
    f32x4 d0 = {0, 0, 0, 0}, d1 = {0, 0, 0, 0};
    static_for<0, 8>([&](auto K) {
      const v4u b = lds128(LB + L_J + K * 1024 + lane * 16);
      d0 = mfma(ldstr2(wr0 + K * 16384, wr0 + K * 16384 + 8192), b, d0);
      d1 = mfma(ldstr2(wr1 + K * 16384, wr1 + K * 16384 + 8192), b, d1);
    });
    u16* op = a.out + ((bat * 250 + 2 * ploc) * 8 + l15) * 256 + wv * 32 + 4 * g;
    *(GAS v2u*)(op) = v2u{pk(d0.x, d0.y), pk(d0.z, d0.w)};
    *(GAS v2u*)(op + 16) = v2u{pk(d1.x, d1.y), pk(d1.z, d1.w)};
  }
}

extern "C" void kernel_launch(void* const* d_in, const int* in_sizes, int n_in, void* d_out, int out_size, void* d_ws, size_t ws_size,
                              hipStream_t stream) {
  Args a{(const u16*)d_in[0], (const u16*)d_in[1], (const u16*)d_in[2], (const u16*)d_in[3], (const u16*)d_in[4],
         (const u16*)d_in[5], (const u16*)d_in[6], (u16*)d_out, (unsigned char*)d_ws};
  rnnt_fused<<<256, 512, 0, stream>>>(a);
}
```

```cpp
#include <hip/hip_runtime.h>
#include <hip/hip_bf16.h>
#include <type_traits>

typedef unsigned u32;
typedef unsigned short u16;
typedef u32 v4u __attribute__((ext_vector_type(4)));
typedef u32 v2u __attribute__((ext_vector_type(2)));
typedef float f32x4 __attribute__((ext_vector_type(4)));
typedef float f32x2 __attribute__((ext_vector_type(2)));
typedef short s16x4 __attribute__((ext_vector_type(4)));
typedef __bf16 bf16x8 __attribute__((ext_vector_type(8)));
typedef __bf16 bf16x2 __attribute__((ext_vector_type(2)));
typedef __amdgpu_buffer_rsrc_t rsrc_t;
#define GAS __attribute__((address_space(1)))
#define LAS __attribute__((address_space(3)))
#define DI __device__ __forceinline__

constexpr u32 WS_EPOCH = 0;
constexpr u32 WS_FLAGS = 4096;
constexpr u32 WS_GE = 65536;
constexpr u32 WS_P1 = 1u << 20;
constexpr u32 WS_P2 = 3u << 20;
constexpr u32 WS_BYTES = 5u << 20;
constexpr u32 OOB = 0x80000000u;
constexpr u32 SENT = 0xFFFFFFFFu;
constexpr u32 SPIN_CAP = 100000;
constexpr u32 LSPIN_CAP = 4000000;
constexpr u32 L_RING = 0;
constexpr u32 L_X = 131072;
constexpr u32 L_F = L_X + 19008;
constexpr u32 L_J = L_F + 1152;
constexpr u32 L_JUNK = L_J + 8192;
constexpr u32 L_HP = L_JUNK + 1024;
constexpr u32 L_CP = L_HP + 16;
constexpr u32 LDS_BYTES = L_CP + 16;
static_assert(LDS_BYTES <= 163840, "lds");
#define CODE_LINES 112
#define LOOK 4
#define SOFF 0
#define CPRIO 1
#define DLY1 0
#define DLY2 0

template <int I, int N, class F> DI void static_for(F&& f) {
  if constexpr (I < N) { f(std::integral_constant<int, I>{}); static_for<I + 1, N>(f); }
}
template <int N> DI void wait_vm() { asm volatile("s_waitcnt vmcnt(%0)" ::"i"(N) : "memory"); }
DI void bar() { asm volatile("s_waitcnt lgkmcnt(0)\n\ts_barrier" ::: "memory"); }
DI void fence_sched() { __builtin_amdgcn_sched_barrier(0); }
DI void mem_order() { asm volatile("" ::: "memory"); }
DI u32 rfl(u32 x) { return __builtin_amdgcn_readfirstlane(x); }
#define WAUX ""
DI void dma16(u32 lds, u32 voff, rsrc_t rs, u32 soff) {
  asm volatile("s_mov_b32 m0, %0\n\ts_nop 0\n\tbuffer_load_dwordx4 %1, %2, %3 offen lds" WAUX ::"s"(lds), "v"(voff), "s"(rs), "s"(soff) : "memory");
}
DI void dma4(u32 lds, u32 voff, rsrc_t rs, u32 soff) {
  asm volatile("s_mov_b32 m0, %0\n\ts_nop 0\n\tbuffer_load_dword %1, %2, %3 offen lds" ::"s"(lds), "v"(voff), "s"(rs), "s"(soff) : "memory");
}
DI rsrc_t mk(const void* p, u32 bytes) { return __builtin_amdgcn_make_buffer_rsrc((void*)p, 0, (int)bytes, 0x00020000); }
DI v4u lds128(u32 a) { return *(const LAS v4u*)(size_t)a; }
DI v2u ldstr(u32 a) { return __builtin_bit_cast(v2u, __builtin_amdgcn_ds_read_tr16_b64_v4i16((LAS s16x4*)(size_t)a)); }
DI v4u ldstr2(u32 a0, u32 a1) { v2u x = ldstr(a0), y = ldstr(a1); return v4u{x.x, x.y, y.x, y.y}; }
DI f32x4 mfma(v4u a, v4u b, f32x4 c) {
  return __builtin_amdgcn_mfma_f32_16x16x32_bf16(__builtin_bit_cast(bf16x8, a), __builtin_bit_cast(bf16x8, b), c, 0, 0, 0);
}
DI u32 pk(float a, float b) { f32x2 f = {a, b}; return __builtin_bit_cast(u32, __builtin_convertvector(f, bf16x2)); }
DI float lo16(u32 x) { return __builtin_bit_cast(float, x << 16); }
DI float hi16(u32 x) { return __builtin_bit_cast(float, x & 0xFFFF0000u); }
DI float gelu(float x) {
  const float c0 = -2.0f * 0.7978845608028654f * 1.4426950408889634f, c1 = c0 * 0.044715f;
  float e = __builtin_amdgcn_exp2f(x * __builtin_fmaf(x * x, c1, c0));
  return x * __builtin_amdgcn_rcpf(1.0f + e);
}
DI float tanh_(float x) {
  float e = __builtin_amdgcn_exp2f(x * 2.8853900817779268f);
  return __builtin_fmaf(-2.0f, __builtin_amdgcn_rcpf(1.0f + e), 1.0f);
}
DI u32 nosent(u32 x) { return min(x, SENT - 1); }
DI v4u pack2(f32x4 a, f32x4 b) { return v4u{nosent(pk(a.x, a.y)), nosent(pk(a.z, a.w)), nosent(pk(b.x, b.y)), nosent(pk(b.z, b.w))}; }
DI void st_sc1(v4u v, rsrc_t rs, u32 off) { __builtin_amdgcn_raw_buffer_store_b128(v, rs, off, 0, 16); }
DI u32 max4(v4u v) { return max(max(v.x, v.y), max(v.z, v.w)); }
DI u32 min4(v4u v) { return min(min(v.x, v.y), min(v.z, v.w)); }
DI u32 prog_read(u32 va) { u32 v = *(const volatile LAS u32*)(size_t)va; mem_order(); return v; }
DI void prog_write(u32 a, u32 v) { mem_order(); *(volatile LAS u32*)(size_t)a = v; mem_order(); }
template <u32 NEED, int SLEEP> DI void prog_ensure(u32 hv, u32 va) {
  u32 cnt;
  asm volatile("v_cmp_gt_u32 vcc, %3, %0\n\ts_cbranch_vccz Ld%=\n\ts_mov_b32 %1, %5\n"
               "Lr%=:\n\ts_sleep %4\n\tds_read_b32 %0, %2\n\ts_waitcnt lgkmcnt(0)\n\tv_cmp_gt_u32 vcc, %3, %0\n\ts_cbranch_vccz Ld%=\n\t"
               "s_sub_u32 %1, %1, 1\n\ts_cbranch_scc0 Lr%=\n"
               "Ld%=:"
               : "+v"(hv), "=&s"(cnt) : "v"(va), "i"(NEED), "i"(SLEEP), "i"(LSPIN_CAP) : "memory", "scc", "vcc");
}
template <u32 NEED, int SLEEP> DI void prog_wait(u32 va) { prog_ensure<NEED, SLEEP>(prog_read(va), va); }
template <int N, u32 STEP, u32 SMASK, bool PUB> DI void rounds(u32& m, u32& so, u32& pv, u32 va, u32 vb, rsrc_t rs, u32 hp) {
  u32 cnt;
  if constexpr (PUB)
    asm volatile("s_mov_b32 %3, %9\n"
               "L%=:\n\ts_mov_b32 m0, %0\n\ts_add_u32 %0, %0, 0x1000\n\tbuffer_load_dwordx4 %4, %6, %1 offen lds" WAUX "\n\t"
               "s_mov_b32 m0, %0\n\ts_add_u32 %0, %0, 0x1000\n\tbuffer_load_dwordx4 %5, %6, %1 offen lds" WAUX "\n\t"
               "s_and_b32 %0, %0, 0x1ffff\n\ts_add_u32 %1, %1, %8\n\ts_and_b32 %1, %1, %10\n\tv_add_u32 %2, 1, %2\n\t"
               "s_waitcnt vmcnt(%11)\n\tds_write_b32 %7, %2\n\ts_sub_u32 %3, %3, 1\n\ts_cbranch_scc0 L%="
               : "+s"(m), "+s"(so), "+v"(pv), "=&s"(cnt)
               : "v"(va), "v"(vb), "s"(rs), "v"(hp), "i"(STEP), "i"(N - 1), "i"(SMASK), "i"(2 * LOOK) : "memory", "scc");
  else
    asm volatile("s_mov_b32 %2, %7\n"
               "L%=:\n\ts_mov_b32 m0, %0\n\ts_add_u32 %0, %0, 0x1000\n\tbuffer_load_dwordx4 %3, %5, %1 offen lds" WAUX "\n\t"
               "s_mov_b32 m0, %0\n\ts_add_u32 %0, %0, 0x1000\n\tbuffer_load_dwordx4 %4, %5, %1 offen lds" WAUX "\n\t"
               "s_and_b32 %0, %0, 0x1ffff\n\ts_add_u32 %1, %1, %6\n\ts_and_b32 %1, %1, %8\n\t"
               "s_sub_u32 %2, %2, 1\n\ts_cbranch_scc0 L%="
               : "+s"(m), "+s"(so), "=&s"(cnt)
               : "v"(va), "v"(vb), "s"(rs), "i"(STEP), "i"(N - 1), "i"(SMASK) : "memory", "scc");
}
DI void delay(int n) { for (int i = 0; i < n; ++i) __builtin_amdgcn_s_sleep(1); }

#define SSTAMP(i)
#define STAMP(i)

struct Args { const u16 *x, *w1, *w2, *gp, *wf, *wg, *wo; u16* out; unsigned char* ws; };

constexpr u32 C_S8 = 1, C_S12 = 2, C_P1 = 3, C_W2A = 4, C_W2B = 5, C_W2C = 6, C_A = 7, C_WFA = 8, C_WFB = 9, C_F = 10;

__global__ void __launch_bounds__(512, 2) rnnt_fused(Args a) {
  __shared__ __attribute__((aligned(16))) unsigned char smem[LDS_BYTES];
  const u32 LB = (u32)(size_t)(LAS unsigned char*)smem;
  const unsigned long long rtp = __builtin_amdgcn_s_memrealtime();
  u32 tid = threadIdx.x;
  {
    unsigned long long pc;
    asm volatile("s_mov_b32 m0, %2\n\ts_getpc_b64 %0\n\tglobal_load_lds_dword %1, %0" : "=&s"(pc) : "v"(min(tid, (u32)CODE_LINES - 1) << 7), "s"(LB + L_JUNK) : "memory");
    if (tid < 8) *(volatile LAS u32*)(size_t)(LB + L_HP + tid * 4) = 0;
  }
  const u32 wv = rfl(tid >> 6), w4 = wv & 3;
  const u32 bid = blockIdx.x, x8 = bid & 7, j = bid >> 3, mt = j >> 1, mloc = mt & 7, bat = mt >> 3;
  const u32 k1 = x8 >> 1, n1 = 2 * (x8 & 1) + (j & 1);
  const u32 c2 = x8, n2 = j & 1;
  const u32 idx16 = x8 * 2 + (j & 1), pi = mt * 16 + idx16, ploc = pi & 127;
  const bool p3ok = ploc < 125;

  u32 lane = tid & 63;
  const rsrc_t rw1 = mk(a.w1, 15 * 131072);
  u32 xv, w1v;
  {
    const u32 rr8 = lane >> 3;
    xv = (u32)((int)(mloc * 128 + w4 * 8 + rr8) - 7) * 512 + k1 * 128 + (lane & 7) * 16;
    w1v = (k1 * 64 + w4 * 8 + rr8) * 512 + n1 * 128 + ((((lane >> 1) & 3) ^ (((rr8 >> 1) & 1) + 2 * (w4 & 1))) * 32) + (lane & 1) * 16;
    constexpr u32 NT = 15 - LOOK;
    const u32 ln = (mt * 4 + w4) * NT + lane;
    const u32 tv = (LOOK + (ln >> 6)) * 131072 + (k1 * 64 + (ln & 63)) * 512 + n1 * 128;
    if (wv < 4) xv = lane < NT ? tv : OOB;
    asm volatile("" : "+v"(xv), "+v"(w1v));
  }
  bar();
  if (wv < 4) dma4(LB + L_JUNK + 256, xv, rw1, 0);

  if (__builtin_expect(wv >= 4, 1)) {
    const rsrc_t rx = mk(a.x + bat * 256000, 512000);
    static_for<0, 4>([&](auto I) { dma16(LB + L_X + (w4 + 4 * I) * 1056, xv + I * 16384, rx, 0); });
    dma16(LB + L_X + ((w4 & 1) + 16) * 1056, w4 < 2 ? xv + 4 * 16384 : xv + 3 * 16384 + 8192, rx, 0);
    u32 m = LB + L_RING + w4 * 1024, so = 0, pv = 0;
    const u32 hp = LB + L_HP + w4 * 4, cpv = LB + L_CP + (lane & 3) * 4;
    rounds<LOOK - 1, 131072, ~0u, false>(m, so, pv, w1v, w1v + 16384, rw1, hp);
    STAMP(1);
    rounds<16 - LOOK, 131072, ~0u, true>(m, so, pv, w1v, w1v + 16384, rw1, hp);
    STAMP(2);
    asm volatile("" : "+v"(lane));
    const u32 g = lane >> 4;
    const rsrc_t rw2 = mk(a.w2, 15 * 131072);
    const u32 w2v = (c2 * 32 + w4 * 4 + g) * 512 + n2 * 256 + ((((lane >> 1) & 7) ^ (g | (((w4 >> 1) & 1) << 2))) * 32) + (lane & 1) * 16;
    so = 0;
    rounds<1, 131072, ~0u, true>(m, so, pv, w2v, w2v + 8192, rw2, hp);
    prog_wait<C_S8, 1>(cpv);
    rounds<7, 131072, ~0u, true>(m, so, pv, w2v, w2v + 8192, rw2, hp);
    prog_wait<C_S12, 1>(cpv);
    rounds<4, 131072, ~0u, true>(m, so, pv, w2v, w2v + 8192, rw2, hp);
    prog_wait<C_P1, 1>(cpv);
    rounds<3, 131072, ~0u, true>(m, so, pv, w2v, w2v + 8192, rw2, hp);
    STAMP(3);
    const rsrc_t rws = mk(a.ws, WS_BYTES);
    const u32 l15 = lane & 15;
    if (!p3ok) {
      wait_vm<0>();
      prog_write(hp, 31);
      if (ploc >= 126) {
        const u32 et = (bat * 2 + (pi & 1)) * 4 + w4;
        const rsrc_t rwg = mk(a.wg, 131072);
        u32 e[64]; v4u b[8];
        const u32 eo = 8 * g * 512 + (et * 16 + l15) * 2;
        static_for<0, 64>([&](auto J) { e[J] = __builtin_amdgcn_raw_buffer_load_b16(rwg, eo + (J & 7) * 512, (J >> 3) * 16384, 0); });
        static_for<0, 8>([&](auto K) { b[K] = *(const GAS v4u*)(a.gp + l15 * 256 + K * 32 + 8 * g); });
        fence_sched();
        f32x4 d = {0, 0, 0, 0};
        static_for<0, 8>([&](auto K) {
          d = mfma(v4u{e[8 * K] | (e[8 * K + 1] << 16), e[8 * K + 2] | (e[8 * K + 3] << 16), e[8 * K + 4] | (e[8 * K + 5] << 16), e[8 * K + 6] | (e[8 * K + 7] << 16)}, b[K], d);
        });
        v4u o = __builtin_bit_cast(v4u, d);
        st_sc1(v4u{nosent(o.x), nosent(o.y), nosent(o.z), nosent(o.w)}, rws, WS_GE + l15 * 1024 + et * 64 + g * 16);
      } else if (pi == 125 && w4 == 0) {
        u32 epoch;
        asm volatile("s_load_dword %0, %1, 0x0\n\ts_waitcnt lgkmcnt(0)" : "=s"(epoch) : "s"(a.ws) : "memory");
        epoch += 1;
        for (u32 spins = 0; spins < SPIN_CAP; ++spins) {
          v4u f4;
          asm volatile("buffer_load_dwordx4 %0, %1, %2, 0 offen sc1\n\ts_waitcnt vmcnt(0)" : "=v"(f4) : "v"(WS_FLAGS + (lane >> 2) * 128 + (lane & 3) * 16), "s"(rws) : "memory");
          if (__all(f4.x == epoch && f4.y == epoch && f4.z == epoch && f4.w == epoch)) break;
        }
        if (lane == 0) __hip_atomic_store((GAS u32*)(a.ws + WS_EPOCH), epoch, __ATOMIC_RELAXED, __HIP_MEMORY_SCOPE_AGENT);
        {
          v4u t4 = __builtin_amdgcn_raw_buffer_load_b128(rws, 32768 + lane * 16, 0, 16);
          const u32 base = (u32)rtp - 100000;
          u32 mn = min(min(t4.x - base, t4.y - base), min(t4.z - base, t4.w - base));
          u32 mx = max(max(t4.x - base, t4.y - base), max(t4.z - base, t4.w - base));
          for (int o = 32; o; o >>= 1) { mn = min(mn, (u32)__shfl_xor((int)mn, o)); mx = max(mx, (u32)__shfl_xor((int)mx, o)); }
          mn = rfl(mn); mx = rfl(mx);
          const u32 dl = mx + 3000;
          for (u32 s = 0; s < 1000000 && (u32)__builtin_amdgcn_s_memrealtime() - base < dl; ++s) __builtin_amdgcn_s_sleep(1);
        }
      }
      return;
    }
    const u32 r2 = 2 * w4 + (lane >> 5);
    const u32 wfv = r2 * 512 + ((((lane >> 1) & 15) ^ r2) * 32) + (lane & 1) * 16;
    const rsrc_t rwf = mk(a.wf, 131072), rwo = mk(a.wo, 131072);
    so = 14 * 8192;
    rounds<1, 8192, 0x1ffff, true>(m, so, pv, wfv, wfv + 4096, rwf, hp);
    prog_wait<C_W2A, 1>(cpv);
    rounds<5, 8192, 0x1ffff, true>(m, so, pv, wfv, wfv + 4096, rwf, hp);
    prog_wait<C_W2B, 1>(cpv);
    rounds<5, 8192, 0x1ffff, true>(m, so, pv, wfv, wfv + 4096, rwf, hp);
    prog_wait<C_W2C, 1>(cpv);
    rounds<5, 8192, 0x1ffff, true>(m, so, pv, wfv, wfv + 4096, rwf, hp);
    wait_vm<0>();
    prog_write(hp, 47);
    STAMP(4);
    m = LB + L_RING + w4 * 1024; so = 0;
    prog_wait<C_WFA, 1>(cpv);
    rounds<8, 8192, 0x1ffff, false>(m, so, pv, wfv, wfv + 4096, rwo, hp);
    prog_wait<C_WFB, 1>(cpv);
    rounds<8, 8192, 0x1ffff, false>(m, so, pv, wfv, wfv + 4096, rwo, hp);
    STAMP(5);
    wait_vm<0>();
    STAMP(6);
  } else {
    __builtin_amdgcn_s_setprio(CPRIO);
    const rsrc_t rws = mk(a.ws, WS_BYTES);
    const u32 p1off = WS_P1 + k1 * 524288 + (2 * n1 + (wv >> 1)) * 65536 + mt * 4096 + (wv & 1) * 2048;
    const u32 p2off = WS_P2 + c2 * 262144 + mt * 16384 + n2 * 8192 + wv * 2048;
    const v4u sv = {SENT, SENT, SENT, SENT};
    u32 epoch;
    __builtin_amdgcn_raw_buffer_store_b32((u32)rtp, rws, tid == 0 ? 32768 + bid * 4 : OOB, 0, 16);
    st_sc1(sv, rws, p1off + lane * 16);
    st_sc1(sv, rws, p1off + 1024 + lane * 16);
    asm volatile("s_load_dword %0, %1, 0x0" : "=s"(epoch) : "s"(a.ws) : "memory");
    asm volatile("" : "+v"(lane));
    const u32 l15 = lane & 15, g = lane >> 4, q4 = l15 >> 2, p4 = lane & 3;
    const u32 cp = LB + L_CP + wv * 4, cpv = LB + L_CP + p4 * 4, hpv = LB + L_HP + p4 * 4;
    f32x4 acc[4] = {{0, 0, 0, 0}, {0, 0, 0, 0}, {0, 0, 0, 0}, {0, 0, 0, 0}};
    u32 fl;
    const u32 fline = lane < 48 ? min(max(mt + g, 1u) - 1, 15u) : 7 + (lane & 8);
    const u32 foff = WS_FLAGS + fline * 128 + (lane < 48 ? l15 : 8 + (lane & 7)) * 4;
    u32 t2v, t3v, gsv, flv;
    {
      const u32 ln = (mt * 4 + wv) * 15 + lane;
      t2v = lane < 15 ? (ln >> 6) * 131072 + (c2 * 32 + ((ln >> 1) & 31)) * 512 + n2 * 256 + (ln & 1) * 128 : OOB;
      t3v = lane < 16 ? ((mt * 4 + wv) * 16 + lane) * 128 : OOB;
      gsv = ploc >= 126 ? WS_GE + (wv * 4 + g) * 1024 + (bat * 2 + (pi & 1)) * 256 + l15 * 16 : OOB;
      flv = tid == 0 ? WS_FLAGS + mt * 128 + idx16 * 4 : OOB;
    }
    const rsrc_t rw2 = mk(a.w2, 15 * 131072), rw3 = mk((j & 1) ? a.wo : a.wf, 131072);
    asm volatile("s_waitcnt lgkmcnt(0)" : "+s"(epoch), "+v"(t2v), "+v"(t3v), "+v"(gsv), "+v"(flv)::"memory");
    epoch += 1;
    STAMP(1);
    {
      v4u F[2][21];
      v4u Wf[2][2];
      const u32 xr = LB + L_X + l15 * 1056 + g * 16;
      const u32 w1r = LB + L_RING + g * 1024 + q4 * 128 + ((wv ^ ((q4 >> 1) + 2 * (g & 1))) * 32) + p4 * 8;
      prog_wait<1, 0>(hpv);
      STAMP(2);
      static_for<0, 8>([&](auto Q) { F[0][Q] = lds128(xr + Q * 128); F[1][Q] = lds128(xr + Q * 128 + 64); });
      prog_wait<2, 0>(hpv);
      static_for<0, 15>([&](auto I) {
        constexpr int i = I;
        Wf[i & 1][0] = ldstr2(w1r + i * 8192, w1r + i * 8192 + 512);
        Wf[i & 1][1] = ldstr2(w1r + i * 8192 + 4096, w1r + i * 8192 + 4096 + 512);
        if constexpr (i >= 2) {
          F[0][i + 6] = lds128(xr + ((i + 6) >> 3) * 1056 + ((i + 6) & 7) * 128);
          F[1][i + 6] = lds128(xr + ((i + 6) >> 3) * 1056 + ((i + 6) & 7) * 128 + 64);
        }
        fence_sched();
        if constexpr (i >= 1) static_for<0, 4>([&](auto J) { acc[J] = mfma(Wf[(i - 1) & 1][0], F[0][2 * J + i - 1], acc[J]); });
        fence_sched();
        u32 hv = 0, cv = 0;
        if constexpr (i < 14) hv = prog_read(hpv);
        if constexpr (i == 10) cv = prog_read(cpv);
        fence_sched();
        if constexpr (i >= 1) static_for<0, 4>([&](auto J) { acc[J] = mfma(Wf[(i - 1) & 1][1], F[1][2 * J + i - 1], acc[J]); });
        fence_sched();
        if constexpr (i == 2) {
          st_sc1(sv, rws, p2off + lane * 16);
          st_sc1(sv, rws, p2off + 1024 + lane * 16);
          st_sc1(sv, rws, gsv);
        }
        if constexpr (i == 4) dma4(LB + L_JUNK, t2v, rw2, 0);
        if constexpr (i == 9) dma4(LB + L_JUNK, t3v, rw3, 0);
        if constexpr (i == 8) { wait_vm<0>(); prog_write(cp, C_S8); }
        if constexpr (i == 10) {
          prog_ensure<C_S8, 0>(cv, cpv);
          __builtin_amdgcn_raw_buffer_store_b32(epoch, rws, flv, 0, 16);
        }
        if constexpr (i == 12) prog_write(cp, C_S12);
        if constexpr (i < 14) prog_ensure<i + 3, 0>(hv, hpv);
        fence_sched();
        SSTAMP(i + 1);
      });
      static_for<0, 2>([&](auto C) { static_for<0, 4>([&](auto J) { acc[J] = mfma(Wf[0][C], F[C][2 * J + 14], acc[J]); }); });
    }
    STAMP(3);
    st_sc1(pack2(acc[0], acc[1]), rws, p1off + l15 * 64 + g * 16);
    st_sc1(pack2(acc[2], acc[3]), rws, p1off + 1024 + l15 * 64 + g * 16);
    prog_write(cp, C_P1);
    asm volatile("buffer_load_dword %0, %1, %2, 0 offen sc1" : "=v"(fl) : "v"(foff), "s"(rws) : "memory");
    STAMP(4);
    v4u W2[2][15];
    const u32 w2r = LB + L_RING + (8 * g + q4) * 256 + (((2 * wv) ^ (q4 | ((g & 1) << 2))) * 32) + p4 * 8;
    auto rdw2 = [&](auto T) {
      constexpr u32 s = (T + 15) & 15;
      W2[0][T] = ldstr2(w2r + s * 8192, w2r + s * 8192 + 1024);
      W2[1][T] = ldstr2((w2r ^ 32) + s * 8192, (w2r ^ 32) + s * 8192 + 1024);
    };
    auto lds_done = [&]() { asm volatile("s_waitcnt lgkmcnt(0)" ::: "memory"); };
    prog_wait<21, 0>(hpv);
    static_for<0, 5>(rdw2);
    lds_done(); prog_write(cp, C_W2A);
    v4u hp[4]; u32 hh[4];
    u32 rho, rhoh, h1off, h1offh;
    {
      const u32 mm = lane >> 2;
      rho = 8 + 4 * mm + 2 * (wv & 1);
      h1off = mloc * 64 + rho - 8 < 500 ? WS_P1 + c2 * 65536 + mt * 4096 + wv * 1024 + lane * 16 : OOB;
      rho = LB + L_X + rho * 64 + (rho >> 2) * 32 + (wv >> 1) * 32 + p4 * 8;
      const u32 nx = lane >> 5, mh = (lane >> 4) & 1, dw = lane & 3;
      rhoh = 72 * nx + 4 * mh + 2 * (wv & 1) + (dw >> 1);
      const int hrow = (int)(mloc * 64 + rhoh) - 8;
      h1offh = (hrow >= 0 && hrow < 500) ? WS_P1 + c2 * 65536 + (mt + 2 * nx - 1) * 4096 + wv * 1024 + (nx ? 0 : 896) + (lane & 31) * 4 : OOB;
      rhoh = LB + L_X + rhoh * 64 + (rhoh >> 2) * 32 + (wv >> 1) * 32 + q4 * 8 + (dw & 1) * 4;
    }
    asm volatile("s_waitcnt vmcnt(0)" : "+v"(fl)::"memory");
    for (u32 spins = 0; !__all(fl == epoch) && spins < SPIN_CAP; ++spins)
      asm volatile("buffer_load_dword %0, %1, %2, 0 offen sc1\n\ts_waitcnt vmcnt(0)" : "=v"(fl) : "v"(foff), "s"(rws) : "memory");
    STAMP(5);
    delay(DLY1);
    auto h1_load = [&]() {
      static_for<0, 4>([&](auto K) { hp[K] = __builtin_amdgcn_raw_buffer_load_b128(rws, h1off, K * 524288, 16); });
      static_for<0, 4>([&](auto K) { hh[K] = __builtin_amdgcn_raw_buffer_load_b32(rws, h1offh, K * 524288, 16); });
      mem_order();
    };
    h1_load();
    prog_wait<26, 0>(hpv);
    static_for<5, 10>(rdw2);
    lds_done(); prog_write(cp, C_W2B);
    prog_wait<31, 0>(hpv);
    static_for<10, 15>(rdw2);
    lds_done(); prog_write(cp, C_W2C);
    STAMP(6);
    for (u32 spins = 0;; ++spins) {
      const u32 mx = max(max(max(max4(hp[0]), max4(hp[1])), max(max4(hp[2]), max4(hp[3]))), max(max(hh[0], hh[1]), max(hh[2], hh[3])));
      if (__all(mx != SENT) || spins >= SPIN_CAP) break;
      h1_load();
    }
    STAMP(7);
    {
      float v[8];
      static_for<0, 4>([&](auto D) {
        v[2 * D] = gelu((lo16(hp[0][+D]) + lo16(hp[1][+D])) + (lo16(hp[2][+D]) + lo16(hp[3][+D])));
        v[2 * D + 1] = gelu((hi16(hp[0][+D]) + hi16(hp[1][+D])) + (hi16(hp[2][+D]) + hi16(hp[3][+D])));
      });
      const float u0 = gelu((lo16(hh[0]) + lo16(hh[1])) + (lo16(hh[2]) + lo16(hh[3])));
      const float u1 = gelu((hi16(hh[0]) + hi16(hh[1])) + (hi16(hh[2]) + hi16(hh[3])));
      *(LAS v2u*)(size_t)(rho) = v2u{pk(v[0], v[1]), pk(v[2], v[3])};
      *(LAS v2u*)(size_t)(rho + 64) = v2u{pk(v[4], v[5]), pk(v[6], v[7])};
      *(LAS u32*)(size_t)(rhoh) = pk(u0, u1);
    }
    prog_write(cp, C_A);
    STAMP(8);
    prog_wait<C_A, 0>(cpv);
    STAMP(9);
    {
      static_for<0, 4>([&](auto J) { acc[J] = f32x4{0, 0, 0, 0}; });
      const u32 ar = LB + L_X + l15 * 288 + g * 16;
      v4u A[17];
      static_for<0, 17>([&](auto Q) { A[Q] = lds128(ar + ((Q + 1) >> 2) * 288 + ((Q + 1) & 3) * 64); });
      static_for<0, 15>([&](auto T) {
        acc[0] = mfma(W2[0][T], A[T], acc[0]);
        acc[2] = mfma(W2[1][T], A[T], acc[2]);
        acc[1] = mfma(W2[0][T], A[T + 2], acc[1]);
        acc[3] = mfma(W2[1][T], A[T + 2], acc[3]);
      });
      st_sc1(pack2(acc[0], acc[1]), rws, p2off + l15 * 64 + g * 16);
      st_sc1(pack2(acc[2], acc[3]), rws, p2off + 1024 + l15 * 64 + g * 16);
    }
    STAMP(10);
    if (!p3ok) return;
    v4u WF[8][4];
    const u32 wr = LB + L_RING + (4 * g + q4) * 512 + (((4 * wv) ^ ((4 * g + q4) & 7)) * 32) + p4 * 8;
    auto rdwf = [&](auto K) { static_for<0, 4>([&](auto T) { WF[K][T] = ldstr2((wr ^ (T * 32)) + K * 16384, (wr ^ (T * 32)) + K * 16384 + 8192); }); };
    prog_wait<41, 0>(hpv);
    static_for<0, 4>(rdwf);
    lds_done(); prog_write(cp, C_WFA);
    prog_wait<47, 0>(hpv);
    static_for<4, 8>(rdwf);
    lds_done(); prog_write(cp, C_WFB);
    STAMP(11);
    delay(DLY2);
    u32 fp[8];
    v4u ge[4];
    {
      const u32 h2off = WS_P2 + mt * 16384 + (tid >> 4) * 1024 + idx16 * 64 + l15 * 4;
      const u32 geoff = WS_GE + (bat * 8 + (lane & 7)) * 1024 + wv * 256 + g * 16;
      for (u32 spins = 0;; ++spins) {
        mem_order();
        static_for<0, 8>([&](auto K) { fp[K] = __builtin_amdgcn_raw_buffer_load_b32(rws, h2off, K * 262144, 16); });
        static_for<0, 4>([&](auto T) { ge[T] = __builtin_amdgcn_raw_buffer_load_b128(rws, geoff + T * 64, 0, 16); });
        u32 mx = max(max(max4(ge[0]), max4(ge[1])), max(max4(ge[2]), max4(ge[3])));
        static_for<0, 8>([&](auto K) { mx = max(mx, fp[K]); });
        if (__all(mx != SENT) || spins >= SPIN_CAP) break;
      }
      STAMP(12);
      float s0 = 0.f, s1 = 0.f;
      static_for<0, 8>([&](auto K) { s0 += lo16(fp[K]); s1 += hi16(fp[K]); });
      const u32 pos = (tid >> 5) * 32 + q4 * 8 + ((tid >> 4) & 1) * 4 + (tid & 1) * 2;
      *(LAS u32*)(size_t)(LB + L_F + ((tid >> 1) & 1) * 576 + pos * 2) = pk(gelu(s0), gelu(s1));
    }
    prog_write(cp, C_F);
    prog_wait<C_F, 0>(cpv);
    {
      f32x4 d[4];
      static_for<0, 4>([&](auto T) { d[T] = __builtin_bit_cast(f32x4, ge[T]); });
      const u32 fr = LB + L_F + (l15 >> 3) * 576 + g * 16;
      v4u b[8];
      static_for<0, 8>([&](auto K) { b[K] = lds128(fr + K * 64); });
      static_for<0, 2>([&](auto H) {
        static_for<0, 8>([&](auto K) { d[2 * H] = mfma(WF[K][2 * H], b[K], d[2 * H]); d[2 * H + 1] = mfma(WF[K][2 * H + 1], b[K], d[2 * H + 1]); });
      });
      static_for<0, 2>([&](auto H) {
        const f32x4 d0 = d[2 * H], d1 = d[2 * H + 1];
        v4u jf = {pk(tanh_(d0.x), tanh_(d0.y)), pk(tanh_(d0.z), tanh_(d0.w)), pk(tanh_(d1.x), tanh_(d1.y)), pk(tanh_(d1.z), tanh_(d1.w))};
        *(LAS v4u*)(size_t)(LB + L_J + (2 * wv + H) * 1024 + lane * 16) = jf;
      });
    }
    STAMP(13);
  }
  bar();
  {
    asm volatile("" : "+v"(lane));
    const u32 l15 = lane & 15, g = lane >> 4, q4 = l15 >> 2, p4 = lane & 3;
    const u32 wr0 = LB + L_RING + (4 * g + q4) * 512 + (((2 * wv) ^ ((4 * g + q4) & 7)) * 32) + p4 * 8;
    const u32 wr1 = wr0 ^ 32;
    f32x4 d0 = {0, 0, 0, 0}, d1 = {0, 0, 0, 0};
    static_for<0, 8>([&](auto K) {
      const v4u b = lds128(LB + L_J + K * 1024 + lane * 16);
      d0 = mfma(ldstr2(wr0 + K * 16384, wr0 + K * 16384 + 8192), b, d0);
      d1 = mfma(ldstr2(wr1 + K * 16384, wr1 + K * 16384 + 8192), b, d1);
    });
    u16* op = a.out + ((bat * 250 + 2 * ploc) * 8 + l15) * 256 + wv * 32 + 4 * g;
    const rsrc_t ro = mk(a.out, 2048000);
    __builtin_amdgcn_raw_buffer_store_b64(v2u{pk(d0.x, d0.y), pk(d0.z, d0.w)}, ro, (u32)(op - a.out) * 2, 0, 16);
    __builtin_amdgcn_raw_buffer_store_b64(v2u{pk(d1.x, d1.y), pk(d1.z, d1.w)}, ro, (u32)(op - a.out) * 2 + 32, 0, 16);
  }
}

extern "C" void kernel_launch(void* const* d_in, const int* in_sizes, int n_in, void* d_out, int out_size, void* d_ws, size_t ws_size,
                              hipStream_t stream) {
  Args a{(const u16*)d_in[0], (const u16*)d_in[1], (const u16*)d_in[2], (const u16*)d_in[3], (const u16*)d_in[4],
         (const u16*)d_in[5], (const u16*)d_in[6], (u16*)d_out, (unsigned char*)d_ws};
  rnnt_fused<<<256, 512, 0, stream>>>(a);
}
```

```cpp
#define HPROBE 2
#define CODE_LINES 88
#include <hip/hip_runtime.h>
#include <hip/hip_bf16.h>
#include <type_traits>

typedef unsigned u32;
typedef unsigned short u16;
typedef u32 v4u __attribute__((ext_vector_type(4)));
typedef u32 v2u __attribute__((ext_vector_type(2)));
typedef float f32x4 __attribute__((ext_vector_type(4)));
typedef float f32x2 __attribute__((ext_vector_type(2)));
typedef short s16x4 __attribute__((ext_vector_type(4)));
typedef __bf16 bf16x8 __attribute__((ext_vector_type(8)));
typedef __bf16 bf16x2 __attribute__((ext_vector_type(2)));
typedef __amdgpu_buffer_rsrc_t rsrc_t;
#define GAS __attribute__((address_space(1)))
#define LAS __attribute__((address_space(3)))
#define DI __device__ __forceinline__

constexpr u32 WS_EPOCH = 0;
constexpr u32 WS_FLAGS = 4096;
constexpr u32 WS_GE = 65536;
constexpr u32 WS_P1 = 1u << 20;
constexpr u32 WS_P2 = 3u << 20;
constexpr u32 WS_BYTES = 5u << 20;
constexpr u32 OOB = 0x80000000u;
constexpr u32 SENT = 0xFFFFFFFFu;
constexpr u32 SPIN_CAP = 100000;
constexpr u32 LSPIN_CAP = 4000000;
constexpr u32 L_RING = 0;
constexpr u32 L_X = 131072;
constexpr u32 L_F = L_X + 19008;
constexpr u32 L_J = L_F + 1152;
constexpr u32 L_JUNK = L_J + 8192;
constexpr u32 L_HP = L_JUNK + 1024;
constexpr u32 L_BP = L_HP + 16;
constexpr u32 L_CP = L_BP + 16;
constexpr u32 L_SP = L_CP + 32;
constexpr u32 L_TS = L_HP + 256;
constexpr u32 LDS_BYTES = L_TS + 1344;
static_assert(LDS_BYTES <= 163840, "lds");
#define LOOK 4
#define SOFF 0
#define CPRIO 1
#define OAUX 0
#define FCHK 12
#define SPRIO 3
#define L0 1
#define TDLY 0
#define SDLY 4
#define BDLY 0
#define DLY1 0
#define DLY2 0

template <int I, int N, class F> DI void static_for(F&& f) {
  if constexpr (I < N) { f(std::integral_constant<int, I>{}); static_for<I + 1, N>(f); }
}
template <int N> DI void wait_vm() { asm volatile("s_waitcnt vmcnt(%0)" ::"i"(N) : "memory"); }
DI void bar() { asm volatile("s_waitcnt lgkmcnt(0)\n\ts_barrier" ::: "memory"); }
DI void fence_sched() { __builtin_amdgcn_sched_barrier(0); }
DI void mem_order() { asm volatile("" ::: "memory"); }
DI u32 rfl(u32 x) { return __builtin_amdgcn_readfirstlane(x); }
#define WAUX ""
DI void dma16(u32 lds, u32 voff, rsrc_t rs, u32 soff) {
  asm volatile("s_mov_b32 m0, %0\n\ts_nop 0\n\tbuffer_load_dwordx4 %1, %2, %3 offen" WAUX " lds" ::"s"(lds), "v"(voff), "s"(rs), "s"(soff) : "memory");
}
DI void dma4(u32 lds, u32 voff, rsrc_t rs, u32 soff) {
  asm volatile("s_mov_b32 m0, %0\n\ts_nop 0\n\tbuffer_load_dword %1, %2, %3 offen lds" ::"s"(lds), "v"(voff), "s"(rs), "s"(soff) : "memory");
}
DI rsrc_t mk(const void* p, u32 bytes) { return __builtin_amdgcn_make_buffer_rsrc((void*)p, 0, (int)bytes, 0x00020000); }
DI v4u lds128(u32 a) { return *(const LAS v4u*)(size_t)a; }
DI v2u ldstr(u32 a) { return __builtin_bit_cast(v2u, __builtin_amdgcn_ds_read_tr16_b64_v4i16((LAS s16x4*)(size_t)a)); }
DI v4u ldstr2(u32 a0, u32 a1) { v2u x = ldstr(a0), y = ldstr(a1); return v4u{x.x, x.y, y.x, y.y}; }
DI f32x4 mfma(v4u a, v4u b, f32x4 c) {
  return __builtin_amdgcn_mfma_f32_16x16x32_bf16(__builtin_bit_cast(bf16x8, a), __builtin_bit_cast(bf16x8, b), c, 0, 0, 0);
}
DI void mfma_ip(f32x4& c, v4u a, v4u b) { asm volatile("v_mfma_f32_16x16x32_bf16 %0, %1, %2, %0" : "+v"(c) : "v"(a), "v"(b)); }
DI u32 pk(float a, float b) { f32x2 f = {a, b}; return __builtin_bit_cast(u32, __builtin_convertvector(f, bf16x2)); }
DI float lo16(u32 x) { return __builtin_bit_cast(float, x << 16); }
DI float hi16(u32 x) { return __builtin_bit_cast(float, x & 0xFFFF0000u); }
DI float gelu(float x) {
  const float c0 = -2.0f * 0.7978845608028654f * 1.4426950408889634f, c1 = c0 * 0.044715f;
  float e = __builtin_amdgcn_exp2f(x * __builtin_fmaf(x * x, c1, c0));
  return x * __builtin_amdgcn_rcpf(1.0f + e);
}
DI float tanh_(float x) {
  float e = __builtin_amdgcn_exp2f(x * 2.8853900817779268f);
  return __builtin_fmaf(-2.0f, __builtin_amdgcn_rcpf(1.0f + e), 1.0f);
}
DI u32 nosent(u32 x) { return min(x, SENT - 1); }
DI v4u pack2(f32x4 a, f32x4 b) { return v4u{nosent(pk(a.x, a.y)), nosent(pk(a.z, a.w)), nosent(pk(b.x, b.y)), nosent(pk(b.z, b.w))}; }
DI void st_sc1(v4u v, rsrc_t rs, u32 off) { __builtin_amdgcn_raw_buffer_store_b128(v, rs, off, 0, 16); }
DI u32 max4(v4u v) { return max(max(v.x, v.y), max(v.z, v.w)); }
DI u32 min4(v4u v) { return min(min(v.x, v.y), min(v.z, v.w)); }
DI u32 prog_read(u32 va) { u32 v = *(const volatile LAS u32*)(size_t)va; mem_order(); return v; }
DI void prog_write(u32 a, u32 v) {
  asm volatile("s_mov_b64 exec, 1\n\tds_write_b32 %0, %1\n\ts_mov_b64 exec, -1" ::"v"(a), "v"(v) : "memory");
}
template <u32 NEED, int SLEEP> DI void prog_ensure(u32 hv, u32 va) {
  u32 cnt;
  asm volatile("v_cmp_gt_u32 vcc, %3, %0\n\ts_cbranch_vccnz Ls%=\n"
               "Ld%=:\n\t.subsection 1\n"
               "Ls%=:\n\ts_mov_b32 %1, %5\n"
               "Lr%=:\n\ts_sleep %4\n\tds_read_b32 %0, %2\n\ts_waitcnt lgkmcnt(0)\n\tv_cmp_gt_u32 vcc, %3, %0\n\ts_cbranch_vccz Ld%=\n\t"
               "s_sub_u32 %1, %1, 1\n\ts_cbranch_scc0 Lr%=\n\ts_branch Ld%=\n\t.subsection 0"
               : "+v"(hv), "=&s"(cnt) : "v"(va), "i"(NEED), "i"(SLEEP), "i"(LSPIN_CAP) : "memory", "scc", "vcc");
}
template <u32 NEED, int SLEEP> DI void prog_spin(u32 va) {
  u32 cnt, hv;
  asm volatile("s_mov_b32 %1, %5\n"
               "Lr%=:\n\tds_read_b32 %0, %2\n\ts_waitcnt lgkmcnt(0)\n\tv_cmp_gt_u32 vcc, %3, %0\n\ts_cbranch_vccz Ld%=\n\t"
               "s_sleep %4\n\ts_sub_u32 %1, %1, 1\n\ts_cbranch_scc0 Lr%=\n"
               "Ld%=:"
               : "=&v"(hv), "=&s"(cnt) : "v"(va), "i"(NEED), "i"(SLEEP), "i"(LSPIN_CAP) : "memory", "scc", "vcc");
}
template <u32 NEED, int SLEEP> DI void prog_wait(u32 va) { prog_ensure<NEED, SLEEP>(prog_read(va), va); }
#define RT0
#define RT1
#define RTC
template <int N, u32 STEP, u32 SMASK> DI void rounds(u32& m, u32& so, int& pv, int& fl, int& need, u32 va, u32 vb, rsrc_t rs, u32 hp, u32 cpv) {
  u32 cnt, spin, cv;
  asm volatile("s_mov_b32 %4, %14\n"
               "L%=:\n\t" RT0 "ds_read_b32 %6, %12\n\ts_mov_b32 m0, %0\n\ts_add_u32 %0, %0, 0x1000\n\tbuffer_load_dwordx4 %8, %10, %1 offen" WAUX " lds\n\t"
               "s_mov_b32 m0, %0\n\ts_add_u32 %0, %0, 0x1000\n\tbuffer_load_dwordx4 %9, %10, %1 offen" WAUX " lds\n\t"
               "s_and_b32 %0, %0, 0x1ffff\n\ts_add_u32 %1, %1, %13\n\ts_and_b32 %1, %1, %15\n\tv_add_u32 %2, 1, %2\n\ts_add_i32 %3, %3, 1\n\tv_max_i32 %7, %7, %2\n\t"
               "s_waitcnt lgkmcnt(0)\n\tv_cmp_gt_i32 vcc, %3, %6\n\t"
               "s_mov_b64 exec, 1\n\ts_waitcnt vmcnt(%16)\n\tds_write_b32 %11, %7\n\t" RT1 "s_mov_b64 exec, -1\n\ts_cbranch_vccnz Ls%=\n"
               "Lb%=:\n\ts_sub_u32 %4, %4, 1\n\ts_cbranch_scc0 L%=\n\t.subsection 1\n"
               "Ls%=:\n\tv_add_u32 %7, %17, %2\n\ts_mov_b64 exec, 1\n\ts_waitcnt vmcnt(0)\n\tds_write_b32 %11, %7\n\ts_mov_b64 exec, -1\n\ts_mov_b32 %5, %18\n"
               "Lr%=:\n\tds_read_b32 %6, %12\n\ts_waitcnt lgkmcnt(0)\n\tv_cmp_gt_i32 vcc, %3, %6\n\ts_cbranch_vccz Lb%=\n\t"
               "s_sleep 1\n\ts_sub_u32 %5, %5, 1\n\ts_cbranch_scc0 Lr%=\n\ts_branch Lb%=\n\t.subsection 0"
               : "+s"(m), "+s"(so), "+v"(pv), "+s"(need), "=&s"(cnt), "=&s"(spin), "=&v"(cv), "+v"(fl)
               : "v"(va), "v"(vb), "s"(rs), "v"(hp), "v"(cpv), "i"(STEP), "i"(N - 1), "i"(SMASK), "i"(2 * LOOK), "i"(LOOK), "i"(LSPIN_CAP)
               : "memory", "scc", "vcc" RTC);
}
template <int N, u32 STRIDE> DI void touch(const void* p, u32 off) {
  u32 d0, d1, cnt, o2 = off + 64;
  asm volatile("s_mov_b32 %2, %6\n"
               "L%=:\n\ts_load_dword %0, %5, %3\n\t"
               "s_load_dword %1, %5, %4\n\ts_add_u32 %4, %4, %7\n\t"
               "s_add_u32 %3, %3, %7\n\t"
               "s_sub_u32 %2, %2, 1\n\ts_cbranch_scc0 L%=\n\ts_waitcnt lgkmcnt(0)"
               : "=&s"(d0), "=&s"(d1), "=&s"(cnt), "+s"(off), "+s"(o2) : "s"(p), "i"(N - 1), "i"(STRIDE) : "memory", "scc");
}
DI float asf(u32 x) { return __builtin_bit_cast(float, x); }
DI u32 opq(u32 x) { asm volatile("" : "+v"(x)); return x; }
DI f32x2 red4(v4u d) {
  const auto r0 = __builtin_amdgcn_permlane32_swap(d.x, d.z, false, false), r1 = __builtin_amdgcn_permlane32_swap(d.y, d.w, false, false);
  const float s0 = lo16(r0[0]) + lo16(r0[1]), s1 = hi16(r0[0]) + hi16(r0[1]), s2 = lo16(r1[0]) + lo16(r1[1]), s3 = hi16(r1[0]) + hi16(r1[1]);
  const auto q0 = __builtin_amdgcn_permlane16_swap(__builtin_bit_cast(u32, s0), __builtin_bit_cast(u32, s2), false, false);
  const auto q1 = __builtin_amdgcn_permlane16_swap(__builtin_bit_cast(u32, s1), __builtin_bit_cast(u32, s3), false, false);
  return f32x2{asf(q0[0]) + asf(q0[1]), asf(q1[0]) + asf(q1[1])};
}
DI void delay(int n) { for (int i = 0; i < n; ++i) __builtin_amdgcn_s_sleep(1); }

#define STAMP(i) do { if (i == HPROBE) { asm volatile("s_memrealtime %0\n\ts_waitcnt lgkmcnt(0)" : "=s"(hp1)::"memory"); } } while (0)
#define SSTAMP(i)

struct Args { const u16 *x, *w1, *w2, *gp, *wf, *wg, *wo; u16* out; unsigned char* ws; };
typedef unsigned long long u64;
typedef u64 v8q __attribute__((ext_vector_type(8)));

constexpr u32 S_A = 1, S_F = 2, S_J = 3;

__global__ void __launch_bounds__(768, 3) rnnt_fused(Args unused) {
  __shared__ __attribute__((aligned(16))) unsigned char smem[LDS_BYTES];
  const u32 LB = (u32)(size_t)(LAS unsigned char*)smem;
  u32 tid = threadIdx.x;
  v8q ka; u64 kws;
  u64 hp0, hp1 = 0;
  asm volatile("s_memrealtime %0\n\ts_waitcnt lgkmcnt(0)" : "=s"(hp0)::"memory");
  u64 pc;
  asm volatile("s_load_dwordx16 %0, %3, 0x0\n\ts_load_dwordx2 %1, %3, 0x40\n\ts_getpc_b64 %2" : "=&s"(ka), "=&s"(kws), "=&s"(pc) : "s"(__builtin_amdgcn_kernarg_segment_ptr()) : "memory");
  asm volatile("s_mov_b32 m0, %1\n\ts_nop 0\n\tglobal_load_lds_dword %0, %2" :: "v"(min(((blockIdx.x >> 3) * 1536 + tid * 2) & 0x3f80, ((u32)CODE_LINES - 1) << 7)), "s"(LB + L_JUNK), "s"(pc) : "memory");
  const u32 wq = rfl(tid >> 6), w4 = wq & 3, wv = wq & 7;
  const u32 bid = blockIdx.x, x8 = bid & 7, j = bid >> 3, mt = j >> 1, mloc = mt & 7, bat = mt >> 3;
  const u32 k1 = x8 >> 1, n1 = 2 * (x8 & 1) + (j & 1);
  u32 lane = tid & 63;
  const u32 wbase = k1 * 32768 + n1 * 128;
  u32 w1v = 0, m = 0;
#define KWAIT(...) asm volatile("s_waitcnt lgkmcnt(0)" : "+s"(ka), "+s"(kws), __VA_ARGS__::"memory")
  {
    const bool isS = wq < 4;
    u32 bS = wbase, bB = mloc * 65536 - 3584 + k1 * 128, mS = LB + L_RING + w4 * 1024, mB = LB + L_X + w4 * 1056, nB = min(mloc * 128 + 137, 1000u) * 512, xo = bat * 512000;
    asm volatile("" : "+s"(bS), "+s"(bB), "+s"(mS), "+s"(mB), "+s"(nB), "+s"(xo));
    const u32 sw = (((tid >> 3) & 2) | ((tid >> 4) & 4)) & (isS ? 6 : 0);
    w1v = ((tid >> 3) & 31) * 512 + (isS ? bS : bB) + (((tid & 7) ^ sw) * 16);
    u32 va = w1v, vb = w1v + 16384, cnt = isS ? L0 - 1 : 2, dm = isS ? 4096 : 4224, dv = isS ? 131072 : 32768, nr = isS ? 15 * 131072 : nB;
    m = isS ? mS : mB;
    KWAIT("+v"(va), "+v"(vb), "+s"(cnt), "+s"(dm), "+s"(dv), "+s"(nr), "+s"(m));
    if (__builtin_expect(wq < 8, 1)) {
      u64 pS = ka[1], pB = ka[0] + xo;
      asm volatile("" : "+s"(pS), "+s"(pB));
      const rsrc_t rs = mk((const void*)(isS ? pS : pB), nr);
      asm volatile("L%=:\n\ts_mov_b32 m0, %0\n\ts_add_u32 %0, %0, %5\n\tbuffer_load_dwordx4 %1, %4, 0 offen" WAUX " lds\n\t"
                   "s_mov_b32 m0, %0\n\ts_add_u32 %0, %0, %5\n\tbuffer_load_dwordx4 %2, %4, 0 offen" WAUX " lds\n\t"
                   "v_add_u32 %1, %6, %1\n\tv_add_u32 %2, %6, %2\n\ts_sub_u32 %3, %3, 1\n\ts_cbranch_scc0 L%=\n\ts_barrier"
                   : "+s"(m), "+v"(va), "+v"(vb), "+s"(cnt) : "s"(rs), "s"(dm), "s"(dv) : "memory", "scc");
    } else {
      *(volatile LAS u32*)(size_t)(LB + L_HP + lane * 4) = 0;
      const u32 p1s = WS_P1 + k1 * 524288 + (2 * n1 + (w4 >> 1)) * 65536 + mt * 4096 + (w4 & 1) * 2048 + lane * 16;
      delay(TDLY);
      dma4(LB + L_JUNK, lane < 15 - L0 ? (L0 + lane) * 131072 + wbase + (mt * 4 + w4) * 512 : OOB, mk((const void*)ka[1], 15 * 131072), 0);
      bar();
      const v4u sv = {SENT, SENT, SENT, SENT};
      st_sc1(sv, mk((const void*)kws, WS_BYTES), p1s);
      st_sc1(sv, mk((const void*)kws, WS_BYTES), p1s + 1024);
    }
  }
  Args a{(const u16*)ka[0], (const u16*)ka[1], (const u16*)ka[2], (const u16*)ka[3], (const u16*)ka[4], (const u16*)ka[5], (const u16*)ka[6], (u16*)ka[7], (unsigned char*)kws};
  const u32 c2 = x8, n2 = j & 1;
  const u32 idx16 = x8 * 2 + (j & 1), pi = mt * 16 + idx16, ploc = pi & 127;
  const bool p3ok = ploc < 125;
  STAMP(1);

  if (__builtin_expect(wq < 4, 1)) {
    __builtin_amdgcn_s_setprio(SPRIO);
    u32 so = L0 * 131072;
    int pv = L0 - LOOK, fl = 0, need = L0 - 15;
    delay(SDLY);
    const u32 hp = LB + L_HP + w4 * 4, cpv = LB + L_CP + (lane & 7) * 4;
    rounds<15 - L0, 131072, ~0u>(m, so, pv, fl, need, w1v, w1v + 16384, mk(a.w1, 15 * 131072), hp, cpv);
    STAMP(2);
    lane = opq(lane);
    const u32 g = lane >> 4;
    const rsrc_t rw2 = mk(a.w2, 15 * 131072);
    const u32 w2v = (c2 * 32 + w4 * 4 + g) * 512 + n2 * 256 + ((((lane >> 1) & 7) ^ (g | (((w4 >> 1) & 1) << 2))) * 32) + (lane & 1) * 16;
    so = 0;
    rounds<15, 131072, ~0u>(m, so, pv, fl, need, w2v, w2v + 8192, rw2, hp, cpv);
    STAMP(3);
    const rsrc_t rws = mk(a.ws, WS_BYTES);
    const u32 l15 = lane & 15;
    if (!p3ok) {
      if (ploc < 126) { wait_vm<0>(); prog_write(hp, 30); }
      if (ploc >= 126) {
        const u32 qd = bat * 2 + (pi & 1), et = qd * 4 + w4;
        const rsrc_t rwg = mk(a.wg, 131072);
        const u32 wgv = w1v - k1 * 32768 - n1 * 128 + qd * 128;
        so = 0;
        rounds<3, 32768, ~0u>(m, so, pv, fl, need, wgv, wgv + 16384, rwg, hp, cpv);
        need = 0;
        rounds<1, 32768, ~0u>(m, so, pv, fl, need, wgv, wgv + 16384, rwg, hp, cpv);
        v4u b[8];
        static_for<0, 8>([&](auto K) { b[K] = *(const GAS v4u*)(a.gp + l15 * 256 + K * 32 + 8 * g); });
        wait_vm<8>();
        prog_write(hp, 34);
        prog_spin<34, 0>(LB + L_HP + (lane & 3) * 4);
        const u32 q4 = l15 >> 2;
        const u32 gr = LB + L_RING + g * 1024 + q4 * 128 + ((w4 ^ ((q4 >> 1) + 2 * (g & 1))) * 32) + (lane & 3) * 8;
        v4u wa[8];
        static_for<0, 8>([&](auto K) { const u32 ad = gr + (((K >> 1) + 14) & 15) * 8192 + (K & 1) * 4096; wa[K] = ldstr2(ad, ad + 512); });
        fence_sched();
        f32x4 d = {0, 0, 0, 0};
        static_for<0, 8>([&](auto K) { d = mfma(wa[K], b[K], d); });
        v4u o = __builtin_bit_cast(v4u, d);
        prog_spin<2, 1>(LB + L_BP + (lane & 3) * 4);
        st_sc1(v4u{nosent(o.x), nosent(o.y), nosent(o.z), nosent(o.w)}, rws, WS_GE + l15 * 1024 + et * 64 + g * 16);
      } else if (pi == 125 && w4 == 0) {
        u32 epoch;
        asm volatile("s_load_dword %0, %1, 0x0\n\ts_waitcnt lgkmcnt(0)" : "=s"(epoch) : "s"(a.ws) : "memory");
        epoch += 1;
        for (u32 spins = 0; spins < SPIN_CAP; ++spins) {
          v4u f4;
          asm volatile("buffer_load_dwordx4 %0, %1, %2, 0 offen sc1\n\ts_waitcnt vmcnt(0)" : "=v"(f4) : "v"(WS_FLAGS + (lane >> 2) * 128 + (lane & 3) * 16), "s"(rws) : "memory");
          if (__all(f4.x == epoch && f4.y == epoch && f4.z == epoch && f4.w == epoch)) break;
        }
        if (lane == 0) __hip_atomic_store((GAS u32*)(a.ws + WS_EPOCH), epoch, __ATOMIC_RELAXED, __HIP_MEMORY_SCOPE_AGENT);
      }
      return;
    }
    const u32 r2 = 2 * w4 + (lane >> 5);
    const u32 wfv = r2 * 512 + ((((lane >> 1) & 15) ^ r2) * 32) + (lane & 1) * 16;
    const rsrc_t rwf = mk(a.wf, 131072), rwo = mk(a.wo, 131072);
    so = 14 * 8192;
    rounds<16, 8192, 0x1ffff>(m, so, pv, fl, need, wfv, wfv + 4096, rwf, hp, cpv);
    STAMP(4);
    rounds<15, 8192, 0x1ffff>(m, so, pv, fl, need, wfv, wfv + 4096, rwo, hp, cpv);
    need = 0;
    rounds<1, 8192, 0x1ffff>(m, so, pv, fl, need, wfv, wfv + 4096, rwo, hp, cpv);
    STAMP(5);
    wait_vm<0>();
    prog_write(hp, 62);
    STAMP(6);
  } else {
    const rsrc_t rws = mk(a.ws, WS_BYTES);
    const u32 p1off = WS_P1 + k1 * 524288 + (2 * n1 + (w4 >> 1)) * 65536 + mt * 4096 + (w4 & 1) * 2048;
    const u32 p2off = WS_P2 + c2 * 262144 + mt * 16384 + n2 * 8192;
    const u32 cp = LB + L_CP + wv * 4;
    if (wv >= 4) {
      u32 epoch;
      asm volatile("s_load_dword %0, %1, 0x0\n\ts_waitcnt lgkmcnt(0)" : "=s"(epoch) : "s"(a.ws) : "memory");
      const v4u sv = {SENT, SENT, SENT, SENT};
      const u32 l15 = lane & 15, g = lane >> 4;
      const u32 bp = LB + L_BP + w4 * 4;
      prog_write(cp, 15);
      wait_vm<0>();
      prog_write(bp, 1);
      STAMP(2);
      delay(BDLY);
      st_sc1(sv, rws, p2off + w4 * 2048 + lane * 16);
      st_sc1(sv, rws, p2off + w4 * 2048 + 1024 + lane * 16);
      st_sc1(sv, rws, ploc >= 126 ? WS_GE + (w4 * 4 + g) * 1024 + (bat * 2 + (pi & 1)) * 256 + l15 * 16 : OOB);
      touch<16, 512>(a.w2, min(mt, 14u) * 131072 + (c2 * 32 + (w4 >> 1) * 16) * 512 + n2 * 256 + (w4 & 1) * 128);
      wait_vm<0>();
      epoch += 1;
      prog_write(bp, w4 == 0 ? 2 : 3);
      STAMP(3);
      if (w4 == 0) {
        prog_spin<2, 1>(LB + L_BP + (lane & 3) * 4);
        prog_spin<3, 1>(LB + L_CP + (lane & 7) * 4);
        __builtin_amdgcn_raw_buffer_store_b32(epoch, rws, lane == 0 ? WS_FLAGS + mt * 128 + idx16 * 4 : OOB, 0, 16);
      }
      touch<16, 128>((j & 1) ? a.wo : a.wf, (mt * 4 + w4) * 2048);
      if (w4 == 0) {
        const u32 fline = lane < 48 ? min(max(mt + g, 1u) - 1, 15u) : 7 + (lane & 8);
        const u32 foff = WS_FLAGS + fline * 128 + (lane < 48 ? l15 : 8 + (lane & 7)) * 4;
        prog_spin<FCHK, 2>(LB + L_CP + (lane & 7) * 4);
        for (u32 spins = 0; spins < SPIN_CAP; ++spins) {
          u32 fl;
          asm volatile("buffer_load_dword %0, %1, %2, 0 offen sc1\n\ts_waitcnt vmcnt(0)" : "=v"(fl) : "v"(foff), "s"(rws) : "memory");
          if (__all(fl == epoch)) break;
          __builtin_amdgcn_s_sleep(4);
        }
        prog_write(bp, 3);
      }
    } else {
      __builtin_amdgcn_s_setprio(CPRIO);
      const u32 l15 = lane & 15, g = lane >> 4, q4 = l15 >> 2, p4 = lane & 3;
      const u32 hpv = LB + L_HP + p4 * 4;
      f32x4 acc[4] = {{0, 0, 0, 0}, {0, 0, 0, 0}, {0, 0, 0, 0}, {0, 0, 0, 0}};
      v4u F[2][21];
      v2u Wf[2][4];
      const u32 xr = opq(LB + L_X + l15 * 1056 + g * 16);
      const u32 w1r = LB + L_RING + g * 1024 + q4 * 128 + ((wv ^ ((q4 >> 1) + 2 * (g & 1))) * 32) + p4 * 8;
      const u32 p1v = opq(p1off + l15 * 64 + g * 16);
      const u32 w1r2 = opq(w1r + 65536);
      prog_spin<1, 0>(hpv + 16);
      STAMP(2);
      static_for<0, 4>([&](auto Q) { F[0][2 * Q] = lds128(xr + 2 * Q * 128); F[1][2 * Q] = lds128(xr + 2 * Q * 128 + 64); });
      prog_spin<1, 0>(hpv);
      static_for<0, 4>([&](auto T) { Wf[0][T] = ldstr(w1r + (T >> 1) * 4096 + (T & 1) * 512); });
      static_for<0, 4>([&](auto Q) { F[0][2 * Q + 1] = lds128(xr + (2 * Q + 1) * 128); F[1][2 * Q + 1] = lds128(xr + (2 * Q + 1) * 128 + 64); });
      prog_wait<2, 0>(hpv);
      asm volatile("s_nop 1" : "+v"(acc[0]), "+v"(acc[1]), "+v"(acc[2]), "+v"(acc[3]));
      fence_sched();
      STAMP(1);
      static_for<1, 16>([&](auto I) {
        constexpr int i = I;
        u32 hv = 0;
        constexpr bool chk = i < 14 && (i >= 11 || (i & 1));
        if constexpr (chk) hv = prog_read(hpv);
        static_for<0, 8>([&](auto M) {
          constexpr int C = M >> 2, J = M & 3;
          const v2u wa = Wf[(i - 1) & 1][2 * C], wb = Wf[(i - 1) & 1][2 * C + 1];
          mfma_ip(acc[J], v4u{wa.x, wa.y, wb.x, wb.y}, F[C][2 * J + i - 1]);
          fence_sched();
          if constexpr (i < 15 && M < 4) Wf[i & 1][M] = ldstr((i < 8 ? w1r : w1r2) + (i & 7) * 8192 + (M >> 1) * 4096 + (M & 1) * 512);
          if constexpr (i >= 2 && i < 15 && (M == 4 || M == 5)) F[M - 4][i + 6] = lds128(xr + ((i + 6) >> 3) * 1056 + ((i + 6) & 7) * 128 + (M - 4) * 64);
          if constexpr (i == 3 && M == 7) wait_vm<0>();
          if constexpr (i % 3 == 0 && M == 7) prog_write(cp, i);
          if constexpr (chk && M == 6) prog_ensure<(i >= 11 ? i + 2 : i + 3), 0>(hv, hpv);
          fence_sched();
        });
        SSTAMP(i + 1);
      });
      asm volatile("s_nop 7" : "+v"(acc[0]), "+v"(acc[1]), "+v"(acc[2]), "+v"(acc[3]));
      STAMP(3);
      st_sc1(pack2(acc[0], acc[1]), rws, p1v);
      st_sc1(pack2(acc[2], acc[3]), rws, p1v + 1024);
      __builtin_amdgcn_s_setprio(0);
    }
    STAMP(4);
    lane = opq(lane);
    const u32 l15 = lane & 15, g = lane >> 4, q4 = l15 >> 2, p4 = lane & 3;
    const u32 hpv = LB + L_HP + p4 * 4, cpv = LB + L_CP + (lane & 7) * 4, spv = cpv + 32, sp = cp + 32;
    v4u W2[15];
    const u32 w2r = LB + L_RING + (8 * g + q4) * 256 + ((wv ^ (q4 | ((g & 1) << 2))) * 32) + p4 * 8, w2r2 = opq(w2r + 65536);
    auto rdw2 = [&](auto T) { constexpr u32 s = (T + 15) & 15; const u32 ad = (s < 8 ? w2r : w2r2) + (s & 7) * 8192; W2[T] = ldstr2(ad, ad + 1024); };
    auto lds_done = [&]() { asm volatile("s_waitcnt lgkmcnt(0)" ::: "memory"); };
    v4u hp[3];
    u32 rho, rhoh, h1off[3];
    {
      const u32 blk = wv >> 1, mm = (wv & 1) * 8 + q4;
      const u32 hb = WS_P1 + g * 524288 + c2 * 65536;
      rho = 8 + 4 * mm + 2 * (blk & 1);
      h1off[0] = opq(mloc * 64 + rho - 8 < 500 ? hb + mt * 4096 + wv * 512 + l15 * 16 : OOB);
      h1off[1] = opq(mloc * 64 + rho + 8 < 500 ? hb + mt * 4096 + wv * 512 + 256 + l15 * 16 : OOB);
      rho = opq(LB + L_X + (rho + (lane >> 5)) * 64 + (rho >> 2) * 32 + (blk >> 1) * 32 + p4 * 8 + (g & 1) * 4);
      const u32 nx = l15 >> 3;
      rhoh = 72 * nx + 4 * (q4 & 1) + 2 * (w4 & 1);
      const int hrow = (int)(mloc * 64 + rhoh) - 8;
      h1off[2] = opq((hrow >= 0 && hrow < 500) ? hb + (mt + 2 * nx - 1) * 4096 + w4 * 1024 + (nx ? 0 : 896) + (lane & 7) * 16 : OOB);
      rhoh = opq(LB + L_X + (rhoh + (lane >> 5)) * 64 + (rhoh >> 2) * 32 + (w4 >> 1) * 32 + p4 * 8 + (g & 1) * 4);
    }
    const u32 ar = opq(LB + L_X + l15 * 288 + g * 16), p2v = opq(p2off + wv * 1024 + l15 * 64 + g * 16);
    prog_spin<20, 1>(hpv);
    static_for<0, 5>(rdw2);
    lds_done(); prog_write(cp, 20);
    prog_spin<3, 1>(hpv + 16);
    prog_spin<15, 1>(cpv);
    STAMP(5);
    delay(DLY1);
    auto h1_load = [&]() {
      static_for<0, 2>([&](auto K) { hp[K] = __builtin_amdgcn_raw_buffer_load_b128(rws, h1off[K], 0, 16); });
      if (wv >= 4) hp[2] = __builtin_amdgcn_raw_buffer_load_b128(rws, h1off[2], 0, 16);
      mem_order();
    };
    hp[2] = v4u{0, 0, 0, 0};
    h1_load();
    prog_spin<25, 1>(hpv);
    static_for<5, 10>(rdw2);
    lds_done(); prog_write(cp, 25);
    STAMP(6);
    u32 tries = 0;
    for (;; ++tries) {
      const u32 mx = max(max(max4(hp[0]), max4(hp[1])), max4(hp[2]));
      if (__all(mx != SENT) || tries >= SPIN_CAP) break;
      __builtin_amdgcn_s_sleep(2);
      h1_load();
    }
    STAMP(7);
    {
      const f32x2 s0 = red4(hp[0]), s1 = red4(hp[1]);
      *(LAS u32*)(size_t)(rho) = pk(gelu(s0.x), gelu(s0.y));
      *(LAS u32*)(size_t)(rho + 4 * 288) = pk(gelu(s1.x), gelu(s1.y));
      if (wv >= 4) {
        const f32x2 s2 = red4(hp[2]);
        *(LAS u32*)(size_t)(rhoh) = pk(gelu(s2.x), gelu(s2.y));
      }
    }
    prog_write(sp, S_A);
    STAMP(8);
    prog_spin<30, 0>(hpv);
    static_for<10, 15>(rdw2);
    prog_spin<S_A, 0>(spv);
    prog_write(cp, 30);
    STAMP(9);
    {
      f32x4 a0 = {0, 0, 0, 0}, a1 = {0, 0, 0, 0};
      v4u A[17];
      static_for<0, 12>([&](auto Q) { A[Q] = lds128(ar + ((Q + 1) >> 2) * 288 + ((Q + 1) & 3) * 64); });
      fence_sched();
      static_for<0, 15>([&](auto T) {
        a0 = mfma(W2[T], A[T], a0);
        a1 = mfma(W2[T], A[T + 2], a1);
        if constexpr (T < 5) A[T + 12] = lds128(ar + ((T + 13) >> 2) * 288 + ((T + 13) & 3) * 64);
        fence_sched();
      });
      st_sc1(pack2(a0, a1), rws, p2v);
    }
    STAMP(10);
    if (!p3ok) return;
    v4u ge[2];
    const u32 geoff = WS_GE + (bat * 8 + (lane & 7)) * 1024 + wv * 128 + g * 16;
    auto ge_load = [&]() { static_for<0, 2>([&](auto T) { ge[T] = __builtin_amdgcn_raw_buffer_load_b128(rws, geoff + T * 64, 0, 16); }); mem_order(); };
    v4u WF[8][2], WO[8][2];
    u32 wr[4];
    wr[0] = LB + L_RING + (4 * g + q4) * 512 + (((2 * wv) ^ ((4 * g + q4) & 7)) * 32) + p4 * 8;
    wr[1] = opq(wr[0] ^ 32); wr[2] = opq(wr[0] + 65536); wr[3] = opq(wr[1] + 65536);
    auto rdw = [&](v4u (&W)[8][2], auto R) {
      constexpr int K = (R + 7) & 7;
      static_for<0, 2>([&](auto T) { const u32 ad = wr[(K >> 2) * 2 + T] + (K & 3) * 16384; W[K][T] = ldstr2(ad, ad + 8192); });
    };
    const u32 h2off = opq(WS_P2 + (lane >> 3) * 262144 + mt * 16384 + (wv * 2 + ((lane >> 2) & 1)) * 1024 + idx16 * 64 + p4 * 16);
    const u32 fw = opq(LB + L_F + (lane >> 5) * 576 + (wv * 32 + p4 * 8 + ((lane >> 2) & 1) * 4 + ((lane >> 3) & 3)) * 2);
    const u32 fr = opq(LB + L_F + (l15 >> 3) * 576 + g * 16), jr = opq(LB + L_J + lane * 16), jw = opq(jr + wv * 1024);
    const u32 ov = opq((((bat * 250 + 2 * ploc) * 8 + l15) * 256 + wv * 32 + (g & 1) * 16 + (g >> 1) * 8) * 2);
    const rsrc_t ro = mk(a.out, 2048000);
    prog_spin<38, 1>(hpv);
    static_for<0, 4>([&](auto R) { rdw(WF, R); });
    lds_done(); prog_write(cp, 38);
    prog_spin<46, 1>(hpv);
    static_for<4, 8>([&](auto R) { rdw(WF, R); });
    lds_done(); prog_write(cp, 46);
    STAMP(11);
    delay(DLY2);
    {
      v4u fp = __builtin_amdgcn_raw_buffer_load_b128(rws, h2off, 0, 16);
      mem_order();
      ge_load();
      for (u32 spins = 0; !__all(max4(fp) != SENT) && spins < SPIN_CAP; ++spins) {
        __builtin_amdgcn_s_sleep(2);
        mem_order();
        fp = __builtin_amdgcn_raw_buffer_load_b128(rws, h2off, 0, 16);
        tries += 256;
      }
      STAMP(12);
      const f32x2 s = red4(fp);
      const float snd = (lane & 8) ? s.x : s.y, kp = (lane & 8) ? s.y : s.x;
      const float tot = kp + __builtin_bit_cast(float, __builtin_amdgcn_update_dpp(0, __builtin_bit_cast(int, snd), 0x128, 0xf, 0xf, false));
      *(LAS u16*)(size_t)(fw) = (u16)pk(gelu(tot), 0.f);
    }
    prog_write(sp, S_F);
    for (u32 spins = 0; !__all(max(max4(ge[0]), max4(ge[1])) != SENT) && spins < SPIN_CAP; ++spins) { __builtin_amdgcn_s_sleep(2); ge_load(); tries += 65536; }
    prog_spin<S_F, 0>(spv);
    {
      f32x4 d0 = __builtin_bit_cast(f32x4, ge[0]), d1 = __builtin_bit_cast(f32x4, ge[1]);
      v4u b[8];
      static_for<0, 8>([&](auto K) { b[K] = lds128(fr + K * 64); });
      const u32 hv = prog_read(hpv);
      fence_sched();
      static_for<0, 8>([&](auto K) { d0 = mfma(WF[K][0], b[K], d0); d1 = mfma(WF[K][1], b[K], d1); });
      fence_sched();
      prog_ensure<62, 0>(hv, hpv);
      static_for<0, 3>([&](auto R) { rdw(WO, R); });
      fence_sched();
      v4u jf = {pk(tanh_(d0.x), tanh_(d0.y)), pk(tanh_(d0.z), tanh_(d0.w)), pk(tanh_(d1.x), tanh_(d1.y)), pk(tanh_(d1.z), tanh_(d1.w))};
      *(LAS v4u*)(size_t)(jw) = jf;
    }
    prog_write(sp, S_J);
    STAMP(13);
    static_for<3, 8>([&](auto R) { rdw(WO, R); });
    prog_spin<S_J, 0>(spv);
    {
      f32x4 d0 = {0, 0, 0, 0}, d1 = {0, 0, 0, 0};
      v4u b[8];
      static_for<0, 8>([&](auto K) { b[K] = lds128(jr + K * 1024); });
      fence_sched();
      static_for<0, 8>([&](auto K) { d0 = mfma(WO[K][0], b[K], d0); d1 = mfma(WO[K][1], b[K], d1); });
      const auto r0 = __builtin_amdgcn_permlane16_swap(pk(d0.x, d0.y), pk(d1.x, d1.y), false, false);
      const auto r1 = __builtin_amdgcn_permlane16_swap(pk(d0.z, d0.w), pk(d1.z, d1.w), false, false);
      __builtin_amdgcn_raw_buffer_store_b128(v4u{r0[0], r1[0], r0[1], r1[1]}, ro, ov, 0, OAUX);
    }
    STAMP(14);
    if (wq == 8 && bid == 100) { const u64 dl = hp0 + 3000 + 4 * (hp1 - hp0); for (u32 s = 0; s < 1000000 && __builtin_amdgcn_s_memrealtime() < dl; ++s) __builtin_amdgcn_s_sleep(1); }
  }
}

extern "C" void kernel_launch(void* const* d_in, const int* in_sizes, int n_in, void* d_out, int out_size, void* d_ws, size_t ws_size,
                              hipStream_t stream) {
  Args a{(const u16*)d_in[0], (const u16*)d_in[1], (const u16*)d_in[2], (const u16*)d_in[3], (const u16*)d_in[4],
         (const u16*)d_in[5], (const u16*)d_in[6], (u16*)d_out, (unsigned char*)d_ws};
  rnnt_fused<<<256, 768, 0, stream>>>(a);
}
```

```cpp
#include <hip/hip_runtime.h>
#include <hip/hip_bf16.h>
#include <type_traits>

typedef unsigned u32;
typedef unsigned short u16;
typedef u32 v4u __attribute__((ext_vector_type(4)));
typedef u32 v2u __attribute__((ext_vector_type(2)));
typedef float f32x4 __attribute__((ext_vector_type(4)));
typedef float f32x2 __attribute__((ext_vector_type(2)));
typedef short s16x4 __attribute__((ext_vector_type(4)));
typedef __bf16 bf16x8 __attribute__((ext_vector_type(8)));
typedef __bf16 bf16x2 __attribute__((ext_vector_type(2)));
typedef __amdgpu_buffer_rsrc_t rsrc_t;
#define GAS __attribute__((address_space(1)))
#define LAS __attribute__((address_space(3)))
#define DI __device__ __forceinline__

constexpr u32 WS_EPOCH = 0;
constexpr u32 WS_FLAGS = 4096;
constexpr u32 WS_GE = 65536;
constexpr u32 WS_P1 = 1u << 20;
constexpr u32 WS_P2 = 3u << 20;
constexpr u32 WS_BYTES = 5u << 20;
constexpr u32 OOB = 0x80000000u;
constexpr u32 SENT = 0xFFFFFFFFu;
constexpr u32 SPIN_CAP = 100000;
constexpr u32 LSPIN_CAP = 4000000;
constexpr u32 L_RING = 0;
constexpr u32 L_X = 131072;
constexpr u32 L_F = L_X + 19008;
constexpr u32 L_J = L_F + 1152;
constexpr u32 L_JUNK = L_J + 8192;
constexpr u32 L_HP = L_JUNK + 1024;
constexpr u32 L_BP = L_HP + 96;
constexpr u32 L_CP = L_HP + 32;
constexpr u32 L_SP = L_CP + 32;
constexpr u32 L_TS = L_HP + 256;
constexpr u32 LDS_BYTES = L_TS;
static_assert(LDS_BYTES <= 163840, "lds");
#define CODE_LINES 108
#define LOOK 2
#define SOFF 0
#define CPRIO 1
#define OAUX 0
#define FCHK 12
#define SPRIO 3
#define B_(n) (1ull << (n))
#define PUBSET (B_(1) | B_(3) | B_(6) | B_(9) | B_(12) | B_(14) | B_(15) | B_(20) | B_(25) | B_(30) | B_(38) | B_(46))
#define W1T 1
#define W2T 2
#define WFT 2
#define S1AT 3
#define T2AT 3
#define S2AT 12
#define T3AT 1
#define SMODE 1
#define NEAR 14
#define TDLY 0
#define SDLY 0
#define BDLY 0
#define DLY1 0
#define DLY2 0

template <int I, int N, class F> DI void static_for(F&& f) {
  if constexpr (I < N) { f(std::integral_constant<int, I>{}); static_for<I + 1, N>(f); }
}
template <int N> DI void wait_vm() { asm volatile("s_waitcnt vmcnt(%0)" ::"i"(N) : "memory"); }
DI void bar() { asm volatile("s_waitcnt lgkmcnt(0)\n\ts_barrier" ::: "memory"); }
DI void fence_sched() { __builtin_amdgcn_sched_barrier(0); }
DI void mem_order() { asm volatile("" ::: "memory"); }
DI u32 rfl(u32 x) { return __builtin_amdgcn_readfirstlane(x); }
#define TAUX ""
#define WAUX ""
DI void dma16(u32 lds, u32 voff, rsrc_t rs, u32 soff) {
  asm volatile("s_mov_b32 m0, %0\n\ts_nop 0\n\tbuffer_load_dwordx4 %1, %2, %3 offen" WAUX " lds" ::"s"(lds), "v"(voff), "s"(rs), "s"(soff) : "memory");
}
DI void dma4(u32 lds, u32 voff, rsrc_t rs, u32 soff) {
  asm volatile("s_mov_b32 m0, %0\n\ts_nop 0\n\tbuffer_load_dword %1, %2, %3 offen" TAUX " lds" ::"s"(lds), "v"(voff), "s"(rs), "s"(soff) : "memory");
}
DI rsrc_t mk(const void* p, u32 bytes) { return __builtin_amdgcn_make_buffer_rsrc((void*)p, 0, (int)bytes, 0x00020000); }
DI v4u lds128(u32 a) { return *(const LAS v4u*)(size_t)a; }
DI v2u ldstr(u32 a) { return __builtin_bit_cast(v2u, __builtin_amdgcn_ds_read_tr16_b64_v4i16((LAS s16x4*)(size_t)a)); }
DI v4u ldstr2(u32 a0, u32 a1) { v2u x = ldstr(a0), y = ldstr(a1); return v4u{x.x, x.y, y.x, y.y}; }
DI f32x4 mfma(v4u a, v4u b, f32x4 c) {
  return __builtin_amdgcn_mfma_f32_16x16x32_bf16(__builtin_bit_cast(bf16x8, a), __builtin_bit_cast(bf16x8, b), c, 0, 0, 0);
}
DI void mfma_ip(f32x4& c, v4u a, v4u b) { asm volatile("v_mfma_f32_16x16x32_bf16 %0, %1, %2, %0" : "+v"(c) : "v"(a), "v"(b)); }
DI u32 pk(float a, float b) { f32x2 f = {a, b}; return __builtin_bit_cast(u32, __builtin_convertvector(f, bf16x2)); }
DI float lo16(u32 x) { return __builtin_bit_cast(float, x << 16); }
DI float hi16(u32 x) { return __builtin_bit_cast(float, x & 0xFFFF0000u); }
DI float gelu(float x) {
  const float c0 = -2.0f * 0.7978845608028654f * 1.4426950408889634f, c1 = c0 * 0.044715f;
  float e = __builtin_amdgcn_exp2f(x * __builtin_fmaf(x * x, c1, c0));
  return x * __builtin_amdgcn_rcpf(1.0f + e);
}
DI float tanh_(float x) {
  float e = __builtin_amdgcn_exp2f(x * 2.8853900817779268f);
  return __builtin_fmaf(-2.0f, __builtin_amdgcn_rcpf(1.0f + e), 1.0f);
}
DI u32 nosent(u32 x) { return min(x, SENT - 1); }
DI v4u pack2(f32x4 a, f32x4 b) { return v4u{nosent(pk(a.x, a.y)), nosent(pk(a.z, a.w)), nosent(pk(b.x, b.y)), nosent(pk(b.z, b.w))}; }
DI void st_sc1(v4u v, rsrc_t rs, u32 off) { __builtin_amdgcn_raw_buffer_store_b128(v, rs, off, 0, 16); }
DI u32 max4(v4u v) { return max(max(v.x, v.y), max(v.z, v.w)); }
DI u32 min4(v4u v) { return min(min(v.x, v.y), min(v.z, v.w)); }
DI u32 prog_read(u32 va) { u32 v = *(const volatile LAS u32*)(size_t)va; mem_order(); return v; }
DI void prog_write(u32 a, u32 v) {
  asm volatile("s_mov_b64 exec, 1\n\tds_write_b32 %0, %1\n\ts_mov_b64 exec, -1" ::"v"(a), "v"(v) : "memory");
}
template <u32 NEED, int SLEEP> DI void prog_ensure(u32 hv, u32 va) {
  u32 cnt;
  asm volatile("v_cmp_gt_u32 vcc, %3, %0\n\ts_cbranch_vccnz Ls%=\n"
               "Ld%=:\n\t.subsection 1\n"
               "Ls%=:\n\ts_mov_b32 %1, %5\n"
               "Lr%=:\n\ts_sleep %4\n\tds_read_b32 %0, %2\n\ts_waitcnt lgkmcnt(0)\n\tv_cmp_gt_u32 vcc, %3, %0\n\ts_cbranch_vccz Ld%=\n\t"
               "s_sub_u32 %1, %1, 1\n\ts_cbranch_scc0 Lr%=\n\ts_branch Ld%=\n\t.subsection 0"
               : "+v"(hv), "=&s"(cnt) : "v"(va), "i"(NEED), "i"(SLEEP), "i"(LSPIN_CAP) : "memory", "scc", "vcc");
}
template <u32 NEED, int SLEEP> DI void prog_spin(u32 va) {
  u32 cnt, hv;
  asm volatile("s_mov_b32 %1, %5\n"
               "Lr%=:\n\tds_read_b32 %0, %2\n\ts_waitcnt lgkmcnt(0)\n\tv_cmp_gt_u32 vcc, %3, %0\n\ts_cbranch_vccz Ld%=\n\t"
               "s_sleep %4\n\ts_sub_u32 %1, %1, 1\n\ts_cbranch_scc0 Lr%=\n"
               "Ld%=:"
               : "=&v"(hv), "=&s"(cnt) : "v"(va), "i"(NEED), "i"(SLEEP), "i"(LSPIN_CAP) : "memory", "scc", "vcc");
}
template <u32 NEED, int SLEEP> DI void prog_wait(u32 va) { prog_ensure<NEED, SLEEP>(prog_read(va), va); }
#define RT0
#define RT1
#define RTC
constexpr unsigned long long PUBS = PUBSET;
template <int N, u32 STEP, u32 SMASK> DI void rounds(u32& m, u32& so, int& pv, int& fl, int& need, int& rel, u32 va, u32 vb, rsrc_t rs, u32 hp, u32 cpv) {
  u32 cnt, spin, cv;
  asm volatile("s_mov_b32 %4, %15\n"
               "L%=:\n\t" RT0 "s_mov_b32 m0, %0\n\ts_add_u32 %0, %0, 0x1000\n\tbuffer_load_dwordx4 %9, %11, %1 offen" WAUX " lds\n\t"
               "s_mov_b32 m0, %0\n\ts_add_u32 %0, %0, 0x1000\n\tbuffer_load_dwordx4 %10, %11, %1 offen" WAUX " lds\n\t"
               "s_and_b32 %0, %0, 0x1ffff\n\ts_add_u32 %1, %1, %14\n\ts_and_b32 %1, %1, %16\n\ts_add_i32 %3, %3, 1\n\ts_add_i32 %2, %2, 1\n\t"
               "s_bitcmp1_b64 %20, %2\n\ts_waitcnt vmcnt(%17)\n\ts_cbranch_scc1 Lp%=\n"
               "Lq%=:\n\ts_cmp_gt_i32 %3, %8\n\ts_cbranch_scc1 Ls%=\n"
               "Lb%=:\n\ts_sub_u32 %4, %4, 1\n\ts_cbranch_scc0 L%=\n\ts_branch Le%=\n"
               "Lp%=:\n\tv_max_i32 %7, %2, %7\n\ts_mov_b64 exec, 1\n\tds_write_b32 %12, %7\n\ts_mov_b64 exec, -1\n\ts_branch Lq%=\n"
               "Ls%=:\n\ts_mov_b32 %5, %19\n"
               "Lr%=:\n\tds_read_b32 %6, %13\n\ts_waitcnt lgkmcnt(0)\n\ts_nop 1\n\t"
               "v_min_i32_dpp %6, %6, %6 quad_perm:[1,0,3,2] row_mask:0xf bank_mask:0xf\n\ts_nop 1\n\t"
               "v_min_i32_dpp %6, %6, %6 quad_perm:[2,3,0,1] row_mask:0xf bank_mask:0xf\n\ts_nop 1\n\t"
               "v_min_i32_dpp %6, %6, %6 row_half_mirror row_mask:0xf bank_mask:0xf\n\ts_nop 1\n\t"
               "v_readfirstlane_b32 %8, %6\n\ts_cmp_gt_i32 %3, %8\n\ts_cbranch_scc0 Lb%=\n\t"
               "s_cmp_lg_u32 %5, %19\n\ts_cbranch_scc1 Lz%=\n\t"
               "v_mov_b32 %7, %2\n\tv_add_u32 %7, %18, %7\n\ts_mov_b64 exec, 1\n\ts_waitcnt vmcnt(0)\n\tds_write_b32 %12, %7\n\ts_mov_b64 exec, -1\n"
               "Lz%=:\n\ts_sleep 1\n\ts_sub_u32 %5, %5, 1\n\ts_cbranch_scc0 Lr%=\n\ts_branch Lb%=\n"
               "Le%=:"
               : "+s"(m), "+s"(so), "+s"(pv), "+s"(need), "=&s"(cnt), "=&s"(spin), "=&v"(cv), "+v"(fl), "+s"(rel)
               : "v"(va), "v"(vb), "s"(rs), "v"(hp), "v"(cpv), "i"(STEP), "i"(N - 1), "i"(SMASK), "i"(2 * LOOK), "i"(LOOK), "i"(LSPIN_CAP), "s"(PUBS)
               : "memory", "scc" RTC);
}
constexpr u32 nextpub(u32 n) { while (!((PUBS >> n) & 1)) ++n; return n; }
template <int N, u32 STRIDE> DI void touch(const void* p, u32 off) {
  u32 d0, d1, cnt, o2 = off + 64;
  asm volatile("s_mov_b32 %2, %6\n"
               "L%=:\n\ts_load_dword %0, %5, %3\n\t"
               "s_load_dword %1, %5, %4\n\ts_add_u32 %4, %4, %7\n\t"
               "s_add_u32 %3, %3, %7\n\t"
               "s_sub_u32 %2, %2, 1\n\ts_cbranch_scc0 L%=\n\ts_waitcnt lgkmcnt(0)"
               : "=&s"(d0), "=&s"(d1), "=&s"(cnt), "+s"(off), "+s"(o2) : "s"(p), "i"(N - 1), "i"(STRIDE) : "memory", "scc");
}
DI float asf(u32 x) { return __builtin_bit_cast(float, x); }
DI u32 opq(u32 x) { asm volatile("" : "+v"(x)); return x; }
DI f32x2 red4(v4u d) {
  const auto r0 = __builtin_amdgcn_permlane32_swap(d.x, d.z, false, false), r1 = __builtin_amdgcn_permlane32_swap(d.y, d.w, false, false);
  const float s0 = lo16(r0[0]) + lo16(r0[1]), s1 = hi16(r0[0]) + hi16(r0[1]), s2 = lo16(r1[0]) + lo16(r1[1]), s3 = hi16(r1[0]) + hi16(r1[1]);
  const auto q0 = __builtin_amdgcn_permlane16_swap(__builtin_bit_cast(u32, s0), __builtin_bit_cast(u32, s2), false, false);
  const auto q1 = __builtin_amdgcn_permlane16_swap(__builtin_bit_cast(u32, s1), __builtin_bit_cast(u32, s3), false, false);
  return f32x2{asf(q0[0]) + asf(q0[1]), asf(q1[0]) + asf(q1[1])};
}
DI void delay(int n) { for (int i = 0; i < n; ++i) __builtin_amdgcn_s_sleep(1); }

#define STAMP(i)
#define SSTAMP(i)

#define PAD(k) asm volatile("PAD%c0:\n\ts_cbranch_execz PAD%c1" ::"i"(k), "i"((k) + 8))
#define PADX(k) asm volatile(".subsection 1\nPAD%c0:\n\ts_cbranch_execz PAD%c1\n\t.subsection 0" ::"i"(k), "i"((k) + 8))
DI void warm(u32 wv) {
  asm volatile("s_mov_b64 exec, 0\n\ts_getpc_b64 vcc\n\ts_add_u32 vcc_lo, vcc_lo, %0\n\ts_addc_u32 vcc_hi, vcc_hi, 0\n\ts_setpc_b64 vcc\n\t"
               "s_branch PAD0\n\ts_branch PAD1\n\ts_branch PAD2\n\ts_branch PAD3\n\ts_branch PAD4\n\ts_branch PAD5\n\ts_branch PAD6\n\ts_branch PAD7\n"
               "PAD24:\nPAD25:\nPAD26:\nPAD27:\nPAD28:\nPAD29:\nPAD30:\nPAD31:\n\ts_mov_b64 exec, -1"
               :: "s"(wv * 4 + 12) : "memory", "scc", "vcc");
}

struct Args { const u16 *x, *w1, *w2, *gp, *wf, *wg, *wo; u16* out; unsigned char* ws; };
typedef unsigned long long u64;
typedef u64 v8q __attribute__((ext_vector_type(8)));

constexpr u32 S_A = 1, S_F = 2, S_J = 3;

__global__ void __launch_bounds__(768, 3) rnnt_fused(Args unused) {
  __shared__ __attribute__((aligned(16))) unsigned char smem[LDS_BYTES];
  const u32 LB = (u32)(size_t)(LAS unsigned char*)smem;
  u32 tid = threadIdx.x;
  v8q ka; u64 kws;
  u64 pc;
  asm volatile("s_load_dwordx16 %0, %3, 0x0\n\ts_load_dwordx2 %1, %3, 0x40\n\ts_getpc_b64 %2" : "=&s"(ka), "=&s"(kws), "=&s"(pc) : "s"(__builtin_amdgcn_kernarg_segment_ptr()) : "memory");
  asm volatile("s_mov_b32 m0, %1\n\ts_nop 0\n\tglobal_load_lds_dword %0, %2" :: "v"(min(((blockIdx.x >> 3) * 1536 + tid * 2) & 0x3f80, ((u32)CODE_LINES - 1) << 7)), "s"(LB + L_JUNK), "s"(pc) : "memory");
  const u32 wq = rfl(tid >> 6), w4 = wq & 3, wv = wq - 4;
  const u32 bid = blockIdx.x, x8 = bid & 7, j = bid >> 3, mt = j >> 1, mloc = mt & 7, bat = mt >> 3;
  const u32 k1 = x8 >> 1, n1 = 2 * (x8 & 1) + (j & 1);
  u32 lane = tid & 63;
  const u32 wbase = k1 * 32768 + n1 * 128;
#define KWAIT(...) asm volatile("s_waitcnt lgkmcnt(0)" : "+s"(ka), "+s"(kws), __VA_ARGS__::"memory")
  u32 w1v = ((tid >> 3) & 31) * 512 + wbase + (((tid & 7) ^ (((tid >> 3) & 2) | ((tid >> 4) & 4))) * 16);
  u32 xv = ((tid >> 3) & 31) * 512 + (mloc * 65536 - 3584 + k1 * 128) + (tid & 7) * 16;
  u32 m = LB + L_RING + w4 * 1024, mx = LB + L_X + w4 * 1056, nx = min(mloc * 128 + 137, 1000u) * 512, xo = bat * 512000;
  asm volatile("" : "+v"(w1v), "+v"(xv));
  KWAIT("+s"(m), "+s"(mx), "+s"(nx), "+s"(xo));
  Args a{(const u16*)ka[0], (const u16*)ka[1], (const u16*)ka[2], (const u16*)ka[3], (const u16*)ka[4], (const u16*)ka[5], (const u16*)ka[6], (u16*)ka[7], (unsigned char*)kws};
  const rsrc_t rw1 = mk(a.w1, 15 * 131072);
  const u32 c2 = x8, n2 = j & 1;
  const u32 idx16 = x8 * 2 + (j & 1), pi = mt * 16 + idx16, ploc = pi & 127;
  const bool p3ok = ploc < 125;

  if (__builtin_expect(wq < 4, 1)) {
    {
      const rsrc_t rx = mk((const void*)(ka[0] + xo), nx);
      static_for<0, 4>([&](auto I) { dma16(mx + I * 4224, xv + I * 16384, rx, 0); });
      if (w4 < 2) dma16(mx + 4 * 4224, xv + 65536, rx, 0);
      dma16(m, w1v, rw1, 0);
      dma16(m + 4096, w1v, rw1, 16384);
    }
    asm volatile("s_barrier" ::: "memory");
    __builtin_amdgcn_s_setprio(SPRIO);
    STAMP(1);
    m += 8192;
    u32 so = 131072;
    int pv = 1 - LOOK, fl = 0, need = -14, rel = 0;
    delay(SDLY);
    const u32 hp = LB + L_HP + w4 * 4, cpv = LB + L_CP + (lane & 7) * 4;
    rounds<14, 131072, ~0u>(m, so, pv, fl, need, rel, w1v, w1v + 16384, rw1, hp, cpv);
    STAMP(2);
    lane = opq(lane);
    const u32 g = lane >> 4;
    const rsrc_t rw2 = mk(a.w2, 15 * 131072);
    const u32 w2v = (c2 * 32 + w4 * 4 + g) * 512 + n2 * 256 + ((((lane >> 1) & 7) ^ (g | (((w4 >> 1) & 1) << 2))) * 32) + (lane & 1) * 16;
    so = 0;
    rounds<15, 131072, ~0u>(m, so, pv, fl, need, rel, w2v, w2v + 8192, rw2, hp, cpv);
    STAMP(3);
    const rsrc_t rws = mk(a.ws, WS_BYTES);
    const u32 l15 = lane & 15;
    if (!p3ok) {
      if (ploc < 126) { wait_vm<0>(); prog_write(hp, 30); }
      if (ploc >= 126) {
        const u32 qd = bat * 2 + (pi & 1), et = qd * 4 + w4;
        const rsrc_t rwg = mk(a.wg, 131072);
        const u32 wgv = w1v - wbase + qd * 128;
        so = 0;
        rounds<3, 32768, ~0u>(m, so, pv, fl, need, rel, wgv, wgv + 16384, rwg, hp, cpv);
        need = 0;
        rounds<1, 32768, ~0u>(m, so, pv, fl, need, rel, wgv, wgv + 16384, rwg, hp, cpv);
        v4u b[8];
        static_for<0, 8>([&](auto K) { b[K] = *(const GAS v4u*)(a.gp + l15 * 256 + K * 32 + 8 * g); });
        wait_vm<8>();
        prog_write(hp, 34);
        prog_spin<34, 0>(LB + L_HP + (lane & 3) * 4);
        const u32 q4 = l15 >> 2;
        const u32 gr = LB + L_RING + g * 1024 + q4 * 128 + ((w4 ^ ((q4 >> 1) + 2 * (g & 1))) * 32) + (lane & 3) * 8;
        v4u wa[8];
        static_for<0, 8>([&](auto K) { const u32 ad = gr + (((K >> 1) + 14) & 15) * 8192 + (K & 1) * 4096; wa[K] = ldstr2(ad, ad + 512); });
        fence_sched();
        f32x4 d = {0, 0, 0, 0};
        static_for<0, 8>([&](auto K) { d = mfma(wa[K], b[K], d); });
        v4u o = __builtin_bit_cast(v4u, d);
        prog_spin<2, 1>(opq(LB + L_BP));
        st_sc1(v4u{nosent(o.x), nosent(o.y), nosent(o.z), nosent(o.w)}, rws, WS_GE + l15 * 1024 + et * 64 + g * 16);
      } else if (pi == 125 && w4 == 0) {
        u32 epoch;
        asm volatile("s_load_dword %0, %1, 0x0\n\ts_waitcnt lgkmcnt(0)" : "=s"(epoch) : "s"(a.ws) : "memory");
        epoch += 1;
        for (u32 spins = 0; spins < SPIN_CAP; ++spins) {
          v4u f4;
          asm volatile("buffer_load_dwordx4 %0, %1, %2, 0 offen sc1\n\ts_waitcnt vmcnt(0)" : "=v"(f4) : "v"(WS_FLAGS + (lane >> 2) * 128 + (lane & 3) * 16), "s"(rws) : "memory");
          if (__all(f4.x == epoch && f4.y == epoch && f4.z == epoch && f4.w == epoch)) break;
        }
        if (lane == 0) __hip_atomic_store((GAS u32*)(a.ws + WS_EPOCH), epoch, __ATOMIC_RELAXED, __HIP_MEMORY_SCOPE_AGENT);
      }
      return;
    }
    const u32 r2 = 2 * w4 + (lane >> 5);
    const u32 wfv = r2 * 512 + ((((lane >> 1) & 15) ^ (r2 & 7)) * 32) + (lane & 1) * 16;
    const rsrc_t rwf = mk(a.wf, 131072), rwo = mk(a.wo, 131072);
    so = 14 * 8192;
    rounds<16, 8192, 0x1ffff>(m, so, pv, fl, need, rel, wfv, wfv + 4096, rwf, hp, cpv);
    STAMP(4);
    rounds<15, 8192, 0x1ffff>(m, so, pv, fl, need, rel, wfv, wfv + 4096, rwo, hp, cpv);
    need = 0;
    rounds<1, 8192, 0x1ffff>(m, so, pv, fl, need, rel, wfv, wfv + 4096, rwo, hp, cpv);
    STAMP(5);
    wait_vm<0>();
    prog_write(hp, 62);
    STAMP(6);
  } else {
    const rsrc_t rws = mk(a.ws, WS_BYTES);
    const u32 p1off = WS_P1 + k1 * 524288 + 2 * n1 * 65536 + mt * 4096;
    const u32 p2off = WS_P2 + c2 * 262144 + mt * 16384 + n2 * 8192;
    const u32 cp = LB + L_CP + wv * 4;
    u32 ep2 = 0, fo2 = 0;
    if constexpr (W1T) dma4(LB + L_JUNK, lane < 7 ? (1 + (wv >> 2) * 7 + lane) * 131072 + wbase + (mt * 4 + w4) * 512 : OOB, rw1, 0);
    *(volatile LAS u32*)(size_t)(LB + L_HP + lane * 4) = 0;
    bar();
    warm(wv);
    STAMP(1);
    if (wv >= 4) {
      prog_write(cp, 15);
      const u32 hpq = LB + L_HP + (lane & 3) * 4;
      if (w4 == 0) {
        const v4u sv = {SENT, SENT, SENT, SENT};
        const u32 l15 = lane & 15, g = lane >> 4;
        u32 epoch;
        asm volatile("s_load_dword %0, %1, 0x0\n\ts_waitcnt lgkmcnt(0)" : "=s"(epoch) : "s"(a.ws) : "memory");
        epoch += 1;
        const u32 ln = mt * 60 + lane;
        const u32 t2v = opq(lane < 60 ? (ln >> 6) * 131072 + (c2 * 32 + ((ln >> 1) & 31)) * 512 + n2 * 256 + (ln & 1) * 128 : OOB);
        const u32 flv = opq(lane == 0 ? WS_FLAGS + mt * 128 + idx16 * 4 : OOB);
        const u32 fline = lane < 48 ? min(max(mt + g, 1u) - 1, 15u) : 7 + (lane & 8);
        const u32 foff = opq(WS_FLAGS + fline * 128 + (lane < 48 ? l15 : 8 + (lane & 7)) * 4);
        auto check = [&](u32 set) {
          for (u32 spins = 0; spins < SPIN_CAP; ++spins) {
            u32 fl;
            asm volatile("buffer_load_dword %0, %1, %2, %3 offen sc1\n\ts_waitcnt vmcnt(0)" : "=v"(fl) : "v"(foff), "s"(rws), "s"(set) : "memory");
            if (__all(fl == epoch)) break;
            __builtin_amdgcn_s_sleep(4);
          }
        };
        if constexpr (S1AT > 0) prog_spin<S1AT, 1>(hpq);
        if constexpr (W2T == 1 && S1AT == T2AT) dma4(LB + L_JUNK, t2v, mk(a.w2, 15 * 131072), 0);
        static_for<0, 8>([&](auto I) { st_sc1(sv, rws, p1off + (I >> 2) * 65536 + (I & 3) * 1024 + lane * 16); });
        if constexpr (W2T == 1 && S1AT != T2AT) { prog_spin<T2AT, 1>(hpq); dma4(LB + L_JUNK, t2v, mk(a.w2, 15 * 131072), 0); }
        wait_vm<0>();
        __builtin_amdgcn_raw_buffer_store_b32(epoch, rws, flv, 0, 16);
        prog_write(LB + L_BP, 1);
        STAMP(2);
        prog_spin<S2AT, 1>(hpq);
        static_for<0, 8>([&](auto I) { st_sc1(sv, rws, p2off + I * 1024 + lane * 16); });
        if (ploc >= 126) static_for<0, 4>([&](auto I) { st_sc1(sv, rws, WS_GE + (I * 4 + g) * 1024 + (bat * 2 + (pi & 1)) * 256 + l15 * 16); });
        prog_spin<FCHK, 1>(LB + L_CP + (lane & 7) * 4);
        STAMP(3);
        check(0);
        __builtin_amdgcn_raw_buffer_store_b32(epoch, rws, flv, 2048, 16);
        prog_write(LB + L_BP, 2);
        ep2 = epoch; fo2 = foff;
      } else {
        if constexpr (T3AT > 0) prog_spin<T3AT, 1>(hpq);
        if constexpr (W2T == 2) {
          const u32 u = mt * 3 + w4 - 1;
          for (u32 q = u; q < 240; q += 48) touch<4, 512>(a.w2, (q >> 4) * 131072 + (c2 * 32 + ((q >> 1) & 7) * 4) * 512 + n2 * 256 + (q & 1) * 128);
        }
        STAMP(2);
        if constexpr (WFT == 1) touch<22, 128>((j & 1) ? a.wo : a.wf, min((mt * 3 + w4 - 1) * 22, 1002u) * 128);
        if constexpr (WFT == 2) { if (w4 < 3) touch<4, 128>(w4 == 1 ? a.wf : a.wo, bid * 512); }
        STAMP(3);
      }
    } else {
      PAD(0);
      __builtin_amdgcn_s_setprio(CPRIO);
      const u32 l15 = lane & 15, g = lane >> 4, q4 = l15 >> 2, p4 = lane & 3;
      const u32 hpv = LB + L_HP + p4 * 4;
      f32x4 acc[4] = {{0, 0, 0, 0}, {0, 0, 0, 0}, {0, 0, 0, 0}, {0, 0, 0, 0}};
      v4u F[2][21];
      v2u Wf[2][4];
      const u32 xr = opq(LB + L_X + l15 * 1056 + g * 16);
      const u32 w1r = LB + L_RING + g * 1024 + q4 * 128 + ((wv ^ ((q4 >> 1) + 2 * (g & 1))) * 32) + p4 * 8;
      const u32 p1v = opq(p1off + (w4 >> 1) * 65536 + (w4 & 1) * 2048 + l15 * 64 + g * 16);
      const u32 w1r2 = opq(w1r + 65536), bpv = opq(LB + L_BP);
      prog_spin<1, 0>(hpv);
      STAMP(2);
      static_for<0, 4>([&](auto T) { Wf[0][T] = ldstr(w1r + (T >> 1) * 4096 + (T & 1) * 512); });
      static_for<0, 4>([&](auto Q) { F[0][2 * Q] = lds128(xr + 2 * Q * 128); });
      static_for<0, 4>([&](auto Q) { F[1][2 * Q] = lds128(xr + 2 * Q * 128 + 64); });
      static_for<0, 4>([&](auto Q) { F[0][2 * Q + 1] = lds128(xr + (2 * Q + 1) * 128); });
      static_for<0, 4>([&](auto Q) { F[1][2 * Q + 1] = lds128(xr + (2 * Q + 1) * 128 + 64); });
      asm volatile("s_nop 1" : "+v"(acc[0]), "+v"(acc[1]), "+v"(acc[2]), "+v"(acc[3]));
      fence_sched();
      static_for<1, 16>([&](auto I) {
        constexpr int i = I;
        SSTAMP(i);
        u32 hv = 0;
        constexpr bool chk = i > 1 && i < 14 && nextpub(i + 2) != nextpub(i + 1);
        if constexpr (chk) hv = prog_read(hpv);
        if constexpr (i == 14) hv = prog_read(bpv);
        if constexpr (i % 3 == 1) PAD(1 + i / 3);
        if constexpr (i == 1) PADX(7);
        static_for<0, 8>([&](auto M) {
          constexpr int C = M >> 2, J = M & 3;
          const v2u wa = Wf[(i - 1) & 1][2 * C], wb = Wf[(i - 1) & 1][2 * C + 1];
          mfma_ip(acc[J], v4u{wa.x, wa.y, wb.x, wb.y}, F[C][2 * J + i - 1]);
          fence_sched();
          if constexpr (i > 1 && i < 15 && M < 4) Wf[i & 1][M] = ldstr((i < 8 ? w1r : w1r2) + (i & 7) * 8192 + (M >> 1) * 4096 + (M & 1) * 512);
          if constexpr (i >= 2 && i < 15 && (M == 4 || M == 5)) F[M - 4][i + 6] = lds128(xr + ((i + 6) >> 3) * 1056 + ((i + 6) & 7) * 128 + (M - 4) * 64);
          if constexpr (i % 3 == 0 && M == 7) prog_write(cp, i);
          if constexpr (chk && M == 6) prog_ensure<nextpub(i + 2), 0>(hv, hpv);
          if constexpr (i == 1 && M == 7) { prog_spin<nextpub(3), 0>(hpv); static_for<0, 4>([&](auto T) { Wf[1][T] = ldstr(w1r + 8192 + (T >> 1) * 4096 + (T & 1) * 512); }); }
          if constexpr (i == 14 && M == 6) prog_ensure<1, 0>(hv, bpv);
          fence_sched();
        });
      });
      asm volatile("s_nop 7" : "+v"(acc[0]), "+v"(acc[1]), "+v"(acc[2]), "+v"(acc[3]));
      STAMP(3);
      PAD(6);
      st_sc1(pack2(acc[0], acc[1]), rws, p1v);
      st_sc1(pack2(acc[2], acc[3]), rws, p1v + 1024);
      __builtin_amdgcn_s_setprio(0);
    }
    STAMP(4);
    lane = opq(lane);
    const u32 l15 = lane & 15, g = lane >> 4, q4 = l15 >> 2, p4 = lane & 3;
    const u32 hpv = LB + L_HP + p4 * 4, cpv = LB + L_CP + (lane & 7) * 4, spv = cpv + 32, sp = cp + 32;
    PAD(8);
    v4u W2[15];
    const u32 w2r = LB + L_RING + (8 * g + q4) * 256 + ((wv ^ (q4 | ((g & 1) << 2))) * 32) + p4 * 8, w2r2 = opq(w2r + 65536);
    auto rdw2 = [&](auto T) { constexpr u32 s = (T + 15) & 15; const u32 ad = (s < 8 ? w2r : w2r2) + (s & 7) * 8192; W2[T] = ldstr2(ad, ad + 1024); };
    auto lds_done = [&]() { asm volatile("s_waitcnt lgkmcnt(0)" ::: "memory"); };
    v4u hp[3];
    u32 rho, rhoh, h1off[3];
    {
      const u32 blk = wv >> 1, mm = (wv & 1) * 8 + q4;
      const u32 hb = WS_P1 + g * 524288 + c2 * 65536;
      rho = 8 + 4 * mm + 2 * (blk & 1);
      h1off[0] = opq(mloc * 64 + rho - 8 < 500 ? hb + mt * 4096 + wv * 512 + l15 * 16 : OOB);
      h1off[1] = opq(mloc * 64 + rho + 8 < 500 ? hb + mt * 4096 + wv * 512 + 256 + l15 * 16 : OOB);
      rho = opq(LB + L_X + (rho + (lane >> 5)) * 64 + (rho >> 2) * 32 + (blk >> 1) * 32 + p4 * 8 + (g & 1) * 4);
      const u32 nx = l15 >> 3;
      rhoh = 72 * nx + 4 * (q4 & 1) + 2 * (w4 & 1);
      const int hrow = (int)(mloc * 64 + rhoh) - 8;
      h1off[2] = opq((hrow >= 0 && hrow < 500) ? hb + (mt + 2 * nx - 1) * 4096 + w4 * 1024 + (nx ? 0 : 896) + (lane & 7) * 16 : OOB);
      rhoh = opq(LB + L_X + (rhoh + (lane >> 5)) * 64 + (rhoh >> 2) * 32 + (w4 >> 1) * 32 + p4 * 8 + (g & 1) * 4);
    }
    const u32 ar = opq(LB + L_X + l15 * 288 + g * 16), p2v = opq(p2off + wv * 1024 + l15 * 64 + g * 16);
    prog_spin<20, 1>(hpv);
    PAD(9);
    static_for<0, 5>(rdw2);
    lds_done(); prog_write(cp, 20);
    prog_spin<2, 1>(opq(LB + L_BP));
    prog_spin<15, 1>(cpv);
    STAMP(5);
    delay(DLY1);
    auto h1_load = [&]() {
      static_for<0, 2>([&](auto K) { hp[K] = __builtin_amdgcn_raw_buffer_load_b128(rws, h1off[K], 0, 16); });
      if (wv >= 4) hp[2] = __builtin_amdgcn_raw_buffer_load_b128(rws, h1off[2], 0, 16);
      mem_order();
    };
    hp[2] = v4u{0, 0, 0, 0};
    h1_load();
    prog_spin<25, 1>(hpv);
    static_for<5, 10>(rdw2);
    lds_done(); prog_write(cp, 25);
    STAMP(6);
    PAD(10);
    u32 tries = 0;
    for (;; ++tries) {
      const u32 mx = max(max(max4(hp[0]), max4(hp[1])), max4(hp[2]));
      if (__all(mx != SENT) || tries >= SPIN_CAP) break;
      __builtin_amdgcn_s_sleep(2);
      h1_load();
    }
    STAMP(7);
    PAD(11);
    {
      const f32x2 s0 = red4(hp[0]), s1 = red4(hp[1]);
      *(LAS u32*)(size_t)(rho) = pk(gelu(s0.x), gelu(s0.y));
      *(LAS u32*)(size_t)(rho + 4 * 288) = pk(gelu(s1.x), gelu(s1.y));
    PAD(12);
      if (wv >= 4) {
        const f32x2 s2 = red4(hp[2]);
        *(LAS u32*)(size_t)(rhoh) = pk(gelu(s2.x), gelu(s2.y));
      }
    }
    prog_write(sp, S_A);
    STAMP(8);
    PAD(13);
    prog_spin<30, 0>(hpv);
    static_for<10, 15>(rdw2);
    prog_spin<S_A, 0>(spv);
    prog_write(cp, 30);
    STAMP(9);
    PAD(14);
    {
      f32x4 a0 = {0, 0, 0, 0}, a1 = {0, 0, 0, 0};
      v4u A[17];
      static_for<0, 9>([&](auto Q) { A[Q] = lds128(ar + ((Q + 1) >> 2) * 288 + ((Q + 1) & 3) * 64); });
      fence_sched();
      static_for<0, 15>([&](auto T) {
        a0 = mfma(W2[T], A[T], a0);
        a1 = mfma(W2[T], A[T + 2], a1);
        if constexpr (T < 8) A[T + 9] = lds128(ar + ((T + 10) >> 2) * 288 + ((T + 10) & 3) * 64);
        fence_sched();
      });
      st_sc1(pack2(a0, a1), rws, p2v);
    }
    STAMP(10);
    PAD(15);
    if (!p3ok) return;
    if (wv == 4) {
      for (u32 spins = 0; spins < SPIN_CAP; ++spins) {
        u32 fl;
        asm volatile("buffer_load_dword %0, %1, %2, 0 offen offset:2048 sc1\n\ts_waitcnt vmcnt(0)" : "=v"(fl) : "v"(fo2), "s"(rws) : "memory");
        if (__all(fl == ep2)) break;
        __builtin_amdgcn_s_sleep(4);
      }
      prog_write(LB + L_BP, 3);
    }
    v4u ge[2];
    const u32 geoff = WS_GE + (bat * 8 + (lane & 7)) * 1024 + wv * 128 + g * 16;
    auto ge_load = [&]() { static_for<0, 2>([&](auto T) { ge[T] = __builtin_amdgcn_raw_buffer_load_b128(rws, geoff + T * 64, 0, 16); }); mem_order(); };
    v4u WF[8][2], WO[8][2];
    u32 wr[4];
    wr[0] = LB + L_RING + (4 * g + q4) * 512 + (((2 * wv) ^ ((4 * g + q4) & 7)) * 32) + p4 * 8;
    wr[1] = opq(wr[0] ^ 32); wr[2] = opq(wr[0] + 65536); wr[3] = opq(wr[1] + 65536);
    auto rdw = [&](v4u (&W)[8][2], auto R) {
      constexpr int K = (R + 7) & 7;
      static_for<0, 2>([&](auto T) { const u32 ad = wr[(K >> 2) * 2 + T] + (K & 3) * 16384; W[K][T] = ldstr2(ad, ad + 8192); });
    };
    const u32 h2off = opq(WS_P2 + (lane >> 3) * 262144 + mt * 16384 + (wv * 2 + ((lane >> 2) & 1)) * 1024 + idx16 * 64 + p4 * 16);
    const u32 fw = opq(LB + L_F + (lane >> 5) * 576 + (wv * 32 + p4 * 8 + ((lane >> 2) & 1) * 4 + ((lane >> 3) & 3)) * 2);
    const u32 fr = opq(LB + L_F + (l15 >> 3) * 576 + g * 16), jr = opq(LB + L_J + lane * 16), jw = opq(jr + wv * 1024);
    const u32 ov = opq((((bat * 250 + 2 * ploc) * 8 + l15) * 256 + wv * 32 + (g & 1) * 16 + (g >> 1) * 8) * 2);
    const rsrc_t ro = mk(a.out, 2048000);
    prog_spin<38, 1>(hpv);
    PAD(16);
    static_for<0, 4>([&](auto R) { rdw(WF, R); });
    lds_done(); prog_write(cp, 38);
    prog_spin<46, 1>(hpv);
    static_for<4, 8>([&](auto R) { rdw(WF, R); });
    lds_done(); prog_write(cp, 46);
    prog_spin<3, 1>(opq(LB + L_BP));
    STAMP(11);
    PAD(17);
    delay(DLY2);
    {
      v4u fp = __builtin_amdgcn_raw_buffer_load_b128(rws, h2off, 0, 16);
      mem_order();
      ge_load();
      for (u32 spins = 0; !__all(max4(fp) != SENT) && spins < SPIN_CAP; ++spins) {
        __builtin_amdgcn_s_sleep(2);
        mem_order();
        fp = __builtin_amdgcn_raw_buffer_load_b128(rws, h2off, 0, 16);
        tries += 256;
      }
      STAMP(12);
    PAD(18);
      const f32x2 s = red4(fp);
      const float snd = (lane & 8) ? s.x : s.y, kp = (lane & 8) ? s.y : s.x;
      const float tot = kp + __builtin_bit_cast(float, __builtin_amdgcn_update_dpp(0, __builtin_bit_cast(int, snd), 0x128, 0xf, 0xf, false));
      *(LAS u16*)(size_t)(fw) = (u16)pk(gelu(tot), 0.f);
    }
    prog_write(sp, S_F);
    for (u32 spins = 0; !__all(max(max4(ge[0]), max4(ge[1])) != SENT) && spins < SPIN_CAP; ++spins) { __builtin_amdgcn_s_sleep(2); ge_load(); tries += 65536; }
    prog_spin<S_F, 0>(spv);
    PAD(19);
    {
      f32x4 d0 = __builtin_bit_cast(f32x4, ge[0]), d1 = __builtin_bit_cast(f32x4, ge[1]);
      v4u b[8];
      static_for<0, 8>([&](auto K) { b[K] = lds128(fr + K * 64); });
      const u32 hv = prog_read(hpv);
      fence_sched();
      static_for<0, 8>([&](auto K) { d0 = mfma(WF[K][0], b[K], d0); d1 = mfma(WF[K][1], b[K], d1); });
      fence_sched();
      prog_ensure<62, 0>(hv, hpv);
    PAD(20);
      static_for<0, 3>([&](auto R) { rdw(WO, R); });
      fence_sched();
      v4u jf = {pk(tanh_(d0.x), tanh_(d0.y)), pk(tanh_(d0.z), tanh_(d0.w)), pk(tanh_(d1.x), tanh_(d1.y)), pk(tanh_(d1.z), tanh_(d1.w))};
      *(LAS v4u*)(size_t)(jw) = jf;
    }
    prog_write(sp, S_J);
    STAMP(13);
    PAD(21);
    static_for<3, 8>([&](auto R) { rdw(WO, R); });
    prog_spin<S_J, 0>(spv);
    PAD(22);
    {
      f32x4 d0 = {0, 0, 0, 0}, d1 = {0, 0, 0, 0};
      v4u b[8];
      static_for<0, 8>([&](auto K) { b[K] = lds128(jr + K * 1024); });
      fence_sched();
      static_for<0, 8>([&](auto K) { d0 = mfma(WO[K][0], b[K], d0); d1 = mfma(WO[K][1], b[K], d1); });
      fence_sched();
      PAD(23);
      const auto r0 = __builtin_amdgcn_permlane16_swap(pk(d0.x, d0.y), pk(d1.x, d1.y), false, false);
      const auto r1 = __builtin_amdgcn_permlane16_swap(pk(d0.z, d0.w), pk(d1.z, d1.w), false, false);
      __builtin_amdgcn_raw_buffer_store_b128(v4u{r0[0], r1[0], r0[1], r1[1]}, ro, ov, 0, OAUX);
    }
    STAMP(14);
  }
}

extern "C" void kernel_launch(void* const* d_in, const int* in_sizes, int n_in, void* d_out, int out_size, void* d_ws, size_t ws_size,
                              hipStream_t stream) {
  Args a{(const u16*)d_in[0], (const u16*)d_in[1], (const u16*)d_in[2], (const u16*)d_in[3], (const u16*)d_in[4],
         (const u16*)d_in[5], (const u16*)d_in[6], (u16*)d_out, (unsigned char*)d_ws};
  rnnt_fused<<<256, 768, 0, stream>>>(a);
}
```

```cpp
#include <hip/hip_runtime.h>
#include <hip/hip_bf16.h>
#include <type_traits>

typedef unsigned u32;
typedef unsigned short u16;
typedef unsigned long long u64;
typedef u32 v4u __attribute__((ext_vector_type(4)));
typedef u32 v2u __attribute__((ext_vector_type(2)));
typedef u64 v8q __attribute__((ext_vector_type(8)));
typedef float f32x4 __attribute__((ext_vector_type(4)));
typedef float f32x2 __attribute__((ext_vector_type(2)));
typedef short s16x4 __attribute__((ext_vector_type(4)));
typedef __bf16 bf16x8 __attribute__((ext_vector_type(8)));
typedef __bf16 bf16x2 __attribute__((ext_vector_type(2)));
typedef __amdgpu_buffer_rsrc_t rsrc_t;
#define GAS __attribute__((address_space(1)))
#define LAS __attribute__((address_space(3)))
#define DI __device__ __forceinline__
template <int N> using IC = std::integral_constant<int, N>;

constexpr u32 WS_EPOCH = 0;
constexpr u32 WS_FLAGS = 4096;
constexpr u32 WS_GE = 65536;
constexpr u32 WS_P1 = 1u << 20;
constexpr u32 WS_P2 = 3u << 20;
constexpr u32 WS_PL = 5u << 20;
constexpr u32 WS_BYTES = 6u << 20;
constexpr u32 OOB = 0x80000000u;
constexpr u32 SPIN_CAP = 100000;
constexpr u32 LSPIN_CAP = 4000000;
constexpr u32 L_RING = 0;
constexpr u32 L_KEY = 131072;
constexpr u32 L_JUNK = L_KEY + 64;
constexpr u32 L_F = L_JUNK + 256;
constexpr u32 L_X = L_F + 1152;
constexpr u32 L_O = L_KEY + 8192;
constexpr u32 L_J = L_KEY + 24576;
constexpr u32 LDS_BYTES = L_J + 8192;
static_assert(L_X + 5760 <= L_O && L_X + 19008 <= L_J && LDS_BYTES <= 163840, "lds map");

#define CODE_LINES 96
#define LOOK 3
#define NA 8
#define NW 6
#define TDLY 6
#define DLY1 5
#define DLY2 6
#define GDLY 4
#define SPRIO 3
#define BPRIO 1
#define AW 5
#define XAUX " nt"

template <int I, int N, class F> DI void static_for(F&& f) {
  if constexpr (I < N) { f(IC<I>{}); static_for<I + 1, N>(f); }
}
template <int N> DI void wait_vm() { asm volatile("s_waitcnt vmcnt(%0)" ::"i"(N) : "memory"); }
DI void bar() { asm volatile("s_waitcnt lgkmcnt(0)\n\ts_barrier" ::: "memory"); }
DI void bar_raw() { asm volatile("s_barrier" ::: "memory"); }
DI void fence_sched() { __builtin_amdgcn_sched_barrier(0); }
DI void mem_order() { asm volatile("" ::: "memory"); }
DI u32 opq(u32 x) { asm volatile("" : "+v"(x)); return x; }
template <int N> DI void nap() { if constexpr (N > 0) __builtin_amdgcn_s_sleep(N); }
DI void dma16(u32 lds, u32 voff, rsrc_t rs, u32 soff) {
  asm volatile("s_mov_b32 m0, %0\n\ts_nop 0\n\tbuffer_load_dwordx4 %1, %2, %3 offen lds" ::"s"(lds), "v"(voff), "s"(rs), "s"(soff) : "memory");
}
DI void dma16x(u32 lds, u32 voff, rsrc_t rs, u32 soff) {
  asm volatile("s_mov_b32 m0, %0\n\ts_nop 0\n\tbuffer_load_dwordx4 %1, %2, %3 offen" XAUX " lds" ::"s"(lds), "v"(voff), "s"(rs), "s"(soff) : "memory");
}
DI void dma4(u32 lds, u32 voff, rsrc_t rs) {
  asm volatile("s_mov_b32 m0, %0\n\ts_nop 0\n\tbuffer_load_dword %1, %2, 0 offen lds" ::"s"(lds), "v"(voff), "s"(rs) : "memory");
}
DI rsrc_t mk(const void* p, u32 bytes) { return __builtin_amdgcn_make_buffer_rsrc((void*)p, 0, (int)bytes, 0x00020000); }
DI v4u lds128(u32 a) { return *(const LAS v4u*)(size_t)a; }
DI v2u ldstr(u32 a) { return __builtin_bit_cast(v2u, __builtin_amdgcn_ds_read_tr16_b64_v4i16((LAS s16x4*)(size_t)a)); }
DI v4u ldstr2(u32 a0, u32 a1) { v2u x = ldstr(a0), y = ldstr(a1); return v4u{x.x, x.y, y.x, y.y}; }
DI f32x4 mfma(v4u a, v4u b, f32x4 c) {
  return __builtin_amdgcn_mfma_f32_16x16x32_bf16(__builtin_bit_cast(bf16x8, a), __builtin_bit_cast(bf16x8, b), c, 0, 0, 0);
}
DI void mfma_ip(f32x4& c, v4u a, v4u b) { asm volatile("v_mfma_f32_16x16x32_bf16 %0, %1, %2, %0" : "+v"(c) : "v"(a), "v"(b)); }
DI u32 pk(float a, float b) { f32x2 f = {a, b}; return __builtin_bit_cast(u32, __builtin_convertvector(f, bf16x2)); }
DI float asf(u32 x) { return __builtin_bit_cast(float, x); }
DI u32 asu(float x) { return __builtin_bit_cast(u32, x); }
DI float lo16(u32 x) { return asf(x << 16); }
DI float hi16(u32 x) { return asf(x & 0xFFFF0000u); }
DI float gelu(float x) {
  const float c0 = -2.0f * 0.7978845608028654f * 1.4426950408889634f, c1 = c0 * 0.044715f;
  float e = __builtin_amdgcn_exp2f(x * __builtin_fmaf(x * x, c1, c0));
  return x * __builtin_amdgcn_rcpf(1.0f + e);
}
DI float tanh_(float x) {
  float e = __builtin_amdgcn_exp2f(x * 2.8853900817779268f);
  return __builtin_fmaf(-2.0f, __builtin_amdgcn_rcpf(1.0f + e), 1.0f);
}
DI v4u pack2(f32x4 a, f32x4 b) { return v4u{pk(a.x, a.y), pk(a.z, a.w), pk(b.x, b.y), pk(b.z, b.w)}; }

DI void st_sc1(v4u v, rsrc_t rs, u32 off) { __builtin_amdgcn_raw_buffer_store_b128(v, rs, off, 0, 16); }
template <int AUX = 16> DI v4u ld16(rsrc_t rs, u32 off) { return __builtin_amdgcn_raw_buffer_load_b128(rs, off, 0, AUX); }
template <int AUX = 16> DI u32 ldck(rsrc_t rs, u32 off) { return __builtin_amdgcn_raw_buffer_load_b32(rs, off, WS_PL, AUX); }
DI u32 ckoff(u32 off) { return (off >> 4) & ~3u; }
DI u32 ck4(v4u d) { return (d.x + __builtin_rotateleft32(d.y, 7)) + __builtin_rotateleft32(d.z, 15) + __builtin_rotateleft32(d.w, 23); }
DI u32 groupsum(u32 c) {
  const auto r = __builtin_amdgcn_permlane32_swap(c, c, false, false);
  c = r[0] + r[1];
  const auto q = __builtin_amdgcn_permlane16_swap(c, c, false, false);
  return q[0] + q[1];
}
DI u32 quadsum(u32 c) {
  c += __builtin_amdgcn_update_dpp(0, (int)c, 0xB1, 0xf, 0xf, false);
  c += __builtin_amdgcn_update_dpp(0, (int)c, 0x4E, 0xf, 0xf, false);
  return c;
}
DI void put(v4u d, rsrc_t rs, u32 off, u32 key) {
  st_sc1(d, rs, off);
  __builtin_amdgcn_raw_buffer_store_b32(groupsum(ck4(d)) + key, rs, (off & 48) ? OOB : off >> 4, WS_PL, 16);
}
DI f32x2 red4(v4u d) {
  const auto r0 = __builtin_amdgcn_permlane32_swap(d.x, d.z, false, false), r1 = __builtin_amdgcn_permlane32_swap(d.y, d.w, false, false);
  const float s0 = lo16(r0[0]) + lo16(r0[1]), s1 = hi16(r0[0]) + hi16(r0[1]), s2 = lo16(r1[0]) + lo16(r1[1]), s3 = hi16(r1[0]) + hi16(r1[1]);
  const auto q0 = __builtin_amdgcn_permlane16_swap(asu(s0), asu(s2), false, false);
  const auto q1 = __builtin_amdgcn_permlane16_swap(asu(s1), asu(s3), false, false);
  return f32x2{asf(q0[0]) + asf(q0[1]), asf(q1[0]) + asf(q1[1])};
}

DI void mark(u32 a, u32 v) { asm volatile("s_mov_b64 exec, 1\n\tds_write_b32 %0, %1\n\ts_mov_b64 exec, -1" ::"v"(a), "v"(v) : "memory"); }
template <u32 NEED> DI void marks_wait(u32 va) {
  u32 cnt, hv;
  asm volatile("s_mov_b32 %1, %4\n"
               "Lr%=:\n\tds_read_b32 %0, %2\n\ts_waitcnt lgkmcnt(0)\n\tv_cmp_gt_u32 vcc, %3, %0\n\ts_cbranch_vccz Ld%=\n\t"
               "s_sub_u32 %1, %1, 1\n\ts_cbranch_scc0 Lr%=\n"
               "Ld%=:"
               : "=&v"(hv), "=&s"(cnt) : "v"(va), "i"(NEED), "i"(LSPIN_CAP) : "memory", "scc", "vcc");
}
template <int N, u32 STEP, u32 BARS> DI void rounds(u32& m, u32& so, int& pv, u32 va, u32 vb, rsrc_t rs) {
  u32 cnt;
  asm volatile("s_mov_b32 %3, %7\n"
               "L%=:\n\ts_mov_b32 m0, %0\n\ts_add_u32 %0, %0, 0x1000\n\tbuffer_load_dwordx4 %4, %6, %1 offen lds\n\t"
               "s_mov_b32 m0, %0\n\ts_add_u32 %0, %0, 0x1000\n\tbuffer_load_dwordx4 %5, %6, %1 offen lds\n\t"
               "s_add_u32 %1, %1, %10\n\t.if %9\n\ts_add_i32 %2, %2, 1\n\ts_bitcmp0_b32 %9, %2\n\t.endif\n\ts_waitcnt vmcnt(%8)\n\t"
               ".if %9\n\ts_cbranch_scc1 Lq%=\n\ts_barrier\n"
               "Lq%=:\n\t.endif\n\ts_sub_u32 %3, %3, 1\n\ts_cbranch_scc0 L%="
               : "+s"(m), "+s"(so), "+s"(pv), "=&s"(cnt) : "v"(va), "v"(vb), "s"(rs), "i"(N - 1), "i"(2 * LOOK), "i"(BARS), "i"(STEP) : "memory", "scc");
}

#define PAD(k) asm volatile("PAD%c0:\n\ts_cbranch_execz RET%c1" ::"i"(k), "i"((k) / 8))
#define HOP "s_mov_b64 exec, 0\n\ts_getpc_b64 vcc\n\ts_add_u32 vcc_lo, vcc_lo, %0\n\ts_addc_u32 vcc_hi, vcc_hi, 0\n\ts_setpc_b64 vcc\n\t"
DI u32 warm0(u32 wv, const void* p) {
  u32 e, o = wv * 4 + 12;
  asm volatile("s_load_dword %1, %2, 0x0\n\t" HOP
               "s_branch PAD0\n\ts_branch PAD1\n\ts_branch PAD2\n\ts_branch PAD3\n\ts_branch PAD4\n\ts_branch PAD5\n\ts_branch PAD6\n\ts_branch PAD7\n"
               "RET0:\n\ts_mov_b64 exec, -1\n\ts_waitcnt lgkmcnt(0)"
               : "+s"(o), "=&s"(e) : "s"(p) : "memory", "scc", "vcc");
  return e;
}
template <int S> DI void warm(u32 wv) {
  asm volatile(HOP
               "s_branch PAD%c2\n\ts_branch PAD%c3\n\ts_branch PAD%c4\n\ts_branch PAD%c5\n\ts_branch PAD%c6\n\ts_branch PAD%c7\n\ts_branch PAD%c8\n\ts_branch PAD%c9\n"
               "RET%c1:\n\ts_mov_b64 exec, -1"
               :: "s"(wv * 4 + 12), "i"(S), "i"(8 * S), "i"(8 * S + 1), "i"(8 * S + 2), "i"(8 * S + 3), "i"(8 * S + 4), "i"(8 * S + 5), "i"(8 * S + 6), "i"(8 * S + 7) : "memory", "scc", "vcc");
}

#define STAMP(i)

struct Args { const u16 *x, *w1, *w2, *gp, *wf, *wg, *wo; u16* out; unsigned char* ws; };

__global__ void __launch_bounds__(768, 3) rnnt_fused(Args unused) {
  __shared__ __attribute__((aligned(16))) unsigned char smem[LDS_BYTES];
  const u32 LB = (u32)(size_t)(LAS unsigned char*)smem;
  const u32 tid = threadIdx.x;
  v8q ka; u64 kws, pc;
  asm volatile("s_load_dwordx16 %0, %3, 0x0\n\ts_load_dwordx2 %1, %3, 0x40\n\ts_getpc_b64 %2" : "=&s"(ka), "=&s"(kws), "=&s"(pc) : "s"(__builtin_amdgcn_kernarg_segment_ptr()) : "memory");
  asm volatile("s_mov_b32 m0, %1\n\ts_nop 0\n\tglobal_load_lds_dword %0, %2" :: "v"(min(((blockIdx.x >> 3) * 1536 + tid * 2) & 0x3f80, ((u32)CODE_LINES - 1) << 7)), "s"(LB + L_JUNK), "s"(pc) : "memory");
  const u32 wq = __builtin_amdgcn_readfirstlane(tid >> 6), w4 = wq & 3, wv = wq - 4;
  const u32 bid = blockIdx.x, x8 = bid & 7, j = bid >> 3, mt = j >> 1, mloc = mt & 7, bat = mt >> 3;
  const u32 k1 = x8 >> 1, n1 = 2 * (x8 & 1) + (j & 1);
  u32 lane = tid & 63;
  const u32 wbase = k1 * 32768 + n1 * 128;
  u32 w1v = ((tid >> 3) & 31) * 512 + wbase + (((tid & 7) ^ (((tid >> 3) & 2) | ((tid >> 4) & 4))) * 16);
  u32 xv = ((tid >> 3) & 31) * 512 + (mloc * 65536 - 3584 + k1 * 128) + (tid & 7) * 16;
  u32 m = LB + L_RING + w4 * 1024, mx = LB + L_X + w4 * 1056, nx = min(mloc * 128 + 137, 1000u) * 512, xo = bat * 512000;
  u32 tv = lane < 7 ? (1 + (wv >> 2) * 7 + lane) * 131072 + wbase + (mt * 4 + w4) * 512 : OOB;
  asm volatile("" : "+v"(w1v), "+v"(xv), "+v"(tv));
  asm volatile("s_waitcnt lgkmcnt(0)" : "+s"(ka), "+s"(kws), "+s"(m), "+s"(mx), "+s"(nx), "+s"(xo)::"memory");
  const Args a{(const u16*)ka[0], (const u16*)ka[1], (const u16*)ka[2], (const u16*)ka[3], (const u16*)ka[4], (const u16*)ka[5], (const u16*)ka[6], (u16*)ka[7], (unsigned char*)kws};
  const rsrc_t rw1 = mk(a.w1, 15 * 131072), rws = mk(a.ws, WS_BYTES);
  asm volatile("s_cmp_lt_u32 %0, 4\n\ts_cbranch_scc1 L%=\n\ts_sleep %3\n\tbuffer_load_dword %1, %2, 0 offen" XAUX " lds\n\ts_barrier\n"
               "L%=:" :: "s"(wq), "v"(tv), "s"(rw1), "i"(TDLY) : "memory", "scc");
  const u32 c2 = x8, n2 = j & 1;
  const u32 pi = x8 * 32 + j, ploc = pi & 127, bat3 = pi >> 7;
  const bool p3ok = ploc < 125;
#define DUMP()

  if (__builtin_expect(wq < 4, 1)) {
    {
      const rsrc_t rx = mk((const void*)(ka[0] + xo), nx);
      static_for<0, 4>([&](auto I) { dma16x(mx + I * 4224, xv + I * 16384, rx, 0); });
      if (w4 < 2) dma16x(mx + 4 * 4224, xv + 65536, rx, 0);
      dma16x(m, w1v, rw1, 0);
      dma16x(m + 4096, w1v, rw1, 16384);
    }
    bar_raw();
    __builtin_amdgcn_s_setprio(SPRIO);
    STAMP(1);
    const u32 m0 = m;
    m += 8192;
    u32 so = 131072;
    int pv = 1 - LOOK;
    constexpr u32 BARS = (1u << 1) | (1u << 4) | (1u << 8) | (1u << 12);
    rounds<LOOK - 1, 131072, BARS>(m, so, pv, w1v, w1v + 16384, rw1);
    lane = opq(lane);
    const u32 g = lane >> 4, l15 = lane & 15, q4 = l15 >> 2, rr8 = lane >> 3, r2 = 2 * w4 + (lane >> 5);
    const u32 w2v = opq((c2 * 32 + w4 * 4 + g) * 512 + n2 * 256 + ((((lane >> 1) & 7) ^ (g | (((w4 >> 1) & 1) << 2))) * 32) + (lane & 1) * 16), w2w = opq(w2v + 8192);
    const u32 wfv = opq(r2 * 512 + ((((lane >> 1) & 15) ^ (r2 & 7)) * 32) + (lane & 1) * 16), wfw = opq(wfv + 4096);
    const u32 wov = opq(rr8 * 512 + w4 * 128 + ((((lane >> 1) & 3) ^ (rr8 >> 1)) * 32) + (lane & 1) * 16), wow = opq(wov + 4096);
    u32 wor[4];
    static_for<0, 4>([&](auto T) { wor[T] = opq(m0 + (g >> 1) * 4096 + (4 * (g & 1) + q4) * 128 + ((T ^ (2 * (g & 1) + (q4 >> 1))) * 32) + (lane & 3) * 8); });
    u32 tq;
    {
      const u32 ln = mt * 60 + w4 * 30 + lane;
      const u32 t2 = lane < 30 ? (ln >> 6) * 131072 + (c2 * 32 + ((ln >> 1) & 31)) * 512 + n2 * 256 + (ln & 1) * 128 : OOB;
      tq = opq(w4 < 2 ? t2 : (lane < 32 ? (j * 32 + lane) * 128 : OOB));
    }
    const rsrc_t rt = mk(w4 < 2 ? a.w2 : (w4 == 2 ? a.wo : a.wf), w4 < 2 ? 15 * 131072 : 131072);
    const u32 jr = opq(LB + L_J + lane * 16);
    const u32 ov = opq((((bat3 * 250 + 2 * ploc) * 8 + l15) * 256 + w4 * 64 + (g & 1) * 16 + (g >> 1) * 8) * 2);
    if (ploc >= 126) mark(LB + L_KEY + 16 + w4 * 4, 0);
    rounds<15 - LOOK, 131072, BARS>(m, so, pv, w1v, w1v + 16384, rw1);
    STAMP(2);
    if (ploc >= 126) {
      PAD(6);
      const u32 wgv = w1v - wbase + (bat3 * 2 + (pi & 1)) * 128;
      static_for<0, 2>([&](auto R) { dma16(m0 + 32768 + R * 4096, (r2 + 8 * R) * 512 + (((lane & 31) ^ (r2 + 8 * R)) * 16), mk(a.gp, 8192), 0); });
      wait_vm<4>(); bar_raw();
      static_for<0, 4>([&](auto R) { dma16(m0 + R * 4096, wgv, mk(a.wg, 131072), R * 16384); });
      wait_vm<6>(); bar_raw();
      static_for<4, 8>([&](auto R) { dma16(m0 + R * 4096, wgv, mk(a.wg, 131072), R * 16384); });
    }
    dma4(LB + L_JUNK, tq, rt);
    if (ploc < 126) { wait_vm<3>(); bar_raw(); wait_vm<1>(); bar_raw(); }
    else wait_vm<1>();
    bar_raw();
    if (ploc >= 126) {
      PAD(7);
      const u32 gr = LB + L_RING + g * 1024 + q4 * 128 + ((w4 ^ ((q4 >> 1) + 2 * (g & 1))) * 32) + (lane & 3) * 8;
      v4u wa[8], gb[8];
      static_for<0, 8>([&](auto K) { gb[K] = lds128(LB + L_RING + 32768 + l15 * 512 + (((K * 4 + g) ^ l15) * 16)); });
      static_for<0, 8>([&](auto K) { wa[K] = ldstr2(gr + K * 4096, gr + K * 4096 + 512); });
      mark(LB + L_KEY + 16 + w4 * 4, 1);
      fence_sched();
      f32x4 d = {0, 0, 0, 0};
      static_for<0, 8>([&](auto K) { d = mfma(wa[K], gb[K], d); });
      put(__builtin_bit_cast(v4u, d), rws, WS_GE + (l15 >> 3) * 8192 + ((bat3 * 2 + (pi & 1)) * 4 + w4) * 512 + (l15 & 7) * 64 + g * 16, *(const volatile LAS u32*)(size_t)(LB + L_KEY));
      marks_wait<1>(LB + L_KEY + 16 + (lane & 3) * 4);
      wait_vm<2>();
    } else wait_vm<0>();
    STAMP(3);
    const rsrc_t rw2 = mk(a.w2, 15 * 131072), rwf = mk(a.wf, 131072), rwo = mk(a.wo, 131072);
    m = m0; so = 0;
    rounds<NA, 131072, 0>(m, so, pv, w2v, w2w, rw2);
    wait_vm<0>();
    STAMP(4);
    bar_raw();
    nap<GDLY>();
    rounds<15 - NA, 131072, 0>(m, so, pv, w2v, w2w, rw2);
    if (!p3ok) {
      wait_vm<0>();
      bar_raw();
      if (pi == 125 && w4 == 0) {
        u32 epoch;
        asm volatile("s_load_dword %0, %1, 0x0\n\ts_waitcnt lgkmcnt(0)" : "=s"(epoch) : "s"(a.ws) : "memory");
        epoch += 1;
        for (u32 spins = 0; spins < SPIN_CAP; ++spins) {
          v4u f4;
          asm volatile("buffer_load_dwordx4 %0, %1, %2, 0 offen sc1\n\ts_waitcnt vmcnt(0)" : "=v"(f4) : "v"(WS_FLAGS + (lane >> 2) * 128 + (lane & 3) * 16), "s"(rws) : "memory");
          if (__all(f4.x == epoch && f4.y == epoch && f4.z == epoch && f4.w == epoch)) break;
          __builtin_amdgcn_s_sleep(8);
        }
        if (lane == 0) __hip_atomic_store((GAS u32*)(a.ws + WS_EPOCH), epoch, __ATOMIC_RELAXED, __HIP_MEMORY_SCOPE_AGENT);
      }
      DUMP();
      return;
    }
    dma16(m0 + 15 * 8192, wfv, rwf, 15 * 8192); dma16(m0 + 15 * 8192 + 4096, wfw, rwf, 15 * 8192);
    static_for<0, 2>([&](auto R) { dma16(m0 + L_O + R * 8192, wov, rwo, R * 8192); dma16(m0 + L_O + R * 8192 + 4096, wow, rwo, R * 8192); });
    wait_vm<6>();
    STAMP(5);
    bar_raw();
    m = m0; so = 0;
    rounds<NA, 8192, 0>(m, so, pv, wfv, wfw, rwf);
    v2u WO[16][4];
    static_for<0, 2>([&](auto R) { static_for<0, 4>([&](auto T) { WO[R][T] = ldstr(wor[T] + L_O + R * 8192); }); });
    wait_vm<0>();
    STAMP(6);
    bar();
    rounds<15 - NA, 8192, 0>(m, so, pv, wfv, wfw, rwf);
    static_for<2, 4>([&](auto R) { dma16(m0 + L_O + (R - 2) * 8192, wov, rwo, R * 8192); dma16(m0 + L_O + (R - 2) * 8192 + 4096, wow, rwo, R * 8192); });
    wait_vm<0>();
    STAMP(7);
    bar_raw();
    static_for<2, 4>([&](auto R) { static_for<0, 4>([&](auto T) { WO[R][T] = ldstr(wor[T] + L_O + (R - 2) * 8192); }); });
    nap<GDLY>();
    {
      auto iss = [&](auto R) {
        if constexpr (R >= 12) asm volatile("s_waitcnt lgkmcnt(8)" ::: "memory");
        dma16(m0 + ((R - 4) & 7) * 8192, wov, rwo, R * 8192); dma16(m0 + ((R - 4) & 7) * 8192 + 4096, wow, rwo, R * 8192);
      };
      PAD(21);
      static_for<4, 7>(iss);
      static_for<4, 16>([&](auto R) {
        if constexpr (R == NW) bar_raw();
        wait_vm<(R + 2 < 16 ? 4 : 2 * (15 - R))>();
        if constexpr (R + 3 < 16) iss(IC<R + 3>{});
        static_for<0, 4>([&](auto T) { WO[R][T] = ldstr(wor[T] + ((R - 4) & 7) * 8192); });
        if constexpr (R == 10) PAD(22);
        fence_sched();
      });
    }
    STAMP(8);
    bar_raw();
    STAMP(9);
    {
      PAD(23);
      f32x4 d[4] = {{0, 0, 0, 0}, {0, 0, 0, 0}, {0, 0, 0, 0}, {0, 0, 0, 0}};
      v4u b[8];
      static_for<0, 4>([&](auto K) { b[K] = lds128(jr + K * 1024); });
      fence_sched();
      static_for<0, 8>([&](auto K) {
        static_for<0, 4>([&](auto T) { d[T] = mfma(v4u{WO[2 * K][T].x, WO[2 * K][T].y, WO[2 * K + 1][T].x, WO[2 * K + 1][T].y}, b[K], d[T]); });
        if constexpr (K < 4) b[K + 4] = lds128(jr + (K + 4) * 1024);
        fence_sched();
      });
      const rsrc_t ro = mk(a.out, 2048000);
      static_for<0, 2>([&](auto H) {
        const f32x4 d0 = d[2 * H], d1 = d[2 * H + 1];
        const auto r0 = __builtin_amdgcn_permlane16_swap(pk(d0.x, d0.y), pk(d1.x, d1.y), false, false);
        const auto r1 = __builtin_amdgcn_permlane16_swap(pk(d0.z, d0.w), pk(d1.z, d1.w), false, false);
        __builtin_amdgcn_raw_buffer_store_b128(v4u{r0[0], r1[0], r0[1], r1[1]}, ro, ov + H * 64, 0, 16);
      });
    }
    STAMP(14);
  } else {
    const u32 p1off = WS_P1 + k1 * 524288 + (2 * n1 + (w4 >> 1)) * 65536 + mt * 4096 + (w4 & 1) * 2048;
    if ((x8 & 3) == 3 && lane < 3) {
      const u32 q = j * 8 + wv;
      const unsigned char* p = lane < 2 ? (const unsigned char*)a.wg + q * 512 + bat3 * 256 + lane * 128 : (const unsigned char*)a.gp + (q & 63) * 128;
      asm volatile("s_mov_b32 m0, %1\n\ts_nop 0\n\tglobal_load_lds_dword %0, off" ::"v"(p), "s"(LB + L_JUNK) : "memory");
    }
    const u32 epoch = warm0(wv, a.ws) + 1, key = epoch * 0x9E3779B1u | 1;
    STAMP(1);
    const u32 l15 = lane & 15, g = lane >> 4, q4 = l15 >> 2, p4 = lane & 3;
    {
      PAD(0);
      const u32 pw = wv >> 2;
      if (pw) __builtin_amdgcn_s_setprio(BPRIO);
      f32x4 acc[4] = {{0, 0, 0, 0}, {0, 0, 0, 0}, {0, 0, 0, 0}, {0, 0, 0, 0}};
      v4u G[2][11];
      v2u Wf[2][4];
      const u32 xr = opq(LB + L_X + l15 * 1056 + g * 16 + pw * 128);
      const u32 w1r = opq(LB + L_RING + pw * 8192 + g * 1024 + q4 * 128 + ((w4 ^ ((q4 >> 1) + 2 * (g & 1))) * 32) + p4 * 8), w1r2 = opq(w1r + 65536);
      const u32 p1v = opq(p1off + l15 * 64 + g * 16), p1c = opq(lane < 32 ? (p1off >> 4) + lane * 4 : OOB);
      const u32 ex = opq(LB + (w4 < 2 ? L_RING + 15 * 8192 : L_J - 8192) + w4 * 4096 + lane * 16);
      auto rdw1 = [&](auto S, auto M) { Wf[S & 1][M] = ldstr((S < 4 ? w1r : w1r2) + (S & 3) * 16384 + (M >> 1) * 4096 + (M & 1) * 512); };
      auto rdx = [&](auto C, auto U) { G[C][U] = lds128(xr + (U >> 2) * 1056 + (U & 3) * 256 + C * 64); };
      bar_raw();
      STAMP(2);
      static_for<0, 2>([&](auto C) { static_for<0, 4>([&](auto U) { rdx(C, U); }); });
      bar_raw();
      static_for<0, 4>([&](auto M) { rdw1(IC<0>{}, M); });
      asm volatile("s_nop 1" : "+v"(acc[0]), "+v"(acc[1]), "+v"(acc[2]), "+v"(acc[3]));
      fence_sched();
      f32x4 oth[4];
      auto step = [&](auto S) {
        if constexpr (S % 3 == 0) PAD(1 + S / 3);
        if constexpr (S == 1 || S == 3 || S == 5) bar_raw();
        static_for<0, 8>([&](auto M) {
          constexpr int C = M >> 2, J = M & 3, W0 = S == 6 ? 4 : 0, X0 = S == 6 ? 0 : 4;
          const v2u wa = Wf[S & 1][2 * C], wb = Wf[S & 1][2 * C + 1];
          mfma_ip(acc[J], v4u{wa.x, wa.y, wb.x, wb.y}, G[C][J + S]);
          fence_sched();
          if constexpr (S == 6 && M == 3) bar_raw();
          if constexpr (S == 7 && M == 4) bar_raw();
          if constexpr (S < 7 && M >= W0 && M < W0 + 4) rdw1(IC<S + 1>{}, IC<M - W0>{});
          if constexpr (S < 7 && M >= X0 && M < X0 + 2) rdx(IC<M - X0>{}, IC<S + 4>{});
          if constexpr (S == 7 && M >= 4) oth[M - 4] = *(const LAS f32x4*)(size_t)(ex + (M - 4) * 1024);
          fence_sched();
        });
      };
      static_for<0, 7>(step);
      asm volatile("s_nop 6" ::: "memory");
      fence_sched();
      if (pw) {
        static_for<0, 4>([&](auto J) { *(LAS f32x4*)(size_t)(ex + J * 1024) = acc[J]; });
        if (w4 == 0) mark(LB + L_KEY, key);
        bar();
        STAMP(3);
        __builtin_amdgcn_raw_buffer_store_b32(epoch, rws, tid == 512 ? WS_FLAGS + (bid >> 4) * 128 + (bid & 15) * 4 : OOB, 0, 16);
      } else {
        PAD(4);
        step(IC<7>{});
        asm volatile("s_nop 6" ::: "memory");
        fence_sched();
        STAMP(3);
        const v4u d0 = pack2(acc[0] + oth[0], acc[1] + oth[1]), d1 = pack2(acc[2] + oth[2], acc[3] + oth[3]);
        st_sc1(d0, rws, p1v);
        st_sc1(d1, rws, p1v + 1024);
        const auto r = __builtin_amdgcn_permlane16_swap(ck4(d0), ck4(d1), false, false);
        const u32 c = r[0] + r[1];
        const auto q = __builtin_amdgcn_permlane32_swap(c, c, false, false);
        __builtin_amdgcn_raw_buffer_store_b32(q[0] + q[1] + key, rws, p1c, WS_PL, 16);
      }
    }
    STAMP(4);
    PAD(5);
    v4u W2[15];
    const u32 w2r = opq(LB + L_RING + (8 * g + q4) * 256 + ((wv ^ (q4 | ((g & 1) << 2))) * 32) + p4 * 8), w2r2 = opq(w2r + 65536);
    auto rdw2 = [&](auto T) { const u32 ad = (T < 8 ? w2r : w2r2) + (T & 7) * 8192; W2[T] = ldstr2(ad, ad + 1024); };
    v4u hp[3];
    u32 rho, rhoh, h1off[3], h1c, h1k, hc;
    {
      const u32 blk = wv >> 1, mm = (wv & 1) * 8 + q4;
      const u32 hb = WS_P1 + g * 524288 + c2 * 65536;
      rho = 8 + 4 * mm + 2 * (blk & 1);
      h1off[0] = opq(mloc * 64 + rho - 8 < 500 ? hb + mt * 4096 + wv * 512 + l15 * 16 : OOB);
      h1off[1] = opq(mloc * 64 + rho + 8 < 500 ? hb + mt * 4096 + wv * 512 + 256 + l15 * 16 : OOB);
      rho = opq(LB + L_X + (rho + (lane >> 5)) * 64 + (rho >> 2) * 32 + (blk >> 1) * 32 + p4 * 8 + (g & 1) * 4);
      const u32 nxt = l15 >> 3;
      rhoh = 72 * nxt + 4 * (q4 & 1) + 2 * (w4 & 1);
      const int hrow = (int)(mloc * 64 + rhoh) - 8;
      h1off[2] = opq((wv >= 4 && hrow >= 0 && hrow < 500) ? hb + (mt + 2 * nxt - 1) * 4096 + w4 * 1024 + (nxt ? 0 : 896) + (lane & 7) * 16 : OOB);
      rhoh = opq(LB + L_X + (rhoh + (lane >> 5)) * 64 + (rhoh >> 2) * 32 + (w4 >> 1) * 32 + p4 * 8 + (g & 1) * 4);
      const u32 sel = p4 == 0 ? h1off[0] : (p4 == 1 ? h1off[1] : (p4 == 2 ? h1off[2] : OOB));
      h1c = opq(ckoff(sel)); h1k = opq(sel == OOB ? 0 : key);
    }
    const u32 ar = opq(LB + L_X + l15 * 288 + g * 16), p2v = opq(WS_P2 + c2 * 262144 + mt * 16384 + n2 * 8192 + wv * 1024 + l15 * 64 + g * 16);
    auto h1_load = [&]() {
      static_for<0, 2>([&](auto K) { hp[K] = ld16(rws, h1off[K]); });
      if (wv >= 4) hp[2] = ld16(rws, h1off[2]);
      hc = ldck(rws, h1c);
      mem_order();
    };
    hp[2] = v4u{0, 0, 0, 0};
    nap<DLY1>();
    bar_raw();
    STAMP(5);
    h1_load();
    warm<1>(wv);
    static_for<0, NA>(rdw2);
    STAMP(6);
    PAD(8);
    u32 tries = 0;
    for (;; ++tries) {
      const u32 c0 = quadsum(ck4(hp[0])), c1 = quadsum(ck4(hp[1])), ch = quadsum(ck4(hp[2]));
      const u32 c = p4 == 0 ? c0 : (p4 == 1 ? c1 : (p4 == 2 ? ch : 0));
      if (__all(c + h1k == hc) || tries >= SPIN_CAP) break;
      __builtin_amdgcn_s_sleep(2);
      h1_load();
    }
    STAMP(7);
    PAD(9);
    {
      const f32x2 s0 = red4(hp[0]), s1 = red4(hp[1]);
      *(LAS u32*)(size_t)(rho) = pk(gelu(s0.x), gelu(s0.y));
      *(LAS u32*)(size_t)(rho + 4 * 288) = pk(gelu(s1.x), gelu(s1.y));
      PAD(10);
      if (wv >= 4) {
        const f32x2 s2 = red4(hp[2]);
        *(LAS u32*)(size_t)(rhoh) = pk(gelu(s2.x), gelu(s2.y));
      }
    }
    STAMP(8);
    PAD(11);
    bar();
    STAMP(9);
    {
      f32x4 a0 = {0, 0, 0, 0}, a1 = {0, 0, 0, 0};
      v4u A[17];
      static_for<0, AW>([&](auto Q) { A[Q] = lds128(ar + ((Q + 1) >> 2) * 288 + ((Q + 1) & 3) * 64); });
      fence_sched();
      static_for<0, 15>([&](auto T) {
        a0 = mfma(W2[T], A[T], a0);
        a1 = mfma(W2[T], A[T + 2], a1);
        if constexpr (T + AW < 17) A[T + AW] = lds128(ar + ((T + AW + 1) >> 2) * 288 + ((T + AW + 1) & 3) * 64);
        if constexpr (T + NA < 15) rdw2(IC<T + NA>{});
        if constexpr (T == 8) PAD(12);
        fence_sched();
      });
      put(pack2(a0, a1), rws, p2v, key);
    }
    STAMP(10);
    PAD(13);
    if (!p3ok) { DUMP(); return; }
    v4u ge[2]; u32 gc[2];
    const u32 geoff = WS_GE + bat3 * 8192 + wv * 1024 + (lane & 7) * 64 + g * 16;
    static_for<0, 2>([&](auto T) { ge[T] = ld16<0>(rws, geoff + T * 512); gc[T] = ldck<0>(rws, ckoff(geoff + T * 512)); });
    bar_raw();
    v4u WF[8][2];
    u32 wr[4];
    wr[0] = opq(LB + L_RING + (4 * g + q4) * 512 + (((2 * wv) ^ ((4 * g + q4) & 7)) * 32) + p4 * 8);
    wr[1] = opq(wr[0] ^ 32); wr[2] = opq(wr[0] + 65536); wr[3] = opq(wr[1] + 65536);
    auto rdwf = [&](auto K) { static_for<0, 2>([&](auto T) { const u32 ad = wr[(K >> 2) * 2 + T] + (K & 3) * 16384; WF[K][T] = ldstr2(ad, ad + 8192); }); };
    static_assert(NA >= 8, "slots 0..7 of w_f land before PE");
    static_for<0, 4>(rdwf);
    PAD(14);
    const u32 h2off = opq(WS_P2 + (lane >> 3) * 262144 + (pi >> 4) * 16384 + (wv * 2 + ((lane >> 2) & 1)) * 1024 + (pi & 15) * 64 + p4 * 16), h2c = opq(ckoff(h2off));
    const u32 fw = opq(LB + L_F + (lane >> 5) * 576 + (wv * 32 + p4 * 8 + ((lane >> 2) & 1) * 4 + ((lane >> 3) & 3)) * 2);
    const u32 fr = opq(LB + L_F + (l15 >> 3) * 576 + g * 16), jw = opq(LB + L_J + lane * 16 + wv * 1024);
    for (u32 spins = 0; __any(((groupsum(ck4(ge[0])) + key) ^ gc[0]) | ((groupsum(ck4(ge[1])) + key) ^ gc[1])) && spins < SPIN_CAP; ++spins) {
      __builtin_amdgcn_s_sleep(2);
      static_for<0, 2>([&](auto T) { ge[T] = ld16(rws, geoff + T * 512); gc[T] = ldck(rws, ckoff(geoff + T * 512)); });
      mem_order();
      tries += 65536;
    }
    nap<DLY2>();
    PAD(15);
    bar();
    STAMP(11);
    {
      v4u fp = ld16(rws, h2off);
      u32 fc = ldck(rws, h2c);
      mem_order();
      warm<2>(wv);
      static_for<4, 8>(rdwf);
      PAD(16);
      for (u32 spins = 0; __any(quadsum(ck4(fp)) + key != fc) && spins < SPIN_CAP; ++spins) {
        __builtin_amdgcn_s_sleep(2);
        fp = ld16(rws, h2off);
        fc = ldck(rws, h2c);
        mem_order();
        tries += 256;
      }
      STAMP(12);
      PAD(17);
      const f32x2 s = red4(fp);
      const float snd = (lane & 8) ? s.x : s.y, kp = (lane & 8) ? s.y : s.x;
      const float tot = kp + asf(__builtin_amdgcn_update_dpp(0, (int)asu(snd), 0x128, 0xf, 0xf, false));
      *(LAS u16*)(size_t)(fw) = (u16)pk(gelu(tot), 0.f);
    }
    PAD(18);
    bar();
    {
      f32x4 d0 = __builtin_bit_cast(f32x4, ge[0]), d1 = __builtin_bit_cast(f32x4, ge[1]);
      v4u b[8];
      static_for<0, 8>([&](auto K) { b[K] = lds128(fr + K * 64); });
      fence_sched();
      PAD(19);
      static_for<0, 8>([&](auto K) { d0 = mfma(WF[K][0], b[K], d0); d1 = mfma(WF[K][1], b[K], d1); });
      fence_sched();
      PAD(20);
      *(LAS v4u*)(size_t)(jw) = v4u{pk(tanh_(d0.x), tanh_(d0.y)), pk(tanh_(d0.z), tanh_(d0.w)), pk(tanh_(d1.x), tanh_(d1.y)), pk(tanh_(d1.z), tanh_(d1.w))};
    }
    STAMP(13);
    bar();
  }
  DUMP();
}

extern "C" void kernel_launch(void* const* d_in, const int* in_sizes, int n_in, void* d_out, int out_size, void* d_ws, size_t ws_size,
                              hipStream_t stream) {
  Args a{(const u16*)d_in[0], (const u16*)d_in[1], (const u16*)d_in[2], (const u16*)d_in[3], (const u16*)d_in[4],
         (const u16*)d_in[5], (const u16*)d_in[6], (u16*)d_out, (unsigned char*)d_ws};
  rnnt_fused<<<256, 768, 0, stream>>>(a);
}
```

```cpp
#define HPROBE 2
#include <hip/hip_runtime.h>
#include <hip/hip_bf16.h>
#include <type_traits>

typedef unsigned u32;
typedef unsigned short u16;
typedef unsigned long long u64;
typedef u32 v4u __attribute__((ext_vector_type(4)));
typedef u32 v2u __attribute__((ext_vector_type(2)));
typedef u64 v8q __attribute__((ext_vector_type(8)));
typedef float f32x4 __attribute__((ext_vector_type(4)));
typedef float f32x2 __attribute__((ext_vector_type(2)));
typedef short s16x4 __attribute__((ext_vector_type(4)));
typedef __bf16 bf16x8 __attribute__((ext_vector_type(8)));
typedef __bf16 bf16x2 __attribute__((ext_vector_type(2)));
typedef __amdgpu_buffer_rsrc_t rsrc_t;
#define GAS __attribute__((address_space(1)))
#define LAS __attribute__((address_space(3)))
#define DI __device__ __forceinline__
template <int N> using IC = std::integral_constant<int, N>;

constexpr u32 WS_EPOCH = 0;
constexpr u32 WS_FLAGS = 4096;
constexpr u32 WS_GE = 65536;
constexpr u32 WS_P1 = 1u << 20;
constexpr u32 WS_P2 = 3u << 20;
constexpr u32 WS_PL = 5u << 20;
constexpr u32 WS_BYTES = 6u << 20;
constexpr u32 OOB = 0x80000000u;
constexpr u32 SPIN_CAP = 100000;
constexpr u32 LSPIN_CAP = 4000000;
constexpr u32 L_RING = 0;
constexpr u32 L_KEY = 131072;
constexpr u32 L_JUNK = L_KEY + 64;
constexpr u32 L_F = L_JUNK + 256;
constexpr u32 L_X = L_F + 1152;
constexpr u32 L_GC = L_X;
constexpr u32 L_O = L_KEY + 8192;
constexpr u32 L_J = L_KEY + 24576;
constexpr u32 LDS_BYTES = L_J + 8192;
static_assert(L_X + 5760 <= L_O && L_X + 19008 <= L_J && LDS_BYTES <= 163840, "lds map");

#define CODE_LINES 108
#define LOOK 3
#define NA 8
#define OA 2
#define OB 2
#define O0 4
#define NW 3
#define SJ2 0
#define TG 1
#define EXL 3
#define FAR 0
#define B2 1
#define ST 1
#define TDLY 6
#define DLY1 6
#define DLY2 6
#define GDLY 4
#define SPRIO 3
#define BPRIO 1
#define AW 5
#define XAUX ""

template <int I, int N, class F> DI void static_for(F&& f) {
  if constexpr (I < N) { f(IC<I>{}); static_for<I + 1, N>(f); }
}
template <int N> DI void wait_vm() { asm volatile("s_waitcnt vmcnt(%0)" ::"i"(N) : "memory"); }
DI void bar() { asm volatile("s_waitcnt lgkmcnt(0)\n\ts_barrier" ::: "memory"); }
DI void bar_raw() { asm volatile("s_barrier" ::: "memory"); }
DI void fence_sched() { __builtin_amdgcn_sched_barrier(0); }
DI void mem_order() { asm volatile("" ::: "memory"); }
DI u32 opq(u32 x) { asm volatile("" : "+v"(x)); return x; }
template <int N> DI void nap() { if constexpr (N > 0) __builtin_amdgcn_s_sleep(N); }
DI void dma16(u32 lds, u32 voff, rsrc_t rs, u32 soff) {
  asm volatile("s_mov_b32 m0, %0\n\ts_nop 0\n\tbuffer_load_dwordx4 %1, %2, %3 offen lds" ::"s"(lds), "v"(voff), "s"(rs), "s"(soff) : "memory");
}
DI void dma16x(u32 lds, u32 voff, rsrc_t rs, u32 soff) {
  asm volatile("s_mov_b32 m0, %0\n\ts_nop 0\n\tbuffer_load_dwordx4 %1, %2, %3 offen" XAUX " lds" ::"s"(lds), "v"(voff), "s"(rs), "s"(soff) : "memory");
}
DI void dma4(u32 lds, u32 voff, rsrc_t rs) {
  asm volatile("s_mov_b32 m0, %0\n\ts_nop 0\n\tbuffer_load_dword %1, %2, 0 offen lds" ::"s"(lds), "v"(voff), "s"(rs) : "memory");
}
DI rsrc_t mk(const void* p, u32 bytes) { return __builtin_amdgcn_make_buffer_rsrc((void*)p, 0, (int)bytes, 0x00020000); }
DI v4u lds128(u32 a) { return *(const LAS v4u*)(size_t)a; }
DI v2u ldstr(u32 a) { return __builtin_bit_cast(v2u, __builtin_amdgcn_ds_read_tr16_b64_v4i16((LAS s16x4*)(size_t)a)); }
DI v4u ldstr2(u32 a0, u32 a1) { v2u x = ldstr(a0), y = ldstr(a1); return v4u{x.x, x.y, y.x, y.y}; }
DI f32x4 mfma(v4u a, v4u b, f32x4 c) {
  return __builtin_amdgcn_mfma_f32_16x16x32_bf16(__builtin_bit_cast(bf16x8, a), __builtin_bit_cast(bf16x8, b), c, 0, 0, 0);
}
DI void mfma_ip(f32x4& c, v4u a, v4u b) { asm volatile("v_mfma_f32_16x16x32_bf16 %0, %1, %2, %0" : "+v"(c) : "v"(a), "v"(b)); }
DI u32 pk(float a, float b) { f32x2 f = {a, b}; return __builtin_bit_cast(u32, __builtin_convertvector(f, bf16x2)); }
DI float asf(u32 x) { return __builtin_bit_cast(float, x); }
DI u32 asu(float x) { return __builtin_bit_cast(u32, x); }
DI float lo16(u32 x) { return asf(x << 16); }
DI float hi16(u32 x) { return asf(x & 0xFFFF0000u); }
DI float gelu(float x) {
  const float c0 = -2.0f * 0.7978845608028654f * 1.4426950408889634f, c1 = c0 * 0.044715f;
  float e = __builtin_amdgcn_exp2f(x * __builtin_fmaf(x * x, c1, c0));
  return x * __builtin_amdgcn_rcpf(1.0f + e);
}
DI float tanh_(float x) {
  float e = __builtin_amdgcn_exp2f(x * 2.8853900817779268f);
  return __builtin_fmaf(-2.0f, __builtin_amdgcn_rcpf(1.0f + e), 1.0f);
}
DI v4u pack2(f32x4 a, f32x4 b) { return v4u{pk(a.x, a.y), pk(a.z, a.w), pk(b.x, b.y), pk(b.z, b.w)}; }

DI void st_sc1(v4u v, rsrc_t rs, u32 off) { __builtin_amdgcn_raw_buffer_store_b128(v, rs, off, 0, 16); }
template <int AUX = 16> DI v4u ld16(rsrc_t rs, u32 off) { return __builtin_amdgcn_raw_buffer_load_b128(rs, off, 0, AUX); }
template <int AUX = 16> DI u32 ldck(rsrc_t rs, u32 off) { return __builtin_amdgcn_raw_buffer_load_b32(rs, off, WS_PL, AUX); }
DI u32 ckoff(u32 off) { return (off >> 4) & ~3u; }
DI u32 ck4(v4u d) { return (d.x + d.y + d.z) + d.w; }
DI u32 groupsum(u32 c) {
  const auto r = __builtin_amdgcn_permlane32_swap(c, c, false, false);
  c = r[0] + r[1];
  const auto q = __builtin_amdgcn_permlane16_swap(c, c, false, false);
  return q[0] + q[1];
}
DI u32 quadsum(u32 c) {
  c += __builtin_amdgcn_update_dpp(0, (int)c, 0xB1, 0xf, 0xf, false);
  c += __builtin_amdgcn_update_dpp(0, (int)c, 0x4E, 0xf, 0xf, false);
  return c;
}
DI u32 putck(u32 off) { return (off & 48) ? OOB : off >> 4; }
DI void put(v4u d, rsrc_t rs, u32 off, u32 coff, u32 key) {
  st_sc1(d, rs, off);
  fence_sched();
  __builtin_amdgcn_raw_buffer_store_b32(groupsum(ck4(d)) + key, rs, coff, WS_PL, 16);
}
DI f32x2 red4(v4u d) {
  const auto r0 = __builtin_amdgcn_permlane32_swap(d.x, d.z, false, false), r1 = __builtin_amdgcn_permlane32_swap(d.y, d.w, false, false);
  const float s0 = lo16(r0[0]) + lo16(r0[1]), s1 = hi16(r0[0]) + hi16(r0[1]), s2 = lo16(r1[0]) + lo16(r1[1]), s3 = hi16(r1[0]) + hi16(r1[1]);
  const auto q0 = __builtin_amdgcn_permlane16_swap(asu(s0), asu(s2), false, false);
  const auto q1 = __builtin_amdgcn_permlane16_swap(asu(s1), asu(s3), false, false);
  return f32x2{asf(q0[0]) + asf(q0[1]), asf(q1[0]) + asf(q1[1])};
}

DI void mark(u32 a, u32 v) { asm volatile("s_mov_b64 exec, 1\n\tds_write_b32 %0, %1\n\ts_mov_b64 exec, -1" ::"v"(a), "v"(v) : "memory"); }
template <u32 NEED> DI void marks_wait(u32 va) {
  u32 cnt, hv;
  asm volatile("s_mov_b32 %1, %4\n"
               "Lr%=:\n\tds_read_b32 %0, %2\n\ts_waitcnt lgkmcnt(0)\n\tv_cmp_gt_u32 vcc, %3, %0\n\ts_cbranch_vccz Ld%=\n\t"
               "s_sub_u32 %1, %1, 1\n\ts_cbranch_scc0 Lr%=\n"
               "Ld%=:"
               : "=&v"(hv), "=&s"(cnt) : "v"(va), "i"(NEED), "i"(LSPIN_CAP) : "memory", "scc", "vcc");
}
template <int N, u32 STEP, u32 BARS> DI void rounds(u32& m, u32& so, int& pv, u32 va, u32 vb, rsrc_t rs) {
  u32 cnt;
  asm volatile("s_mov_b32 %3, %7\n"
               "L%=:\n\ts_mov_b32 m0, %0\n\ts_add_u32 %0, %0, 0x1000\n\tbuffer_load_dwordx4 %4, %6, %1 offen lds\n\t"
               "s_mov_b32 m0, %0\n\ts_add_u32 %0, %0, 0x1000\n\tbuffer_load_dwordx4 %5, %6, %1 offen lds\n\t"
               "s_add_u32 %1, %1, %10\n\t.if %9\n\ts_add_i32 %2, %2, 1\n\ts_bitcmp0_b32 %9, %2\n\t.endif\n\ts_waitcnt vmcnt(%8)\n\t"
               ".if %9\n\ts_cbranch_scc1 Lq%=\n\ts_barrier\n"
               "Lq%=:\n\t.endif\n\ts_sub_u32 %3, %3, 1\n\ts_cbranch_scc0 L%="
               : "+s"(m), "+s"(so), "+s"(pv), "=&s"(cnt) : "v"(va), "v"(vb), "s"(rs), "i"(N - 1), "i"(2 * LOOK), "i"(BARS), "i"(STEP) : "memory", "scc");
}
DI void sl2(const unsigned char* p, u32 o) { asm volatile("s_load_dword s99, %0, %1\n\ts_load_dword s99, %0, %2" ::"s"(p), "s"(o), "s"(o + 64) : "memory", "s99"); }
template <int O> DI void sl1(const unsigned char* p) { asm volatile("s_load_dword s99, %0, %1" ::"s"(p), "i"(O) : "memory", "s99"); }
template <int N> DI void stouch_taps(const unsigned char* p, u32 r) {
  static_for<0, N>([&](auto T) { sl2(p, T * 131072 + ((r + 5 * T) & 31) * 512); });
}
template <int N> DI void stouch_run(const unsigned char* p) { static_for<0, N>([&](auto T) { sl1<T * 64>(p); }); }

#define PAD(k) asm volatile("PAD%c0:\n\ts_cbranch_execz RET%c1" ::"i"(k), "i"((k) / 8))
#define HOP "s_mov_b64 exec, 0\n\ts_getpc_b64 vcc\n\ts_add_u32 vcc_lo, vcc_lo, %0\n\ts_addc_u32 vcc_hi, vcc_hi, 0\n\ts_setpc_b64 vcc\n\t"
DI u32 warm0(u32 wv, const void* p) {
  u32 e, o = wv * 4 + 12;
  asm volatile("s_load_dword %1, %2, 0x0\n\t" HOP
               "s_branch PAD0\n\ts_branch PAD1\n\ts_branch PAD2\n\ts_branch PAD3\n\ts_branch PAD4\n\ts_branch PAD5\n\ts_branch PAD6\n\ts_branch PAD7\n"
               "RET0:\n\ts_mov_b64 exec, -1\n\ts_waitcnt lgkmcnt(0)"
               : "+s"(o), "=&s"(e) : "s"(p) : "memory", "scc", "vcc");
  return e;
}
template <int S> DI void warm(u32 wv) {
  asm volatile(HOP
               "s_branch PAD%c2\n\ts_branch PAD%c3\n\ts_branch PAD%c4\n\ts_branch PAD%c5\n\ts_branch PAD%c6\n\ts_branch PAD%c7\n\ts_branch PAD%c8\n\ts_branch PAD%c9\n"
               "RET%c1:\n\ts_mov_b64 exec, -1"
               :: "s"(wv * 4 + 12), "i"(S), "i"(8 * S), "i"(8 * S + 1), "i"(8 * S + 2), "i"(8 * S + 3), "i"(8 * S + 4), "i"(8 * S + 5), "i"(8 * S + 6), "i"(8 * S + 7) : "memory", "scc", "vcc");
}

#define STAMP(i) do { if (i == HPROBE) { asm volatile("s_memrealtime %0\n\ts_waitcnt lgkmcnt(0)" : "=s"(hp1)::"memory"); } } while (0)

struct Args { const u16 *x, *w1, *w2, *gp, *wf, *wg, *wo; u16* out; unsigned char* ws; };

__global__ void __launch_bounds__(768, 3) rnnt_fused(Args unused) {
  __shared__ __attribute__((aligned(16))) unsigned char smem[LDS_BYTES];
  const u32 LB = (u32)(size_t)(LAS unsigned char*)smem;
  const u32 tid = threadIdx.x;
  u64 hp0, hp1 = 0;
  asm volatile("s_memrealtime %0\n\ts_waitcnt lgkmcnt(0)" : "=s"(hp0)::"memory");
  v8q ka; u64 kws, pc;
  asm volatile("s_load_dwordx16 %0, %3, 0x0\n\ts_load_dwordx2 %1, %3, 0x40\n\ts_getpc_b64 %2" : "=&s"(ka), "=&s"(kws), "=&s"(pc) : "s"(__builtin_amdgcn_kernarg_segment_ptr()) : "memory");
  asm volatile("s_mov_b32 m0, %1\n\ts_nop 0\n\tglobal_load_lds_dword %0, %2" :: "v"(min(((blockIdx.x >> 3) * 1536 + tid * 2) & 0x3f80, ((u32)CODE_LINES - 1) << 7)), "s"(LB + L_JUNK), "s"(pc) : "memory");
  const u32 wq = __builtin_amdgcn_readfirstlane(tid >> 6), w4 = wq & 3, wv = wq - 4;
  const u32 bid = blockIdx.x, x8 = bid & 7, j = bid >> 3, mt = j >> 1, mloc = mt & 7, bat = mt >> 3;
  const u32 k1 = x8 >> 1, n1 = 2 * (x8 & 1) + (j & 1);
  u32 lane = tid & 63;
  const u32 wbase = k1 * 32768 + n1 * 128;
  u32 w1v = ((tid >> 3) & 31) * 512 + wbase + (((tid & 7) ^ (((tid >> 3) & 2) | ((tid >> 4) & 4))) * 16);
  u32 xv = ((tid >> 3) & 31) * 512 + (mloc * 65536 - 3584 + k1 * 128) + (tid & 7) * 16;
  u32 m = LB + L_RING + w4 * 1024, mx = LB + L_X + w4 * 1056, nx = min(mloc * 128 + 137, 1000u) * 512, xo = bat * 512000;
  u32 tv = lane < 7 && (!FAR || wv < 4) ? (1 + (wv >> 2) * 7 + lane) * 131072 + wbase + (mt * 4 + w4) * 512 : OOB;
  asm volatile("" : "+v"(w1v), "+v"(xv), "+v"(tv));
  asm volatile("s_waitcnt lgkmcnt(0)" : "+s"(ka), "+s"(kws), "+s"(m), "+s"(mx), "+s"(nx), "+s"(xo)::"memory");
  const Args a{(const u16*)ka[0], (const u16*)ka[1], (const u16*)ka[2], (const u16*)ka[3], (const u16*)ka[4], (const u16*)ka[5], (const u16*)ka[6], (u16*)ka[7], (unsigned char*)kws};
  const rsrc_t rw1 = mk(a.w1, 15 * 131072), rws = mk(a.ws, WS_BYTES);
  asm volatile("s_cmp_lt_u32 %0, 4\n\ts_cbranch_scc1 L%=\n\ts_sleep %3\n\tbuffer_load_dword %1, %2, 0 offen" XAUX " lds\n\ts_barrier\n"
               "L%=:" :: "s"(wq), "v"(tv), "s"(rw1), "i"(TDLY) : "memory", "scc");
  const u32 c2 = x8, n2 = j & 1;
  const u32 pi = x8 * 32 + j, ploc = pi & 127, bat3 = pi >> 7;
  const bool p3ok = ploc < 125;
#define DUMP()

  if (__builtin_expect(wq < 4, 1)) {
    {
      const rsrc_t rx = mk((const void*)(ka[0] + xo), nx);
      static_for<0, 4>([&](auto I) { dma16x(mx + I * 4224, xv + I * 16384, rx, 0); });
      if (w4 < 2) dma16x(mx + 4 * 4224, xv + 65536, rx, 0);
      dma16x(m, w1v, rw1, 0);
      dma16x(m + 4096, w1v, rw1, 16384);
    }
    bar_raw();
    __builtin_amdgcn_s_setprio(SPRIO);

    const u32 m0 = m;
    m += 8192;
    u32 so = 131072;
    int pv = 1 - LOOK;
    rounds<LOOK - 1, 131072, 2>(m, so, pv, w1v, w1v + 16384, rw1);
    lane = opq(lane);
    const u32 g = lane >> 4, l15 = lane & 15, q4 = l15 >> 2, rr8 = lane >> 3, r2 = 2 * w4 + (lane >> 5);
    const u32 w2v = opq((c2 * 32 + w4 * 4 + g) * 512 + n2 * 256 + ((((lane >> 1) & 7) ^ (g | (((w4 >> 1) & 1) << 2))) * 32) + (lane & 1) * 16), w2w = opq(w2v + 8192);
    const u32 wfv = opq(r2 * 512 + ((((lane >> 1) & 15) ^ (r2 & 7)) * 32) + (lane & 1) * 16), wfw = opq(wfv + 4096);
    const u32 wov = opq(rr8 * 512 + w4 * 128 + ((((lane >> 1) & 3) ^ (rr8 >> 1)) * 32) + (lane & 1) * 16), wow = opq(wov + 4096);
    u32 wor[4];
    static_for<0, 4>([&](auto T) { wor[T] = opq(m0 + (g >> 1) * 4096 + (4 * (g & 1) + q4) * 128 + ((T ^ (2 * (g & 1) + (q4 >> 1))) * 32) + (lane & 3) * 8); });
    const unsigned char* tp = (const unsigned char*)a.w2 + c2 * 16384 + n2 * 256 + (w4 & 1) * 128;
    const u32 tq = opq(lane < 16 ? (j * 32 + (w4 & 1) * 16 + lane) * 128 : OOB);
    const rsrc_t rt = mk(w4 < 2 ? a.wf : a.wo, 131072);
    const u32 jr = opq(LB + L_J + lane * 16);
    const u32 ov = opq((((bat3 * 250 + 2 * ploc) * 8 + l15) * 256 + w4 * 64 + (g & 1) * 16 + (g >> 1) * 8) * 2);
    if (ploc >= 126) mark(LB + L_KEY + 16 + w4 * 4, 0);
    const u32 tr = mt * 2 + (w4 >> 1);
    const unsigned char *tf = (const unsigned char*)a.wf + (j * 4 + w4) * 1024, *to = (const unsigned char*)a.wo + (j * 4 + w4) * 1024;
    if constexpr (FAR) static_for<0, 7>([&](auto T) { sl2((const unsigned char*)a.w1 + wbase + 8 * 131072, T * 131072 + ((mt * 4 + w4 + 9 * T) & 63) * 512); });
    rounds<4, 131072, (B2 ? 6 : 2)>(m, so, pv, w1v, w1v + 16384, rw1);
    if constexpr (ST == 0) stouch_taps<4>(tp, tr);
    bar_raw();
    if constexpr (ST == 1) stouch_taps<4>(tp, tr);
    rounds<4, 131072, 0>(m, so, pv, w1v, w1v + 16384, rw1);
    stouch_taps<3>(tp + 4 * 131072, tr + 3);
    bar_raw();
    rounds<7 - LOOK, 131072, 0>(m, so, pv, w1v, w1v + 16384, rw1);
    static_assert(LOOK == 3, "barriers below");
    bar_raw();
    const rsrc_t rw2 = mk(a.w2, 15 * 131072), rwf = mk(a.wf, 131072), rwo = mk(a.wo, 131072);
    m = m0; so = 0;
    rounds<2, 131072, 0>(m, so, pv, w2v, w2w, rw2);
    bar_raw();
    rounds<1, 131072, 0>(m, so, pv, w2v, w2w, rw2);
    stouch_taps<3>(tp + 8 * 131072, tr);
    bar_raw();
    rounds<EXL, 131072, 0>(m, so, pv, w2v, w2w, rw2);
    bar_raw();
    rounds<5 - EXL, 131072, 0>(m, so, pv, w2v, w2w, rw2);
    stouch_taps<4>(tp + 11 * 131072, tr + 3);
    if (ploc >= 126) {
      PAD(6);
      const u32 wgv = w1v - wbase + (bat3 * 2 + (pi & 1)) * 128;
      static_for<0, 2>([&](auto R) { dma16(m0 + 14 * 8192 + R * 4096, (r2 + 8 * R) * 512 + (((lane & 31) ^ (r2 + 8 * R)) * 16), mk(a.gp, 8192), 0); });
      static_for<0, 8>([&](auto R) { dma16(m0 + 10 * 8192 + R * 4096, wgv, mk(a.wg, 131072), R * 16384); });
    }
    if constexpr (NA > 8) rounds<NA - 8, 131072, 0>(m, so, pv, w2v, w2w, rw2);
    if constexpr (TG == 2) stouch_run<16>(tf);
    if constexpr (!TG) { dma4(LB + L_JUNK, tq, rt); wait_vm<1>(); } else wait_vm<0>();

    bar_raw();
    if (ploc >= 126) {
      PAD(7);
      const u32 gr = LB + L_RING + 10 * 8192 + g * 1024 + q4 * 128 + ((w4 ^ ((q4 >> 1) + 2 * (g & 1))) * 32) + (lane & 3) * 8;
      v4u wa[8], gb[8];
      static_for<0, 8>([&](auto K) { gb[K] = lds128(LB + L_RING + 14 * 8192 + l15 * 512 + (((K * 4 + g) ^ l15) * 16)); });
      static_for<0, 8>([&](auto K) { wa[K] = ldstr2(gr + K * 4096, gr + K * 4096 + 512); });
      mark(LB + L_KEY + 16 + w4 * 4, 1);
      fence_sched();
      f32x4 d = {0, 0, 0, 0};
      static_for<0, 8>([&](auto K) { d = mfma(wa[K], gb[K], d); });
      const u32 go = WS_GE + (l15 >> 3) * 8192 + ((bat3 * 2 + (pi & 1)) * 4 + w4) * 512 + (l15 & 7) * 64 + g * 16;
      put(__builtin_bit_cast(v4u, d), rws, go, putck(go), *(const volatile LAS u32*)(size_t)(LB + L_KEY));
      marks_wait<1>(LB + L_KEY + 16 + (lane & 3) * 4);
    } else if constexpr (TG != 2) nap<GDLY>();
    if constexpr (TG == 1) dma4(LB + L_JUNK, tq, rt);
    if constexpr (TG == 2) stouch_run<16>(to);
    rounds<15 - NA, 131072, 0>(m, so, pv, w2v, w2w, rw2);
    if constexpr (TG == 3) dma4(LB + L_JUNK, tq, rt);
    if (!p3ok) {
      wait_vm<(TG == 3 ? 1 : 0)>();
      bar_raw();
      if (pi == 125 && w4 == 0) {
        u32 epoch;
        asm volatile("s_load_dword %0, %1, 0x0\n\ts_waitcnt lgkmcnt(0)" : "=s"(epoch) : "s"(a.ws) : "memory");
        epoch += 1;
        for (u32 spins = 0; spins < SPIN_CAP; ++spins) {
          v4u f4;
          asm volatile("buffer_load_dwordx4 %0, %1, %2, 0 offen sc1\n\ts_waitcnt vmcnt(0)" : "=v"(f4) : "v"(WS_FLAGS + (lane >> 2) * 128 + (lane & 3) * 16), "s"(rws) : "memory");
          if (__all(f4.x == epoch && f4.y == epoch && f4.z == epoch && f4.w == epoch)) break;
          __builtin_amdgcn_s_sleep(8);
        }
        if (lane == 0) __hip_atomic_store((GAS u32*)(a.ws + WS_EPOCH), epoch, __ATOMIC_RELAXED, __HIP_MEMORY_SCOPE_AGENT);
      }
      DUMP();
      return;
    }
    v2u WO[16][4];
    auto o_iss = [&](auto R) { dma16(m0 + L_O + (R & 1) * 8192, wov, rwo, R * 8192); dma16(m0 + L_O + (R & 1) * 8192 + 4096, wow, rwo, R * 8192); };
    auto o_rd = [&](auto R) { static_for<0, 4>([&](auto T) { WO[R][T] = ldstr(wor[T] + L_O + (R & 1) * 8192); }); };
    auto lds_done = [&]() { asm volatile("s_waitcnt lgkmcnt(0)" ::: "memory"); };
    static_for<0, 2>(o_iss);
    dma16(m0 + 15 * 8192, wfv, rwf, 15 * 8192); dma16(m0 + 15 * 8192 + 4096, wfw, rwf, 15 * 8192);
    if constexpr (OA == 4) { wait_vm<2>(); static_for<0, 2>(o_rd); lds_done(); static_for<2, 4>(o_iss); }
    else wait_vm<(TG == 3 ? 7 : 6)>();

    bar_raw();
    m = m0; so = 0;
    if constexpr (OB > OA) {
      rounds<NA - 2, 8192, 0>(m, so, pv, wfv, wfw, rwf);
      static_for<OA - 2, OA>(o_rd);
      rounds<2, 8192, 0>(m, so, pv, wfv, wfw, rwf);
      lds_done(); static_for<OA, OB>(o_iss);
      wait_vm<2 * (OB - OA)>();
    } else {
      rounds<NA, 8192, 0>(m, so, pv, wfv, wfw, rwf);
      static_for<OA - 2, OA>(o_rd);
      wait_vm<0>();
    }

    bar();
    dma16(m0 + L_J, lane * 16, rws, WS_GE + bat3 * 8192 + w4 * 1024); dma16(m0 + L_J + 4096, lane * 16, rws, WS_GE + bat3 * 8192 + w4 * 1024 + 4096);
    if (w4 == 0) dma16(LB + L_GC, lane < 32 ? lane * 16 : OOB, rws, WS_PL + ((WS_GE + bat3 * 8192) >> 4));
    rounds<15 - NA, 8192, 0>(m, so, pv, wfv, wfw, rwf);
    if constexpr (O0 > OB) { if constexpr (OB > OA) { static_for<OA, OB>(o_rd); lds_done(); } static_for<OB, O0>(o_iss); }
    wait_vm<0>();

    bar_raw();
    if constexpr (O0 > OB) static_for<OB, O0>(o_rd); else static_for<OA, OB>(o_rd);
    nap<GDLY>();
    {
      auto iss = [&](auto R) {
        if constexpr (R >= O0 + 8) asm volatile("s_waitcnt lgkmcnt(8)" ::: "memory");
        dma16(m0 + ((R - O0) & 7) * 8192, wov, rwo, R * 8192); dma16(m0 + ((R - O0) & 7) * 8192 + 4096, wow, rwo, R * 8192);
      };
      PAD(21);
      static_for<O0, O0 + 3>(iss);
      static_for<O0, 16>([&](auto R) {
        if constexpr (R == O0 + NW) bar_raw();
        wait_vm<(R + 2 < 16 ? 4 : 2 * (15 - R))>();
        if constexpr (R + 3 < 16) iss(IC<R + 3>{});
        static_for<0, 4>([&](auto T) { WO[R][T] = ldstr(wor[T] + ((R - O0) & 7) * 8192); });
        if constexpr (R == 10) PAD(22);
        fence_sched();
      });
    }

    bar_raw();

    {
      PAD(23);
      f32x4 d[4] = {{0, 0, 0, 0}, {0, 0, 0, 0}, {0, 0, 0, 0}, {0, 0, 0, 0}};
      v4u b[8];
      static_for<4, 8>([&](auto K) { b[K] = lds128(jr + K * 1024); });
      fence_sched();
      static_for<0, 8>([&](auto I) {
        constexpr int K = (I + 4) & 7;
        static_for<0, 4>([&](auto T) { d[T] = mfma(v4u{WO[2 * K][T].x, WO[2 * K][T].y, WO[2 * K + 1][T].x, WO[2 * K + 1][T].y}, b[K], d[T]); });
        if constexpr (I < 4) b[I] = lds128(jr + I * 1024);
        fence_sched();
      });
      const rsrc_t ro = mk(a.out, 2048000);
      static_for<0, 2>([&](auto H) {
        const f32x4 d0 = d[2 * H], d1 = d[2 * H + 1];
        const auto r0 = __builtin_amdgcn_permlane16_swap(pk(d0.x, d0.y), pk(d1.x, d1.y), false, false);
        const auto r1 = __builtin_amdgcn_permlane16_swap(pk(d0.z, d0.w), pk(d1.z, d1.w), false, false);
        __builtin_amdgcn_raw_buffer_store_b128(v4u{r0[0], r1[0], r0[1], r1[1]}, ro, ov + H * 64, 0, 16);
      });
    }

  } else {
    const u32 p1off = WS_P1 + k1 * 524288 + (2 * n1 + (w4 >> 1)) * 65536 + mt * 4096 + (w4 & 1) * 2048;
    if (lane < ((x8 & 3) == 3 ? 4 : 1)) {
      const u32 q = j * 8 + wv;
      const unsigned char* p = lane == 0 ? (const unsigned char*)a.w2 + 7 * 131072 + (c2 * 32 + mt * 2 + (w4 >> 1)) * 512 + n2 * 256 + (w4 & 1) * 128
                             : lane < 3 ? (const unsigned char*)a.wg + q * 512 + bat3 * 256 + (lane & 1) * 128 : (const unsigned char*)a.gp + (q & 63) * 128;
      asm volatile("s_mov_b32 m0, %1\n\ts_nop 0\n\tglobal_load_lds_dword %0, off" ::"v"(p), "s"(LB + L_JUNK) : "memory");
    }
    const u32 epoch = warm0(wv, a.ws) + 1, key = epoch * 0x9E3779B1u | 1;
    STAMP(1);
    const u32 l15 = lane & 15, g = lane >> 4, q4 = l15 >> 2, p4 = lane & 3;
    {
      PAD(0);
      const u32 pw = wv >> 2;
      if (pw) __builtin_amdgcn_s_setprio(BPRIO);
      f32x4 acc[4] = {{0, 0, 0, 0}, {0, 0, 0, 0}, {0, 0, 0, 0}, {0, 0, 0, 0}};
      v4u G[2][11];
      v2u Wf[2][4];
      const u32 xr = opq(LB + L_X + l15 * 1056 + g * 16 + pw * 128);
      const u32 w1r = opq(LB + L_RING + pw * 8192 + g * 1024 + q4 * 128 + ((w4 ^ ((q4 >> 1) + 2 * (g & 1))) * 32) + p4 * 8), w1r2 = opq(w1r + 65536);
      const u32 p1v = opq(p1off + l15 * 64 + g * 16), p1c = opq(lane < 32 ? (p1off >> 4) + lane * 4 : OOB);
      const u32 ex = opq(LB + (w4 < 2 ? L_RING + 15 * 8192 : L_J - 8192) + w4 * 4096 + lane * 16);
      auto rdw1 = [&](auto S, auto M) { Wf[S & 1][M] = ldstr((S < 4 ? w1r : w1r2) + (S & 3) * 16384 + (M >> 1) * 4096 + (M & 1) * 512); };
      auto rdx = [&](auto C, auto U) { G[C][U] = lds128(xr + (U >> 2) * 1056 + (U & 3) * 256 + C * 64); };
      bar_raw();
      STAMP(2);
      static_for<0, 2>([&](auto C) { static_for<0, 4>([&](auto U) { rdx(C, U); }); });
      bar_raw();
      static_for<0, 4>([&](auto M) { rdw1(IC<0>{}, M); });
      asm volatile("s_nop 1" : "+v"(acc[0]), "+v"(acc[1]), "+v"(acc[2]), "+v"(acc[3]));
      fence_sched();
      f32x4 oth[4];
      auto step = [&](auto S) {
        if constexpr (S % 3 == 0) PAD(1 + S / 3);
        if constexpr (S == 1 || S == 3 || S == 5) bar_raw();
        static_for<0, 8>([&](auto M) {
          constexpr bool late = S == 6 || (B2 && S == 0);
          constexpr int C = M >> 2, J = M & 3, W0 = late ? 4 : 0, X0 = late ? 0 : 4;
          const v2u wa = Wf[S & 1][2 * C], wb = Wf[S & 1][2 * C + 1];
          mfma_ip(acc[J], v4u{wa.x, wa.y, wb.x, wb.y}, G[C][J + S]);
          fence_sched();
          if constexpr (late && M == 3) bar_raw();
          if constexpr (S == 7 && M == 4) bar_raw();
          if constexpr (S < 7 && M >= W0 && M < W0 + 4) rdw1(IC<S + 1>{}, IC<M - W0>{});
          if constexpr (S < 7 && M >= X0 && M < X0 + 2) rdx(IC<M - X0>{}, IC<S + 4>{});
          if constexpr (S == 7 && M >= 4) oth[M - 4] = *(const LAS f32x4*)(size_t)(ex + (M - 4) * 1024);
          fence_sched();
        });
      };
      static_for<0, 7>(step);
      asm volatile("s_nop 6" ::: "memory");
      fence_sched();
      if (pw) {
        static_for<0, 4>([&](auto J) { *(LAS f32x4*)(size_t)(ex + J * 1024) = acc[J]; });
        if (w4 == 0) mark(LB + L_KEY, key);
        bar();
        STAMP(3);
        __builtin_amdgcn_raw_buffer_store_b32(epoch, rws, tid == 512 ? WS_FLAGS + (bid >> 4) * 128 + (bid & 15) * 4 : OOB, 0, 16);
      } else {
        PAD(4);
        step(IC<7>{});
        asm volatile("s_nop 6" ::: "memory");
        fence_sched();
        STAMP(3);
        const v4u d0 = pack2(acc[0] + oth[0], acc[1] + oth[1]), d1 = pack2(acc[2] + oth[2], acc[3] + oth[3]);
        st_sc1(d0, rws, p1v);
        st_sc1(d1, rws, p1v + 1024);
        const auto r = __builtin_amdgcn_permlane16_swap(ck4(d0), ck4(d1), false, false);
        const u32 c = r[0] + r[1];
        const auto q = __builtin_amdgcn_permlane32_swap(c, c, false, false);
        __builtin_amdgcn_raw_buffer_store_b32(q[0] + q[1] + key, rws, p1c, WS_PL, 16);
      }
    }
    STAMP(4);
    PAD(5);
    v4u W2[15];
    const u32 w2r = opq(LB + L_RING + (8 * g + q4) * 256 + ((wv ^ (q4 | ((g & 1) << 2))) * 32) + p4 * 8), w2r2 = opq(w2r + 65536);
    auto rdw2 = [&](auto T) { const u32 ad = (T < 8 ? w2r : w2r2) + (T & 7) * 8192; W2[T] = ldstr2(ad, ad + 1024); };
    v4u hp[3];
    u32 rho, rhoh, h1off[3], h1c, h1k, hc;
    {
      const u32 blk = wv >> 1, mm = (wv & 1) * 8 + q4;
      const u32 hb = WS_P1 + g * 524288 + c2 * 65536;
      rho = 8 + 4 * mm + 2 * (blk & 1);
      h1off[0] = opq(mloc * 64 + rho - 8 < 500 ? hb + mt * 4096 + wv * 512 + l15 * 16 : OOB);
      h1off[1] = opq(mloc * 64 + rho + 8 < 500 ? hb + mt * 4096 + wv * 512 + 256 + l15 * 16 : OOB);
      rho = opq(LB + L_X + (rho + (lane >> 5)) * 64 + (rho >> 2) * 32 + (blk >> 1) * 32 + p4 * 8 + (g & 1) * 4);
      const u32 nxt = l15 >> 3;
      rhoh = 72 * nxt + 4 * (q4 & 1) + 2 * (w4 & 1);
      const int hrow = (int)(mloc * 64 + rhoh) - 8;
      h1off[2] = opq((wv >= 4 && hrow >= 0 && hrow < 500) ? hb + (mt + 2 * nxt - 1) * 4096 + w4 * 1024 + (nxt ? 0 : 896) + (lane & 7) * 16 : OOB);
      rhoh = opq(LB + L_X + (rhoh + (lane >> 5)) * 64 + (rhoh >> 2) * 32 + (w4 >> 1) * 32 + p4 * 8 + (g & 1) * 4);
      const u32 sel = p4 == 0 ? h1off[0] : (p4 == 1 ? h1off[1] : (p4 == 2 ? h1off[2] : OOB));
      h1c = opq(ckoff(sel)); h1k = opq(sel == OOB ? 0 : key);
    }
    const u32 ar = opq(LB + L_X + l15 * 288 + g * 16), p2v = opq(WS_P2 + c2 * 262144 + mt * 16384 + n2 * 8192 + wv * 1024 + l15 * 64 + g * 16), p2c = opq(putck(p2v));
    auto h1_load = [&]() {
      static_for<0, 2>([&](auto K) { hp[K] = ld16(rws, h1off[K]); });
      if (wv >= 4) hp[2] = ld16(rws, h1off[2]);
      hc = ldck(rws, h1c);
      mem_order();
    };
    v4u ge[2]; u32 gc[2];
    const u32 geoff = opq(WS_GE + bat3 * 8192 + wv * 1024 + (lane & 7) * 64 + g * 16);
    hp[2] = v4u{0, 0, 0, 0};
    nap<DLY1>();
    bar_raw();
    STAMP(5);
    h1_load();
    warm<1>(wv);
    static_for<0, NA>(rdw2);
    STAMP(6);
    PAD(8);
    u32 tries = 0;
    auto h1_bad = [&]() { return __any(quadsum(ck4(hp[0]) + ck4(hp[1]) + ck4(hp[2]) + h1k - hc) != 0); };
    if (__builtin_expect(h1_bad(), 0)) {
      do { __builtin_amdgcn_s_sleep(2); h1_load(); ++tries; } while (h1_bad() && tries < SPIN_CAP);
    }
    STAMP(7);
    PAD(9);
    {
      const f32x2 s0 = red4(hp[0]), s1 = red4(hp[1]);
      *(LAS u32*)(size_t)(rho) = pk(gelu(s0.x), gelu(s0.y));
      *(LAS u32*)(size_t)(rho + 4 * 288) = pk(gelu(s1.x), gelu(s1.y));
      PAD(10);
      if (wv >= 4) {
        const f32x2 s2 = red4(hp[2]);
        *(LAS u32*)(size_t)(rhoh) = pk(gelu(s2.x), gelu(s2.y));
      }
    }
    STAMP(8);
    PAD(11);
    bar();
    STAMP(9);
    {
      f32x4 a0 = {0, 0, 0, 0}, a1 = {0, 0, 0, 0};
      v4u A[17];
      static_for<0, AW>([&](auto Q) { A[Q] = lds128(ar + ((Q + 1) >> 2) * 288 + ((Q + 1) & 3) * 64); });
      asm volatile("s_nop 1" : "+v"(a0), "+v"(a1));
      fence_sched();
      static_for<0, 15>([&](auto T) {
        mfma_ip(a0, W2[T], A[T]);
        mfma_ip(a1, W2[T], A[T + 2]);
        if constexpr (T + AW < 17) A[T + AW] = lds128(ar + ((T + AW + 1) >> 2) * 288 + ((T + AW + 1) & 3) * 64);
        if constexpr (T + NA < 15) rdw2(IC<T + NA>{});
        if constexpr (T == 8) PAD(12);
        fence_sched();
      });
      asm volatile("s_nop 7" ::: "memory");
      fence_sched();
      put(pack2(a0, a1), rws, p2v, p2c, key);
    }
    STAMP(10);
    PAD(13);
    if (!p3ok) { DUMP(); return; }
    bar_raw();
    v4u WF[8][2];
    u32 wr[4];
    wr[0] = opq(LB + L_RING + (4 * g + q4) * 512 + (((2 * wv) ^ ((4 * g + q4) & 7)) * 32) + p4 * 8);
    wr[1] = opq(wr[0] ^ 32); wr[2] = opq(wr[0] + 65536); wr[3] = opq(wr[1] + 65536);
    auto rdwf = [&](auto K) { static_for<0, 2>([&](auto T) { const u32 ad = wr[(K >> 2) * 2 + T] + (K & 3) * 16384; WF[K][T] = ldstr2(ad, ad + 8192); }); };
    static_assert(NA >= 8 && NA <= 10, "slots 0..7 of w_f land before PE");
    static_for<0, 4>(rdwf);
    PAD(14);
    const u32 h2off = opq(WS_P2 + (lane >> 3) * 262144 + (pi >> 4) * 16384 + (wv * 2 + ((lane >> 2) & 1)) * 1024 + (pi & 15) * 64 + p4 * 16), h2c = opq(ckoff(h2off));
    const u32 fw = opq(LB + L_F + (lane >> 5) * 576 + (wv * 32 + p4 * 8 + ((lane >> 2) & 1) * 4 + ((lane >> 3) & 3)) * 2);
    const u32 fr = opq(LB + L_F + (l15 >> 3) * 576 + g * 16), jw = opq(LB + L_J + lane * 16 + wv * 1024);
    nap<DLY2>();
    PAD(15);
    bar();
    STAMP(11);
    {
      v4u fp = ld16(rws, h2off);
      u32 fc = ldck(rws, h2c);
      mem_order();
      warm<2>(wv);
      static_for<4, 8>(rdwf);
      static_for<0, 2>([&](auto T) { ge[T] = lds128(LB + L_J + (geoff & 8191) + T * 512); gc[T] = *(const LAS u32*)(size_t)(LB + L_GC + ((ckoff(geoff) & 511) + T * 32)); });
    for (u32 spins = 0; __any(((groupsum(ck4(ge[0])) + key) ^ gc[0]) | ((groupsum(ck4(ge[1])) + key) ^ gc[1])) && spins < SPIN_CAP; ++spins) {
      __builtin_amdgcn_s_sleep(2);
      static_for<0, 2>([&](auto T) { ge[T] = ld16(rws, geoff + T * 512); gc[T] = ldck(rws, ckoff(geoff + T * 512)); });
      mem_order();
      tries += 65536;
    }
      PAD(16);
      if (__builtin_expect(__any(quadsum(ck4(fp)) + key != fc), 0)) {
        u32 spins = 0;
        do {
          __builtin_amdgcn_s_sleep(2);
          fp = ld16(rws, h2off);
          fc = ldck(rws, h2c);
          mem_order();
          tries += 256;
        } while (__any(quadsum(ck4(fp)) + key != fc) && ++spins < SPIN_CAP);
      }
      STAMP(12);
      PAD(17);
      const f32x2 s = red4(fp);
      const float snd = (lane & 8) ? s.x : s.y, kp = (lane & 8) ? s.y : s.x;
      const float tot = kp + asf(__builtin_amdgcn_update_dpp(0, (int)asu(snd), 0x128, 0xf, 0xf, false));
      *(LAS u16*)(size_t)(fw) = (u16)pk(gelu(tot), 0.f);
    }
    PAD(18);
    bar();
    {
      f32x4 d0 = __builtin_bit_cast(f32x4, ge[0]), d1 = __builtin_bit_cast(f32x4, ge[1]);
      v4u b[8];
      static_for<0, 8>([&](auto K) { b[K] = lds128(fr + K * 64); });
      fence_sched();
      PAD(19);
      static_for<0, 8>([&](auto K) { d0 = mfma(WF[K][0], b[K], d0); });
      fence_sched();
      float t0[4];
      static_for<0, 8>([&](auto K) {
        d1 = mfma(WF[K][1], b[K], d1);
        if constexpr (K >= 3 && K < 7) t0[K - 3] = tanh_(d0[K - 3]);
        fence_sched();
      });
      PAD(20);
      *(LAS v4u*)(size_t)(jw) = v4u{pk(t0[0], t0[1]), pk(t0[2], t0[3]), pk(tanh_(d1.x), tanh_(d1.y)), pk(tanh_(d1.z), tanh_(d1.w))};
    }
    STAMP(13);
    bar();
    if (wq == 4 && bid == 100) { const u64 dl = hp0 + 3000 + 4 * (hp1 - hp0); for (u32 s = 0; s < 1000000 && __builtin_amdgcn_s_memrealtime() < dl; ++s) __builtin_amdgcn_s_sleep(1); }
  }
  DUMP();
}

extern "C" void kernel_launch(void* const* d_in, const int* in_sizes, int n_in, void* d_out, int out_size, void* d_ws, size_t ws_size,
                              hipStream_t stream) {
  Args a{(const u16*)d_in[0], (const u16*)d_in[1], (const u16*)d_in[2], (const u16*)d_in[3], (const u16*)d_in[4],
         (const u16*)d_in[5], (const u16*)d_in[6], (u16*)d_out, (unsigned char*)d_ws};
  rnnt_fused<<<256, 768, 0, stream>>>(a);
}
```

```cpp
#define GRP 0
#include <hip/hip_runtime.h>
#include <hip/hip_bf16.h>
#include <type_traits>

typedef unsigned u32;
typedef unsigned short u16;
typedef unsigned long long u64;
typedef u32 v4u __attribute__((ext_vector_type(4)));
typedef u32 v2u __attribute__((ext_vector_type(2)));
typedef u64 v8q __attribute__((ext_vector_type(8)));
typedef float f32x4 __attribute__((ext_vector_type(4)));
typedef float f32x2 __attribute__((ext_vector_type(2)));
typedef short s16x4 __attribute__((ext_vector_type(4)));
typedef __bf16 bf16x8 __attribute__((ext_vector_type(8)));
typedef __bf16 bf16x2 __attribute__((ext_vector_type(2)));
typedef __amdgpu_buffer_rsrc_t rsrc_t;
#define GAS __attribute__((address_space(1)))
#define LAS __attribute__((address_space(3)))
#define DI __device__ __forceinline__
template <int N> using IC = std::integral_constant<int, N>;

constexpr u32 WS_EPOCH = 0;
constexpr u32 WS_FLAGS = 4096;
constexpr u32 WS_GE = 65536;
constexpr u32 WS_P1 = 1u << 20;
constexpr u32 WS_P2 = 3u << 20;
constexpr u32 WS_PL = 5u << 20;
constexpr u32 WS_BYTES = 6u << 20;
constexpr u32 OOB = 0x80000000u;
constexpr u32 SPIN_CAP = 100000;
constexpr u32 LSPIN_CAP = 4000000;
constexpr u32 L_RING = 0;
constexpr u32 L_KEY = 131072;
constexpr u32 L_JUNK = L_KEY + 64;
constexpr u32 L_F = L_JUNK + 256;
constexpr u32 L_X = L_F + 1152;
constexpr u32 L_GC = L_X;
constexpr u32 L_O = L_KEY + 8192;
constexpr u32 L_J = L_KEY + 24576;
constexpr u32 LDS_BYTES = L_J + 8192;
static_assert(L_X + 5760 <= L_O && L_X + 19008 <= L_J && LDS_BYTES <= 163840, "lds map");

#define CODE_LINES 108
#define LOOK 3
#define NA 8
#define OA 2
#define OB 2
#define O0 4
#define NW 3
#define SJ2 0
#define TG 1
#define EXL 2
#define BM 3
#define ST 0
#define TDLY 6
#define DLY1 6
#define DLY2 6
#define GDLY 4
#define SPRIO 3
#define BPRIO 1
#define AW 5
#define XAUX ""

template <int I, int N, class F> DI void static_for(F&& f) {
  if constexpr (I < N) { f(IC<I>{}); static_for<I + 1, N>(f); }
}
template <int N> DI void wait_vm() { asm volatile("s_waitcnt vmcnt(%0)" ::"i"(N) : "memory"); }
DI void bar() { asm volatile("s_waitcnt lgkmcnt(0)\n\ts_barrier" ::: "memory"); }
DI void bar_raw() { asm volatile("s_barrier" ::: "memory"); }
DI void fence_sched() { __builtin_amdgcn_sched_barrier(0); }
DI void mem_order() { asm volatile("" ::: "memory"); }
DI u32 opq(u32 x) { asm volatile("" : "+v"(x)); return x; }
template <int N> DI void nap() { if constexpr (N > 0) __builtin_amdgcn_s_sleep(N); }
DI void dma16(u32 lds, u32 voff, rsrc_t rs, u32 soff) {
  asm volatile("s_mov_b32 m0, %0\n\ts_nop 0\n\tbuffer_load_dwordx4 %1, %2, %3 offen lds" ::"s"(lds), "v"(voff), "s"(rs), "s"(soff) : "memory");
}
DI void dma16x(u32 lds, u32 voff, rsrc_t rs, u32 soff) {
  asm volatile("s_mov_b32 m0, %0\n\ts_nop 0\n\tbuffer_load_dwordx4 %1, %2, %3 offen" XAUX " lds" ::"s"(lds), "v"(voff), "s"(rs), "s"(soff) : "memory");
}
DI void dma4(u32 lds, u32 voff, rsrc_t rs) {
  asm volatile("s_mov_b32 m0, %0\n\ts_nop 0\n\tbuffer_load_dword %1, %2, 0 offen lds" ::"s"(lds), "v"(voff), "s"(rs) : "memory");
}
DI rsrc_t mk(const void* p, u32 bytes) { return __builtin_amdgcn_make_buffer_rsrc((void*)p, 0, (int)bytes, 0x00020000); }
DI v4u lds128(u32 a) { return *(const LAS v4u*)(size_t)a; }
DI v2u ldstr(u32 a) { return __builtin_bit_cast(v2u, __builtin_amdgcn_ds_read_tr16_b64_v4i16((LAS s16x4*)(size_t)a)); }
DI v4u ldstr2(u32 a0, u32 a1) { v2u x = ldstr(a0), y = ldstr(a1); return v4u{x.x, x.y, y.x, y.y}; }
DI f32x4 mfma(v4u a, v4u b, f32x4 c) {
  return __builtin_amdgcn_mfma_f32_16x16x32_bf16(__builtin_bit_cast(bf16x8, a), __builtin_bit_cast(bf16x8, b), c, 0, 0, 0);
}
DI void mfma_ip(f32x4& c, v4u a, v4u b) { asm volatile("v_mfma_f32_16x16x32_bf16 %0, %1, %2, %0" : "+v"(c) : "v"(a), "v"(b)); }
DI u32 pk(float a, float b) { f32x2 f = {a, b}; return __builtin_bit_cast(u32, __builtin_convertvector(f, bf16x2)); }
DI float asf(u32 x) { return __builtin_bit_cast(float, x); }
DI u32 asu(float x) { return __builtin_bit_cast(u32, x); }
DI float lo16(u32 x) { return asf(x << 16); }
DI float hi16(u32 x) { return asf(x & 0xFFFF0000u); }
DI float gelu(float x) {
  const float c0 = -2.0f * 0.7978845608028654f * 1.4426950408889634f, c1 = c0 * 0.044715f;
  float e = __builtin_amdgcn_exp2f(x * __builtin_fmaf(x * x, c1, c0));
  return x * __builtin_amdgcn_rcpf(1.0f + e);
}
DI float tanh_(float x) {
  float e = __builtin_amdgcn_exp2f(x * 2.8853900817779268f);
  return __builtin_fmaf(-2.0f, __builtin_amdgcn_rcpf(1.0f + e), 1.0f);
}
DI v4u pack2(f32x4 a, f32x4 b) { return v4u{pk(a.x, a.y), pk(a.z, a.w), pk(b.x, b.y), pk(b.z, b.w)}; }

DI void st_sc1(v4u v, rsrc_t rs, u32 off) { __builtin_amdgcn_raw_buffer_store_b128(v, rs, off, 0, 16); }
template <int AUX = 16> DI v4u ld16(rsrc_t rs, u32 off) { return __builtin_amdgcn_raw_buffer_load_b128(rs, off, 0, AUX); }
template <int AUX = 16> DI u32 ldck(rsrc_t rs, u32 off) { return __builtin_amdgcn_raw_buffer_load_b32(rs, off, WS_PL, AUX); }
DI u32 ckoff(u32 off) { return (off >> 4) & ~3u; }
DI u32 ck4(v4u d) { return (d.x + d.y + d.z) + d.w; }
DI u32 groupsum(u32 c) {
  const auto r = __builtin_amdgcn_permlane32_swap(c, c, false, false);
  c = r[0] + r[1];
  const auto q = __builtin_amdgcn_permlane16_swap(c, c, false, false);
  return q[0] + q[1];
}
DI u32 quadsum(u32 c) {
  c += __builtin_amdgcn_update_dpp(0, (int)c, 0xB1, 0xf, 0xf, false);
  c += __builtin_amdgcn_update_dpp(0, (int)c, 0x4E, 0xf, 0xf, false);
  return c;
}
DI u32 putck(u32 off) { return (off & 48) ? OOB : off >> 4; }
DI void put(v4u d, rsrc_t rs, u32 off, u32 coff, u32 key) {
  st_sc1(d, rs, off);
  fence_sched();
  __builtin_amdgcn_raw_buffer_store_b32(groupsum(ck4(d)) + key, rs, coff, WS_PL, 16);
}
DI f32x2 red4(v4u d) {
  const auto r0 = __builtin_amdgcn_permlane32_swap(d.x, d.z, false, false), r1 = __builtin_amdgcn_permlane32_swap(d.y, d.w, false, false);
  const float s0 = lo16(r0[0]) + lo16(r0[1]), s1 = hi16(r0[0]) + hi16(r0[1]), s2 = lo16(r1[0]) + lo16(r1[1]), s3 = hi16(r1[0]) + hi16(r1[1]);
  const auto q0 = __builtin_amdgcn_permlane16_swap(asu(s0), asu(s2), false, false);
  const auto q1 = __builtin_amdgcn_permlane16_swap(asu(s1), asu(s3), false, false);
  return f32x2{asf(q0[0]) + asf(q0[1]), asf(q1[0]) + asf(q1[1])};
}

DI void mark(u32 a, u32 v) { asm volatile("s_mov_b64 exec, 1\n\tds_write_b32 %0, %1\n\ts_mov_b64 exec, -1" ::"v"(a), "v"(v) : "memory"); }
template <u32 NEED> DI void marks_wait(u32 va) {
  u32 cnt, hv;
  asm volatile("s_mov_b32 %1, %4\n"
               "Lr%=:\n\tds_read_b32 %0, %2\n\ts_waitcnt lgkmcnt(0)\n\tv_cmp_gt_u32 vcc, %3, %0\n\ts_cbranch_vccz Ld%=\n\t"
               "s_sub_u32 %1, %1, 1\n\ts_cbranch_scc0 Lr%=\n"
               "Ld%=:"
               : "=&v"(hv), "=&s"(cnt) : "v"(va), "i"(NEED), "i"(LSPIN_CAP) : "memory", "scc", "vcc");
}
template <int N, u32 STEP, u32 BARS> DI void rounds(u32& m, u32& so, int& pv, u32 va, u32 vb, rsrc_t rs) {
  u32 cnt;
  asm volatile("s_mov_b32 %3, %7\n"
               "L%=:\n\ts_mov_b32 m0, %0\n\ts_add_u32 %0, %0, 0x1000\n\tbuffer_load_dwordx4 %4, %6, %1 offen lds\n\t"
               "s_mov_b32 m0, %0\n\ts_add_u32 %0, %0, 0x1000\n\tbuffer_load_dwordx4 %5, %6, %1 offen lds\n\t"
               "s_add_u32 %1, %1, %10\n\t.if %9\n\ts_add_i32 %2, %2, 1\n\ts_bitcmp0_b32 %9, %2\n\t.endif\n\ts_waitcnt vmcnt(%8)\n\t"
               ".if %9\n\ts_cbranch_scc1 Lq%=\n\ts_barrier\n"
               "Lq%=:\n\t.endif\n\ts_sub_u32 %3, %3, 1\n\ts_cbranch_scc0 L%="
               : "+s"(m), "+s"(so), "+s"(pv), "=&s"(cnt) : "v"(va), "v"(vb), "s"(rs), "i"(N - 1), "i"(2 * LOOK), "i"(BARS), "i"(STEP) : "memory", "scc");
}
DI void sl2(const unsigned char* p, u32 o) { asm volatile("s_load_dword s99, %0, %1\n\ts_load_dword s99, %0, %2" ::"s"(p), "s"(o), "s"(o + 64) : "memory", "s99"); }
template <int O> DI void sl1(const unsigned char* p) { asm volatile("s_load_dword s99, %0, %1" ::"s"(p), "i"(O) : "memory", "s99"); }
template <int N> DI void stouch_taps(const unsigned char* p, u32 r) {
  static_for<0, N>([&](auto T) { sl2(p, T * 131072 + ((r + 5 * T) & 31) * 512); });
}
template <int N> DI void stouch_run(const unsigned char* p) { static_for<0, N>([&](auto T) { sl1<T * 64>(p); }); }

#define PAD(k) asm volatile("PAD%c0:\n\ts_cbranch_execz RET%c1" ::"i"(k), "i"((k) / 8))
#define HOP "s_mov_b64 exec, 0\n\ts_getpc_b64 vcc\n\ts_add_u32 vcc_lo, vcc_lo, %0\n\ts_addc_u32 vcc_hi, vcc_hi, 0\n\ts_setpc_b64 vcc\n\t"
DI u32 warm0(u32 wv, const void* p) {
  u32 e, o = wv * 4 + 12;
  asm volatile("s_load_dword %1, %2, 0x0\n\t" HOP
               "s_branch PAD0\n\ts_branch PAD1\n\ts_branch PAD2\n\ts_branch PAD3\n\ts_branch PAD4\n\ts_branch PAD5\n\ts_branch PAD6\n\ts_branch PAD7\n"
               "RET0:\n\ts_mov_b64 exec, -1\n\ts_waitcnt lgkmcnt(0)"
               : "+s"(o), "=&s"(e) : "s"(p) : "memory", "scc", "vcc");
  return e;
}
template <int S> DI void warm(u32 wv) {
  asm volatile(HOP
               "s_branch PAD%c2\n\ts_branch PAD%c3\n\ts_branch PAD%c4\n\ts_branch PAD%c5\n\ts_branch PAD%c6\n\ts_branch PAD%c7\n\ts_branch PAD%c8\n\ts_branch PAD%c9\n"
               "RET%c1:\n\ts_mov_b64 exec, -1"
               :: "s"(wv * 4 + 12), "i"(S), "i"(8 * S), "i"(8 * S + 1), "i"(8 * S + 2), "i"(8 * S + 3), "i"(8 * S + 4), "i"(8 * S + 5), "i"(8 * S + 6), "i"(8 * S + 7) : "memory", "scc", "vcc");
}

#define STAMP(i)

struct Args { const u16 *x, *w1, *w2, *gp, *wf, *wg, *wo; u16* out; unsigned char* ws; };

__global__ void __launch_bounds__(768, 3) rnnt_fused(Args unused) {
  __shared__ __attribute__((aligned(16))) unsigned char smem[LDS_BYTES];
  const u32 LB = (u32)(size_t)(LAS unsigned char*)smem;
  const u32 tid = threadIdx.x;
  v8q ka; u64 kws, pc;
  asm volatile("s_load_dwordx16 %0, %3, 0x0\n\ts_load_dwordx2 %1, %3, 0x40\n\ts_getpc_b64 %2" : "=&s"(ka), "=&s"(kws), "=&s"(pc) : "s"(__builtin_amdgcn_kernarg_segment_ptr()) : "memory");
  asm volatile("s_mov_b32 m0, %1\n\ts_nop 0\n\tglobal_load_lds_dword %0, %2" :: "v"(min(((blockIdx.x >> 3) * 1536 + tid * 2) & 0x3f80, ((u32)CODE_LINES - 1) << 7)), "s"(LB + L_JUNK), "s"(pc) : "memory");
  const u32 wq = __builtin_amdgcn_readfirstlane(tid >> 6), w4 = wq & 3, wv = wq - 4;
  const u32 bid = blockIdx.x, x8 = bid & 7, j = bid >> 3, mt = j >> 1, mloc = mt & 7, bat = mt >> 3;
  const u32 k1 = x8 >> 1, n1 = 2 * (x8 & 1) + (j & 1);
  u32 lane = tid & 63;
  const u32 wbase = k1 * 32768 + n1 * 128;
  u32 w1v = ((tid >> 3) & 31) * 512 + wbase + (((tid & 7) ^ (((tid >> 3) & 2) | ((tid >> 4) & 4))) * 16);
  u32 xv = ((tid >> 3) & 31) * 512 + (mloc * 65536 - 3584 + k1 * 128) + (tid & 7) * 16;
  u32 m = LB + L_RING + w4 * 1024, mx = LB + L_X + w4 * 1056, nx = min(mloc * 128 + 137, 1000u) * 512, xo = bat * 512000;
  u32 tv = lane < 14 ? (1 + lane) * 131072 + wbase + (mt * 4 + w4) * 512 : OOB;
  asm volatile("" : "+v"(w1v), "+v"(xv), "+v"(tv));
  asm volatile("s_waitcnt lgkmcnt(0)" : "+s"(ka), "+s"(kws), "+s"(m), "+s"(mx), "+s"(nx), "+s"(xo)::"memory");
  const Args a{(const u16*)ka[0], (const u16*)ka[1], (const u16*)ka[2], (const u16*)ka[3], (const u16*)ka[4], (const u16*)ka[5], (const u16*)ka[6], (u16*)ka[7], (unsigned char*)kws};
  const rsrc_t rw1 = mk(a.w1, 15 * 131072), rws = mk(a.ws, WS_BYTES);
  const u32 c2 = x8, n2 = j & 1;
  const u32 pi = x8 * 32 + j, ploc = pi & 127, bat3 = pi >> 7;
  const bool p3ok = ploc < 125;
#define DUMP()

  if (__builtin_expect(wq < 4, 1)) {
    {
      const rsrc_t rx = mk((const void*)(ka[0] + xo), nx);
      static_for<0, 4>([&](auto I) { dma16x(mx + I * 4224, xv + I * 16384, rx, 0); });
      if (w4 < 2) dma16x(mx + 4 * 4224, xv + 65536, rx, 0);
      dma16x(m, w1v, rw1, 0);
      dma16x(m + 4096, w1v, rw1, 16384);
    }
    dma4(LB + L_JUNK, tv, rw1);
    __builtin_amdgcn_s_setprio(SPRIO);
    STAMP(1);
    const u32 m0 = m;
    m += 8192;
    u32 so = 131072;
    int pv = 1 - LOOK;
    constexpr u32 BARS = GRP ? 0x1114 : 0x1554;
    rounds<LOOK - 1, 131072, BARS>(m, so, pv, w1v, w1v + 16384, rw1);
    lane = opq(lane);
    const u32 g = lane >> 4, l15 = lane & 15, q4 = l15 >> 2, rr8 = lane >> 3, r2 = 2 * w4 + (lane >> 5);
    const u32 w2v = opq((c2 * 32 + w4 * 4 + g) * 512 + n2 * 256 + ((((lane >> 1) & 7) ^ (g | (((w4 >> 1) & 1) << 2))) * 32) + (lane & 1) * 16), w2w = opq(w2v + 8192);
    const u32 wfv = opq(r2 * 512 + ((((lane >> 1) & 15) ^ (r2 & 7)) * 32) + (lane & 1) * 16), wfw = opq(wfv + 4096);
    const u32 wov = opq(rr8 * 512 + w4 * 128 + ((((lane >> 1) & 3) ^ (rr8 >> 1)) * 32) + (lane & 1) * 16), wow = opq(wov + 4096);
    u32 wor[4];
    static_for<0, 4>([&](auto T) { wor[T] = opq(m0 + (g >> 1) * 4096 + (4 * (g & 1) + q4) * 128 + ((T ^ (2 * (g & 1) + (q4 >> 1))) * 32) + (lane & 3) * 8); });
    const unsigned char* tp = (const unsigned char*)a.w2 + c2 * 16384 + n2 * 256 + (w4 & 1) * 128;
    const u32 tq = opq(lane < 16 ? (j * 32 + (w4 & 1) * 16 + lane) * 128 : OOB);
    const rsrc_t rt = mk(w4 < 2 ? a.wf : a.wo, 131072);
    const u32 jr = opq(LB + L_J + lane * 16);
    const u32 ov = opq((((bat3 * 250 + 2 * ploc) * 8 + l15) * 256 + w4 * 64 + (g & 1) * 16 + (g >> 1) * 8) * 2);
    if (ploc >= 126) mark(LB + L_KEY + 16 + w4 * 4, 0);
    const u32 tr = mt * 2 + (w4 >> 1);
    const unsigned char *tf = (const unsigned char*)a.wf + (j * 4 + w4) * 1024, *to = (const unsigned char*)a.wo + (j * 4 + w4) * 1024;
    if constexpr (ST == 0) stouch_taps<7>(tp, tr);
    static_assert(LOOK == 3, "waits below");
    wait_vm<5>();
    bar_raw();
    if constexpr (ST == 1) stouch_taps<7>(tp, tr);
    rounds<12, 131072, BARS>(m, so, pv, w1v, w1v + 16384, rw1);
    const rsrc_t rw2 = mk(a.w2, 15 * 131072), rwf = mk(a.wf, 131072), rwo = mk(a.wo, 131072);
    m = m0; so = 0;
    rounds<8, 131072, (1u << 14) | (1u << 15) | (1u << (15 + EXL))>(m, so, pv, w2v, w2w, rw2);
    stouch_taps<7>(tp + 8 * 131072, tr);
    if (ploc >= 126) {
      PAD(6);
      const u32 wgv = w1v - wbase + (bat3 * 2 + (pi & 1)) * 128;
      static_for<0, 2>([&](auto R) { dma16(m0 + 14 * 8192 + R * 4096, (r2 + 8 * R) * 512 + (((lane & 31) ^ (r2 + 8 * R)) * 16), mk(a.gp, 8192), 0); });
      static_for<0, 8>([&](auto R) { dma16(m0 + 10 * 8192 + R * 4096, wgv, mk(a.wg, 131072), R * 16384); });
    }
    if constexpr (NA > 8) rounds<NA - 8, 131072, 0>(m, so, pv, w2v, w2w, rw2);
    if constexpr (TG == 2) stouch_run<16>(tf);
    if constexpr (!TG) { dma4(LB + L_JUNK, tq, rt); wait_vm<1>(); } else wait_vm<0>();
    STAMP(4);
    bar_raw();
    if (ploc >= 126) {
      PAD(7);
      const u32 gr = LB + L_RING + 10 * 8192 + g * 1024 + q4 * 128 + ((w4 ^ ((q4 >> 1) + 2 * (g & 1))) * 32) + (lane & 3) * 8;
      v4u wa[8], gb[8];
      static_for<0, 8>([&](auto K) { gb[K] = lds128(LB + L_RING + 14 * 8192 + l15 * 512 + (((K * 4 + g) ^ l15) * 16)); });
      static_for<0, 8>([&](auto K) { wa[K] = ldstr2(gr + K * 4096, gr + K * 4096 + 512); });
      mark(LB + L_KEY + 16 + w4 * 4, 1);
      fence_sched();
      f32x4 d = {0, 0, 0, 0};
      static_for<0, 8>([&](auto K) { d = mfma(wa[K], gb[K], d); });
      const u32 go = WS_GE + (l15 >> 3) * 8192 + ((bat3 * 2 + (pi & 1)) * 4 + w4) * 512 + (l15 & 7) * 64 + g * 16;
      put(__builtin_bit_cast(v4u, d), rws, go, putck(go), *(const volatile LAS u32*)(size_t)(LB + L_KEY));
      marks_wait<1>(LB + L_KEY + 16 + (lane & 3) * 4);
    } else if constexpr (TG != 2) nap<GDLY>();
    if constexpr (TG == 1) dma4(LB + L_JUNK, tq, rt);
    if constexpr (TG == 2) stouch_run<16>(to);
    rounds<15 - NA, 131072, 0>(m, so, pv, w2v, w2w, rw2);
    if constexpr (TG == 3) dma4(LB + L_JUNK, tq, rt);
    if (!p3ok) {
      wait_vm<(TG == 3 ? 1 : 0)>();
      bar_raw();
      if (pi == 125 && w4 == 0) {
        u32 epoch;
        asm volatile("s_load_dword %0, %1, 0x0\n\ts_waitcnt lgkmcnt(0)" : "=s"(epoch) : "s"(a.ws) : "memory");
        epoch += 1;
        for (u32 spins = 0; spins < SPIN_CAP; ++spins) {
          v4u f4;
          asm volatile("buffer_load_dwordx4 %0, %1, %2, 0 offen sc1\n\ts_waitcnt vmcnt(0)" : "=v"(f4) : "v"(WS_FLAGS + (lane >> 2) * 128 + (lane & 3) * 16), "s"(rws) : "memory");
          if (__all(f4.x == epoch && f4.y == epoch && f4.z == epoch && f4.w == epoch)) break;
          __builtin_amdgcn_s_sleep(8);
        }
        if (lane == 0) __hip_atomic_store((GAS u32*)(a.ws + WS_EPOCH), epoch, __ATOMIC_RELAXED, __HIP_MEMORY_SCOPE_AGENT);
      }
      DUMP();
      return;
    }
    v2u WO[16][4];
    auto o_iss = [&](auto R) { dma16(m0 + L_O + (R & 1) * 8192, wov, rwo, R * 8192); dma16(m0 + L_O + (R & 1) * 8192 + 4096, wow, rwo, R * 8192); };
    auto o_rd = [&](auto R) { static_for<0, 4>([&](auto T) { WO[R][T] = ldstr(wor[T] + L_O + (R & 1) * 8192); }); };
    auto lds_done = [&]() { asm volatile("s_waitcnt lgkmcnt(0)" ::: "memory"); };
    static_for<0, 2>(o_iss);
    dma16(m0 + 15 * 8192, wfv, rwf, 15 * 8192); dma16(m0 + 15 * 8192 + 4096, wfw, rwf, 15 * 8192);
    if constexpr (OA == 4) { wait_vm<2>(); static_for<0, 2>(o_rd); lds_done(); static_for<2, 4>(o_iss); }
    else wait_vm<(TG == 3 ? 7 : 6)>();
    STAMP(5);
    bar_raw();
    m = m0; so = 0;
    if constexpr (OB > OA) {
      rounds<NA - 2, 8192, 0>(m, so, pv, wfv, wfw, rwf);
      static_for<OA - 2, OA>(o_rd);
      rounds<2, 8192, 0>(m, so, pv, wfv, wfw, rwf);
      lds_done(); static_for<OA, OB>(o_iss);
      wait_vm<2 * (OB - OA)>();
    } else {
      rounds<NA, 8192, 0>(m, so, pv, wfv, wfw, rwf);
      static_for<OA - 2, OA>(o_rd);
      wait_vm<0>();
    }
    STAMP(6);
    bar();
    dma16(m0 + L_J, lane * 16, rws, WS_GE + bat3 * 8192 + w4 * 1024); dma16(m0 + L_J + 4096, lane * 16, rws, WS_GE + bat3 * 8192 + w4 * 1024 + 4096);
    if (w4 == 0) dma16(LB + L_GC, lane < 32 ? lane * 16 : OOB, rws, WS_PL + ((WS_GE + bat3 * 8192) >> 4));
    rounds<15 - NA, 8192, 0>(m, so, pv, wfv, wfw, rwf);
    if constexpr (O0 > OB) { if constexpr (OB > OA) { static_for<OA, OB>(o_rd); lds_done(); } static_for<OB, O0>(o_iss); }
    wait_vm<0>();
    STAMP(7);
    bar_raw();
    if constexpr (O0 > OB) static_for<OB, O0>(o_rd); else static_for<OA, OB>(o_rd);
    nap<GDLY>();
    {
      auto iss = [&](auto R) {
        if constexpr (R >= O0 + 8) asm volatile("s_waitcnt lgkmcnt(8)" ::: "memory");
        dma16(m0 + ((R - O0) & 7) * 8192, wov, rwo, R * 8192); dma16(m0 + ((R - O0) & 7) * 8192 + 4096, wow, rwo, R * 8192);
      };
      PAD(21);
      static_for<O0, O0 + 3>(iss);
      static_for<O0, 16>([&](auto R) {
        if constexpr (R == O0 + NW) bar_raw();
        wait_vm<(R + 2 < 16 ? 4 : 2 * (15 - R))>();
        if constexpr (R + 3 < 16) iss(IC<R + 3>{});
        static_for<0, 4>([&](auto T) { WO[R][T] = ldstr(wor[T] + ((R - O0) & 7) * 8192); });
        if constexpr (R == 10) PAD(22);
        fence_sched();
      });
    }
    STAMP(8);
    bar_raw();
    STAMP(9);
    {
      PAD(23);
      f32x4 d[4] = {{0, 0, 0, 0}, {0, 0, 0, 0}, {0, 0, 0, 0}, {0, 0, 0, 0}};
      v4u b[8];
      static_for<4, 8>([&](auto K) { b[K] = lds128(jr + K * 1024); });
      fence_sched();
      static_for<0, 8>([&](auto I) {
        constexpr int K = (I + 4) & 7;
        static_for<0, 4>([&](auto T) { d[T] = mfma(v4u{WO[2 * K][T].x, WO[2 * K][T].y, WO[2 * K + 1][T].x, WO[2 * K + 1][T].y}, b[K], d[T]); });
        if constexpr (I < 4) b[I] = lds128(jr + I * 1024);
        fence_sched();
      });
      const rsrc_t ro = mk(a.out, 2048000);
      static_for<0, 2>([&](auto H) {
        const f32x4 d0 = d[2 * H], d1 = d[2 * H + 1];
        const auto r0 = __builtin_amdgcn_permlane16_swap(pk(d0.x, d0.y), pk(d1.x, d1.y), false, false);
        const auto r1 = __builtin_amdgcn_permlane16_swap(pk(d0.z, d0.w), pk(d1.z, d1.w), false, false);
        __builtin_amdgcn_raw_buffer_store_b128(v4u{r0[0], r1[0], r0[1], r1[1]}, ro, ov + H * 64, 0, 16);
      });
    }
    STAMP(14);
  } else {
    const u32 p1off = WS_P1 + k1 * 524288 + (2 * n1 + (w4 >> 1)) * 65536 + mt * 4096 + (w4 & 1) * 2048;
    if (lane < ((x8 & 3) == 3 ? 4 : 1)) {
      const u32 q = j * 8 + wv;
      const unsigned char* p = lane == 0 ? (const unsigned char*)a.w2 + 7 * 131072 + (c2 * 32 + mt * 2 + (w4 >> 1)) * 512 + n2 * 256 + (w4 & 1) * 128
                             : lane < 3 ? (const unsigned char*)a.wg + q * 512 + bat3 * 256 + (lane & 1) * 128 : (const unsigned char*)a.gp + (q & 63) * 128;
      asm volatile("s_mov_b32 m0, %1\n\ts_nop 0\n\tglobal_load_lds_dword %0, off" ::"v"(p), "s"(LB + L_JUNK) : "memory");
    }
    const u32 epoch = warm0(wv, a.ws) + 1, key = epoch * 0x9E3779B1u | 1;
    STAMP(1);
    const u32 l15 = lane & 15, g = lane >> 4, q4 = l15 >> 2, p4 = lane & 3;
    {
      PAD(0);
      const u32 pw = wv >> 2;
      if (pw) __builtin_amdgcn_s_setprio(BPRIO);
      f32x4 acc[4] = {{0, 0, 0, 0}, {0, 0, 0, 0}, {0, 0, 0, 0}, {0, 0, 0, 0}};
      v4u G[2][11];
      v2u Wf[2][4];
      const u32 xr = opq(LB + L_X + l15 * 1056 + g * 16 + pw * 128);
      const u32 w1r = opq(LB + L_RING + pw * 8192 + g * 1024 + q4 * 128 + ((w4 ^ ((q4 >> 1) + 2 * (g & 1))) * 32) + p4 * 8), w1r2 = opq(w1r + 65536);
      const u32 p1v = opq(p1off + l15 * 64 + g * 16), p1c = opq(lane < 32 ? (p1off >> 4) + lane * 4 : OOB);
      const u32 ex = opq(LB + (w4 < 2 ? L_RING + 15 * 8192 : L_J - 8192) + w4 * 4096 + lane * 16);
      auto rdw1 = [&](auto S, auto M) { Wf[S & 1][M] = ldstr((S < 4 ? w1r : w1r2) + (S & 3) * 16384 + (M >> 1) * 4096 + (M & 1) * 512); };
      auto rdx = [&](auto C, auto U) { G[C][U] = lds128(xr + (U >> 2) * 1056 + (U & 3) * 256 + C * 64); };
      bar_raw();
      STAMP(2);
      static_for<0, 2>([&](auto C) { static_for<0, 4>([&](auto U) { rdx(C, U); }); });
      bar_raw();
      static_for<0, 4>([&](auto M) { rdw1(IC<0>{}, M); });
      asm volatile("s_nop 1" : "+v"(acc[0]), "+v"(acc[1]), "+v"(acc[2]), "+v"(acc[3]));
      fence_sched();
      f32x4 oth[4];
      auto step = [&](auto S) {
        if constexpr (S % 3 == 0) PAD(1 + S / 3);
        if constexpr (GRP && (S == 1 || S == 3 || (GRP == 1 && S == 5))) bar_raw();
        static_for<0, 8>([&](auto M) {
          constexpr bool late = !GRP || S == 0 || S == 6 || (GRP == 2 && S == 5);
          constexpr int C = M >> 2, J = M & 3, W0 = late ? BM + 1 : 0, X0 = late ? (BM < 2 ? 6 : 0) : 4;
          const v2u wa = Wf[S & 1][2 * C], wb = Wf[S & 1][2 * C + 1];
          mfma_ip(acc[J], v4u{wa.x, wa.y, wb.x, wb.y}, G[C][J + S]);
          fence_sched();
          if constexpr (S < 7 && late && M == BM) bar_raw();
          if constexpr (S == 7 && M == 4) bar_raw();
          if constexpr (S < 7 && M >= W0 && M < W0 + 4) rdw1(IC<S + 1>{}, IC<M - W0>{});
          if constexpr (S < 7 && M >= X0 && M < X0 + 2) rdx(IC<M - X0>{}, IC<S + 4>{});
          if constexpr (S == 7 && M >= 4) oth[M - 4] = *(const LAS f32x4*)(size_t)(ex + (M - 4) * 1024);
          fence_sched();
        });
      };
      static_for<0, 7>(step);
      asm volatile("s_nop 6" ::: "memory");
      fence_sched();
      if (pw) {
        static_for<0, 4>([&](auto J) { *(LAS f32x4*)(size_t)(ex + J * 1024) = acc[J]; });
        if (w4 == 0) mark(LB + L_KEY, key);
        bar();
        STAMP(3);
        __builtin_amdgcn_raw_buffer_store_b32(epoch, rws, tid == 512 ? WS_FLAGS + (bid >> 4) * 128 + (bid & 15) * 4 : OOB, 0, 16);
      } else {
        PAD(4);
        step(IC<7>{});
        asm volatile("s_nop 6" ::: "memory");
        fence_sched();
        STAMP(3);
        const v4u d0 = pack2(acc[0] + oth[0], acc[1] + oth[1]), d1 = pack2(acc[2] + oth[2], acc[3] + oth[3]);
        st_sc1(d0, rws, p1v);
        st_sc1(d1, rws, p1v + 1024);
        const auto r = __builtin_amdgcn_permlane16_swap(ck4(d0), ck4(d1), false, false);
        const u32 c = r[0] + r[1];
        const auto q = __builtin_amdgcn_permlane32_swap(c, c, false, false);
        __builtin_amdgcn_raw_buffer_store_b32(q[0] + q[1] + key, rws, p1c, WS_PL, 16);
      }
    }
    STAMP(4);
    PAD(5);
    v4u W2[15];
    const u32 w2r = opq(LB + L_RING + (8 * g + q4) * 256 + ((wv ^ (q4 | ((g & 1) << 2))) * 32) + p4 * 8), w2r2 = opq(w2r + 65536);
    auto rdw2 = [&](auto T) { const u32 ad = (T < 8 ? w2r : w2r2) + (T & 7) * 8192; W2[T] = ldstr2(ad, ad + 1024); };
    v4u hp[3];
    u32 rho, rhoh, h1off[3], h1c, h1k, hc;
    {
      const u32 blk = wv >> 1, mm = (wv & 1) * 8 + q4;
      const u32 hb = WS_P1 + g * 524288 + c2 * 65536;
      rho = 8 + 4 * mm + 2 * (blk & 1);
      h1off[0] = opq(mloc * 64 + rho - 8 < 500 ? hb + mt * 4096 + wv * 512 + l15 * 16 : OOB);
      h1off[1] = opq(mloc * 64 + rho + 8 < 500 ? hb + mt * 4096 + wv * 512 + 256 + l15 * 16 : OOB);
      rho = opq(LB + L_X + (rho + (lane >> 5)) * 64 + (rho >> 2) * 32 + (blk >> 1) * 32 + p4 * 8 + (g & 1) * 4);
      const u32 nxt = l15 >> 3;
      rhoh = 72 * nxt + 4 * (q4 & 1) + 2 * (w4 & 1);
      const int hrow = (int)(mloc * 64 + rhoh) - 8;
      h1off[2] = opq((wv >= 4 && hrow >= 0 && hrow < 500) ? hb + (mt + 2 * nxt - 1) * 4096 + w4 * 1024 + (nxt ? 0 : 896) + (lane & 7) * 16 : OOB);
      rhoh = opq(LB + L_X + (rhoh + (lane >> 5)) * 64 + (rhoh >> 2) * 32 + (w4 >> 1) * 32 + p4 * 8 + (g & 1) * 4);
      const u32 sel = p4 == 0 ? h1off[0] : (p4 == 1 ? h1off[1] : (p4 == 2 ? h1off[2] : OOB));
      h1c = opq(ckoff(sel)); h1k = opq(sel == OOB ? 0 : key);
    }
    const u32 ar = opq(LB + L_X + l15 * 288 + g * 16), p2v = opq(WS_P2 + c2 * 262144 + mt * 16384 + n2 * 8192 + wv * 1024 + l15 * 64 + g * 16), p2c = opq(putck(p2v));
    auto h1_load = [&]() {
      static_for<0, 2>([&](auto K) { hp[K] = ld16(rws, h1off[K]); });
      if (wv >= 4) hp[2] = ld16(rws, h1off[2]);
      hc = ldck(rws, h1c);
      mem_order();
    };
    v4u ge[2]; u32 gc[2];
    const u32 geoff = opq(WS_GE + bat3 * 8192 + wv * 1024 + (lane & 7) * 64 + g * 16);
    hp[2] = v4u{0, 0, 0, 0};
    nap<DLY1>();
    bar_raw();
    STAMP(5);
    h1_load();
    warm<1>(wv);
    static_for<0, NA>(rdw2);
    STAMP(6);
    PAD(8);
    u32 tries = 0;
    auto h1_bad = [&]() { return __any(quadsum(ck4(hp[0]) + ck4(hp[1]) + ck4(hp[2]) + h1k - hc) != 0); };
    if (__builtin_expect(h1_bad(), 0)) {
      do { __builtin_amdgcn_s_sleep(2); h1_load(); ++tries; } while (h1_bad() && tries < SPIN_CAP);
    }
    STAMP(7);
    PAD(9);
    {
      const f32x2 s0 = red4(hp[0]), s1 = red4(hp[1]);
      *(LAS u32*)(size_t)(rho) = pk(gelu(s0.x), gelu(s0.y));
      *(LAS u32*)(size_t)(rho + 4 * 288) = pk(gelu(s1.x), gelu(s1.y));
      PAD(10);
      if (wv >= 4) {
        const f32x2 s2 = red4(hp[2]);
        *(LAS u32*)(size_t)(rhoh) = pk(gelu(s2.x), gelu(s2.y));
      }
    }
    STAMP(8);
    PAD(11);
    bar();
    STAMP(9);
    {
      f32x4 a0 = {0, 0, 0, 0}, a1 = {0, 0, 0, 0};
      v4u A[17];
      static_for<0, AW>([&](auto Q) { A[Q] = lds128(ar + ((Q + 1) >> 2) * 288 + ((Q + 1) & 3) * 64); });
      asm volatile("s_nop 1" : "+v"(a0), "+v"(a1));
      fence_sched();
      static_for<0, 15>([&](auto T) {
        mfma_ip(a0, W2[T], A[T]);
        mfma_ip(a1, W2[T], A[T + 2]);
        if constexpr (T + AW < 17) A[T + AW] = lds128(ar + ((T + AW + 1) >> 2) * 288 + ((T + AW + 1) & 3) * 64);
        if constexpr (T + NA < 15) rdw2(IC<T + NA>{});
        if constexpr (T == 8) PAD(12);
        fence_sched();
      });
      asm volatile("s_nop 7" ::: "memory");
      fence_sched();
      put(pack2(a0, a1), rws, p2v, p2c, key);
    }
    STAMP(10);
    PAD(13);
    if (!p3ok) { DUMP(); return; }
    bar_raw();
    v4u WF[8][2];
    u32 wr[4];
    wr[0] = opq(LB + L_RING + (4 * g + q4) * 512 + (((2 * wv) ^ ((4 * g + q4) & 7)) * 32) + p4 * 8);
    wr[1] = opq(wr[0] ^ 32); wr[2] = opq(wr[0] + 65536); wr[3] = opq(wr[1] + 65536);
    auto rdwf = [&](auto K) { static_for<0, 2>([&](auto T) { const u32 ad = wr[(K >> 2) * 2 + T] + (K & 3) * 16384; WF[K][T] = ldstr2(ad, ad + 8192); }); };
    static_assert(NA >= 8 && NA <= 10, "slots 0..7 of w_f land before PE");
    static_for<0, 4>(rdwf);
    PAD(14);
    const u32 h2off = opq(WS_P2 + (lane >> 3) * 262144 + (pi >> 4) * 16384 + (wv * 2 + ((lane >> 2) & 1)) * 1024 + (pi & 15) * 64 + p4 * 16), h2c = opq(ckoff(h2off));
    const u32 fw = opq(LB + L_F + (lane >> 5) * 576 + (wv * 32 + p4 * 8 + ((lane >> 2) & 1) * 4 + ((lane >> 3) & 3)) * 2);
    const u32 fr = opq(LB + L_F + (l15 >> 3) * 576 + g * 16), jw = opq(LB + L_J + lane * 16 + wv * 1024);
    nap<DLY2>();
    PAD(15);
    bar();
    STAMP(11);
    {
      v4u fp = ld16(rws, h2off);
      u32 fc = ldck(rws, h2c);
      mem_order();
      warm<2>(wv);
      static_for<4, 8>(rdwf);
      static_for<0, 2>([&](auto T) { ge[T] = lds128(LB + L_J + (geoff & 8191) + T * 512); gc[T] = *(const LAS u32*)(size_t)(LB + L_GC + ((ckoff(geoff) & 511) + T * 32)); });
    for (u32 spins = 0; __any(((groupsum(ck4(ge[0])) + key) ^ gc[0]) | ((groupsum(ck4(ge[1])) + key) ^ gc[1])) && spins < SPIN_CAP; ++spins) {
      __builtin_amdgcn_s_sleep(2);
      static_for<0, 2>([&](auto T) { ge[T] = ld16(rws, geoff + T * 512); gc[T] = ldck(rws, ckoff(geoff + T * 512)); });
      mem_order();
      tries += 65536;
    }
      PAD(16);
      if (__builtin_expect(__any(quadsum(ck4(fp)) + key != fc), 0)) {
        u32 spins = 0;
        do {
          __builtin_amdgcn_s_sleep(2);
          fp = ld16(rws, h2off);
          fc = ldck(rws, h2c);
          mem_order();
          tries += 256;
        } while (__any(quadsum(ck4(fp)) + key != fc) && ++spins < SPIN_CAP);
      }
      STAMP(12);
      PAD(17);
      const f32x2 s = red4(fp);
      const float snd = (lane & 8) ? s.x : s.y, kp = (lane & 8) ? s.y : s.x;
      const float tot = kp + asf(__builtin_amdgcn_update_dpp(0, (int)asu(snd), 0x128, 0xf, 0xf, false));
      *(LAS u16*)(size_t)(fw) = (u16)pk(gelu(tot), 0.f);
    }
    PAD(18);
    bar();
    {
      f32x4 d0 = __builtin_bit_cast(f32x4, ge[0]), d1 = __builtin_bit_cast(f32x4, ge[1]);
      v4u b[8];
      static_for<0, 8>([&](auto K) { b[K] = lds128(fr + K * 64); });
      fence_sched();
      PAD(19);
      static_for<0, 8>([&](auto K) { d0 = mfma(WF[K][0], b[K], d0); });
      fence_sched();
      float t0[4];
      static_for<0, 8>([&](auto K) {
        d1 = mfma(WF[K][1], b[K], d1);
        if constexpr (K >= 3 && K < 7) t0[K - 3] = tanh_(d0[K - 3]);
        fence_sched();
      });
      PAD(20);
      *(LAS v4u*)(size_t)(jw) = v4u{pk(t0[0], t0[1]), pk(t0[2], t0[3]), pk(tanh_(d1.x), tanh_(d1.y)), pk(tanh_(d1.z), tanh_(d1.w))};
    }
    STAMP(13);
    bar();
  }
  DUMP();
}

extern "C" void kernel_launch(void* const* d_in, const int* in_sizes, int n_in, void* d_out, int out_size, void* d_ws, size_t ws_size,
                              hipStream_t stream) {
  Args a{(const u16*)d_in[0], (const u16*)d_in[1], (const u16*)d_in[2], (const u16*)d_in[3], (const u16*)d_in[4],
         (const u16*)d_in[5], (const u16*)d_in[6], (u16*)d_out, (unsigned char*)d_ws};
  rnnt_fused<<<256, 768, 0, stream>>>(a);
}
```

```cpp
#define DLY1 22
#include <hip/hip_runtime.h>
#include <hip/hip_bf16.h>
#include <type_traits>

typedef unsigned u32;
typedef unsigned short u16;
typedef unsigned long long u64;
typedef u32 v4u __attribute__((ext_vector_type(4)));
typedef u32 v2u __attribute__((ext_vector_type(2)));
typedef u64 v8q __attribute__((ext_vector_type(8)));
typedef float f32x4 __attribute__((ext_vector_type(4)));
typedef float f32x2 __attribute__((ext_vector_type(2)));
typedef short s16x4 __attribute__((ext_vector_type(4)));
typedef __bf16 bf16x8 __attribute__((ext_vector_type(8)));
typedef __bf16 bf16x2 __attribute__((ext_vector_type(2)));
typedef __amdgpu_buffer_rsrc_t rsrc_t;
#define GAS __attribute__((address_space(1)))
#define LAS __attribute__((address_space(3)))
#define DI __device__ __forceinline__
template <int N> using IC = std::integral_constant<int, N>;

constexpr u32 WS_EPOCH = 0;
constexpr u32 WS_FLAGS = 4096;
constexpr u32 WS_GE = 65536;
constexpr u32 WS_P1 = 1u << 20;
constexpr u32 WS_P2 = 3u << 20;
constexpr u32 WS_PL = 5u << 20;
constexpr u32 WS_BYTES = 6u << 20;
constexpr u32 OOB = 0x80000000u;
constexpr u32 SPIN_CAP = 100000;
constexpr u32 LSPIN_CAP = 4000000;
constexpr u32 L_RING = 0;
constexpr u32 L_KEY = 131072;
constexpr u32 L_JUNK = L_KEY + 64;
constexpr u32 L_F = L_JUNK + 256;
constexpr u32 L_X = L_F + 1152;
constexpr u32 L_GC = L_X;
constexpr u32 L_O = L_KEY + 8192;
constexpr u32 L_J = L_KEY + 24576;
constexpr u32 LDS_BYTES = L_J + 8192;
static_assert(L_X + 5760 <= L_O && L_X + 19008 <= L_J && LDS_BYTES <= 163840, "lds map");

#define LOOK 3
#define NA 8
#define OA 2
#define OB 2
#define O0 4
#define NW 3
#define SJ2 0
#define TG 1
#define EXL 2
#define W1E 0
#define SDLY 0
#define GRP 0
#define BM 3
#define ST 0
#define TDLY 6
#define DLY2 8
#define GDLY 4
#define SPRIO 3
#define BPRIO 1
#define AW 5
#define XAUX ""

template <int I, int N, class F> DI void static_for(F&& f) {
  if constexpr (I < N) { f(IC<I>{}); static_for<I + 1, N>(f); }
}
template <int N> DI void wait_vm() { asm volatile("s_waitcnt vmcnt(%0)" ::"i"(N) : "memory"); }
DI void bar() { asm volatile("s_waitcnt lgkmcnt(0)\n\ts_barrier" ::: "memory"); }
DI void bar_raw() { asm volatile("s_barrier" ::: "memory"); }
DI void fence_sched() { __builtin_amdgcn_sched_barrier(0); }
DI void mem_order() { asm volatile("" ::: "memory"); }
DI u32 opq(u32 x) { asm volatile("" : "+v"(x)); return x; }
template <int N> DI void nap() { if constexpr (N > 0) __builtin_amdgcn_s_sleep(N); }
DI void dma16(u32 lds, u32 voff, rsrc_t rs, u32 soff) {
  asm volatile("s_mov_b32 m0, %0\n\ts_nop 0\n\tbuffer_load_dwordx4 %1, %2, %3 offen lds" ::"s"(lds), "v"(voff), "s"(rs), "s"(soff) : "memory");
}
DI void dma16x(u32 lds, u32 voff, rsrc_t rs, u32 soff) {
  asm volatile("s_mov_b32 m0, %0\n\ts_nop 0\n\tbuffer_load_dwordx4 %1, %2, %3 offen" XAUX " lds" ::"s"(lds), "v"(voff), "s"(rs), "s"(soff) : "memory");
}
DI void dma4(u32 lds, u32 voff, rsrc_t rs) {
  asm volatile("s_mov_b32 m0, %0\n\ts_nop 0\n\tbuffer_load_dword %1, %2, 0 offen lds" ::"s"(lds), "v"(voff), "s"(rs) : "memory");
}
DI rsrc_t mk(const void* p, u32 bytes) { return __builtin_amdgcn_make_buffer_rsrc((void*)p, 0, (int)bytes, 0x00020000); }
DI v4u lds128(u32 a) { return *(const LAS v4u*)(size_t)a; }
DI v2u ldstr(u32 a) { return __builtin_bit_cast(v2u, __builtin_amdgcn_ds_read_tr16_b64_v4i16((LAS s16x4*)(size_t)a)); }
DI v4u ldstr2(u32 a0, u32 a1) { v2u x = ldstr(a0), y = ldstr(a1); return v4u{x.x, x.y, y.x, y.y}; }
DI f32x4 mfma(v4u a, v4u b, f32x4 c) {
  return __builtin_amdgcn_mfma_f32_16x16x32_bf16(__builtin_bit_cast(bf16x8, a), __builtin_bit_cast(bf16x8, b), c, 0, 0, 0);
}
DI void mfma_ip(f32x4& c, v4u a, v4u b) { asm volatile("v_mfma_f32_16x16x32_bf16 %0, %1, %2, %0" : "+v"(c) : "v"(a), "v"(b)); }
DI u32 pk(float a, float b) { f32x2 f = {a, b}; return __builtin_bit_cast(u32, __builtin_convertvector(f, bf16x2)); }
DI float asf(u32 x) { return __builtin_bit_cast(float, x); }
DI u32 asu(float x) { return __builtin_bit_cast(u32, x); }
DI float lo16(u32 x) { return asf(x << 16); }
DI float hi16(u32 x) { return asf(x & 0xFFFF0000u); }
DI float gelu(float x) {
  const float c0 = -2.0f * 0.7978845608028654f * 1.4426950408889634f, c1 = c0 * 0.044715f;
  float e = __builtin_amdgcn_exp2f(x * __builtin_fmaf(x * x, c1, c0));
  return x * __builtin_amdgcn_rcpf(1.0f + e);
}
DI float tanh_(float x) {
  float e = __builtin_amdgcn_exp2f(x * 2.8853900817779268f);
  return __builtin_fmaf(-2.0f, __builtin_amdgcn_rcpf(1.0f + e), 1.0f);
}
DI v4u pack2(f32x4 a, f32x4 b) { return v4u{pk(a.x, a.y), pk(a.z, a.w), pk(b.x, b.y), pk(b.z, b.w)}; }

DI void st_sc1(v4u v, rsrc_t rs, u32 off) { __builtin_amdgcn_raw_buffer_store_b128(v, rs, off, 0, 16); }
template <int AUX = 16> DI v4u ld16(rsrc_t rs, u32 off) { return __builtin_amdgcn_raw_buffer_load_b128(rs, off, 0, AUX); }
template <int AUX = 16> DI u32 ldck(rsrc_t rs, u32 off) { return __builtin_amdgcn_raw_buffer_load_b32(rs, off, WS_PL, AUX); }
DI u32 ckoff(u32 off) { return (off >> 4) & ~3u; }
DI u32 ck4(v4u d) { return (d.x + d.y + d.z) + d.w; }
DI u32 groupsum(u32 c) {
  const auto r = __builtin_amdgcn_permlane32_swap(c, c, false, false);
  c = r[0] + r[1];
  const auto q = __builtin_amdgcn_permlane16_swap(c, c, false, false);
  return q[0] + q[1];
}
DI u32 quadsum(u32 c) {
  c += __builtin_amdgcn_update_dpp(0, (int)c, 0xB1, 0xf, 0xf, false);
  c += __builtin_amdgcn_update_dpp(0, (int)c, 0x4E, 0xf, 0xf, false);
  return c;
}
DI u32 putck(u32 off) { return (off & 48) ? OOB : off >> 4; }
DI void put(v4u d, rsrc_t rs, u32 off, u32 coff, u32 key) {
  st_sc1(d, rs, off);
  fence_sched();
  __builtin_amdgcn_raw_buffer_store_b32(groupsum(ck4(d)) + key, rs, coff, WS_PL, 16);
}
DI f32x2 red4(v4u d) {
  const auto r0 = __builtin_amdgcn_permlane32_swap(d.x, d.z, false, false), r1 = __builtin_amdgcn_permlane32_swap(d.y, d.w, false, false);
  const float s0 = lo16(r0[0]) + lo16(r0[1]), s1 = hi16(r0[0]) + hi16(r0[1]), s2 = lo16(r1[0]) + lo16(r1[1]), s3 = hi16(r1[0]) + hi16(r1[1]);
  const auto q0 = __builtin_amdgcn_permlane16_swap(asu(s0), asu(s2), false, false);
  const auto q1 = __builtin_amdgcn_permlane16_swap(asu(s1), asu(s3), false, false);
  return f32x2{asf(q0[0]) + asf(q0[1]), asf(q1[0]) + asf(q1[1])};
}

DI void mark(u32 a, u32 v) { asm volatile("s_mov_b64 exec, 1\n\tds_write_b32 %0, %1\n\ts_mov_b64 exec, -1" ::"v"(a), "v"(v) : "memory"); }
template <u32 NEED> DI void marks_wait(u32 va) {
  u32 cnt, hv;
  asm volatile("s_mov_b32 %1, %4\n"
               "Lr%=:\n\tds_read_b32 %0, %2\n\ts_waitcnt lgkmcnt(0)\n\tv_cmp_gt_u32 vcc, %3, %0\n\ts_cbranch_vccz Ld%=\n\t"
               "s_sub_u32 %1, %1, 1\n\ts_cbranch_scc0 Lr%=\n"
               "Ld%=:"
               : "=&v"(hv), "=&s"(cnt) : "v"(va), "i"(NEED), "i"(LSPIN_CAP) : "memory", "scc", "vcc");
}
template <int N, u32 STEP, u32 BARS> DI void rounds(u32& m, u32& so, int& pv, u32 va, u32 vb, rsrc_t rs) {
  u32 cnt;
  asm volatile("s_mov_b32 %3, %7\n"
               "L%=:\n\ts_mov_b32 m0, %0\n\ts_add_u32 %0, %0, 0x1000\n\tbuffer_load_dwordx4 %4, %6, %1 offen lds\n\t"
               "s_mov_b32 m0, %0\n\ts_add_u32 %0, %0, 0x1000\n\tbuffer_load_dwordx4 %5, %6, %1 offen lds\n\t"
               "s_add_u32 %1, %1, %10\n\t.if %9\n\ts_add_i32 %2, %2, 1\n\ts_bitcmp0_b32 %9, %2\n\t.endif\n\ts_waitcnt vmcnt(%8)\n\t"
               ".if %9\n\ts_cbranch_scc1 Lq%=\n\ts_barrier\n"
               "Lq%=:\n\t.endif\n\ts_sub_u32 %3, %3, 1\n\ts_cbranch_scc0 L%="
               : "+s"(m), "+s"(so), "+s"(pv), "=&s"(cnt) : "v"(va), "v"(vb), "s"(rs), "i"(N - 1), "i"(2 * LOOK), "i"(BARS), "i"(STEP) : "memory", "scc");
}
DI void sl2(const unsigned char* p, u32 o) { asm volatile("s_load_dword s99, %0, %1\n\ts_load_dword s99, %0, %2" ::"s"(p), "s"(o), "s"(o + 64) : "memory", "s99"); }
template <int O> DI void sl1(const unsigned char* p) { asm volatile("s_load_dword s99, %0, %1" ::"s"(p), "i"(O) : "memory", "s99"); }
template <int N> DI void stouch_taps(const unsigned char* p, u32 r) {
  static_for<0, N>([&](auto T) { sl2(p, T * 131072 + ((r + 5 * T) & 31) * 512); });
}
template <int N> DI void stouch_run(const unsigned char* p) { static_for<0, N>([&](auto T) { sl1<T * 64>(p); }); }

#define PAD(k) asm volatile("PAD%c0:\n\ts_cbranch_execz RET%c1" ::"i"(k), "i"((k) / 8))
#define HOP "s_mov_b64 exec, 0\n\ts_getpc_b64 vcc\n\ts_add_u32 vcc_lo, vcc_lo, %0\n\ts_addc_u32 vcc_hi, vcc_hi, 0\n\ts_setpc_b64 vcc\n\t"
DI void warm0(u32 wq) {
  asm volatile("s_cmp_lt_i32 %0, 12\n\ts_cbranch_scc1 RET0\n\t" HOP
               "s_branch PAD0\n\ts_branch PAD1\n\ts_branch PAD2\n\ts_branch PAD3\n\ts_branch PAD4\n\ts_branch PAD5\n\ts_branch PAD6\n\ts_branch PAD7\n"
               "RET0:\n\ts_mov_b64 exec, -1"
               :: "s"(wq * 4 - 4) : "memory", "scc", "vcc");
}
template <int S> DI void warm(u32 wv) {
  asm volatile(HOP
               "s_branch PAD%c2\n\ts_branch PAD%c3\n\ts_branch PAD%c4\n\ts_branch PAD%c5\n\ts_branch PAD%c6\n\ts_branch PAD%c7\n\ts_branch PAD%c8\n\ts_branch PAD%c9\n"
               "RET%c1:\n\ts_mov_b64 exec, -1"
               :: "s"(wv * 4 + 12), "i"(S), "i"(8 * S), "i"(8 * S + 1), "i"(8 * S + 2), "i"(8 * S + 3), "i"(8 * S + 4), "i"(8 * S + 5), "i"(8 * S + 6), "i"(8 * S + 7) : "memory", "scc", "vcc");
}

#define STAMP(i)

struct Args { const u16 *x, *w1, *w2, *gp, *wf, *wg, *wo; u16* out; unsigned char* ws; };

__global__ void __launch_bounds__(768, 3) rnnt_fused(Args unused) {
  __shared__ __attribute__((aligned(16))) unsigned char smem[LDS_BYTES];
  const u32 LB = (u32)(size_t)(LAS unsigned char*)smem;
  const u32 tid = threadIdx.x;
  v8q ka; u64 kws, pc; u32 clen;
  asm volatile("s_load_dwordx16 %0, %4, 0x0\n\ts_load_dwordx2 %1, %4, 0x40\n\ts_getpc_b64 %2\nLpc:\n\ts_mov_b32 %3, Lend-Lpc-4\n\t.subsection 1\nLend:\n\t.subsection 0" : "=&s"(ka), "=&s"(kws), "=&s"(pc), "=&s"(clen) : "s"(__builtin_amdgcn_kernarg_segment_ptr()) : "memory");
  asm volatile("s_mov_b32 m0, %1\n\ts_nop 0\n\tglobal_load_lds_dword %0, %2" :: "v"(min(((blockIdx.x >> 3) * 1536 + tid * 2) & 0x3f80, clen)), "s"(LB + L_JUNK), "s"(pc) : "memory");
  const u32 wq = __builtin_amdgcn_readfirstlane(tid >> 6), w4 = wq & 3, wv = wq - 4;
  warm0(wq);
  const u32 bid = blockIdx.x, x8 = bid & 7, j = bid >> 3, mt = j >> 1, mloc = mt & 7, bat = mt >> 3;
  const u32 k1 = x8 >> 1, n1 = 2 * (x8 & 1) + (j & 1);
  u32 lane = tid & 63;
  const u32 wbase = k1 * 32768 + n1 * 128;
  u32 w1v = ((tid >> 3) & 31) * 512 + wbase + (((tid & 7) ^ (((tid >> 3) & 2) | ((tid >> 4) & 4))) * 16);
  u32 xv = ((tid >> 3) & 31) * 512 + (mloc * 65536 - 3584 + k1 * 128) + (tid & 7) * 16;
  u32 m = LB + L_RING + w4 * 1024, mx = LB + L_X + w4 * 1056, nx = min(mloc * 128 + 137, 1000u) * 512, xo = bat * 512000;
  u32 tv = lane < 14 ? (1 + lane) * 131072 + wbase + (mt * 4 + w4) * 512 : OOB;
  asm volatile("" : "+v"(w1v), "+v"(xv), "+v"(tv));
  asm volatile("s_waitcnt lgkmcnt(0)" : "+s"(ka), "+s"(kws), "+s"(m), "+s"(mx), "+s"(nx), "+s"(xo)::"memory");
  const Args a{(const u16*)ka[0], (const u16*)ka[1], (const u16*)ka[2], (const u16*)ka[3], (const u16*)ka[4], (const u16*)ka[5], (const u16*)ka[6], (u16*)ka[7], (unsigned char*)kws};
  asm volatile("s_load_dword s98, %0, 0x0" ::"s"(a.ws) : "memory", "s98");
  const rsrc_t rw1 = mk(a.w1, 15 * 131072), rws = mk(a.ws, WS_BYTES);
  const u32 c2 = x8, n2 = j & 1;
  const u32 pi = x8 * 32 + j, ploc = pi & 127, bat3 = pi >> 7;
  const bool p3ok = ploc < 125;
#define DUMP()

  if (__builtin_expect(wq < 4, 1)) {
    {
      const rsrc_t rx = mk((const void*)(ka[0] + xo), nx);
      static_for<0, 4>([&](auto I) { dma16x(mx + I * 4224, xv + I * 16384, rx, 0); });
      if (w4 < 2) dma16x(mx + 4 * 4224, xv + 65536, rx, 0);
      dma16x(m, w1v, rw1, 0);
      dma16x(m + 4096, w1v, rw1, 16384);
    }
    dma4(LB + L_JUNK, tv, rw1);
    __builtin_amdgcn_s_setprio(SPRIO);
    STAMP(1);
    const u32 m0 = m;
    m += 8192;
    u32 so = 131072;
    int pv = 1 - LOOK;
    constexpr u32 BARS = GRP ? 0x1114 : 0x1554;
    rounds<LOOK - 1, 131072, BARS>(m, so, pv, w1v, w1v + 16384, rw1);
    lane = opq(lane);
    const u32 g = lane >> 4, l15 = lane & 15, q4 = l15 >> 2, rr8 = lane >> 3, r2 = 2 * w4 + (lane >> 5);
    const u32 w2v = opq((c2 * 32 + w4 * 4 + g) * 512 + n2 * 256 + ((((lane >> 1) & 7) ^ (g | (((w4 >> 1) & 1) << 2))) * 32) + (lane & 1) * 16), w2w = opq(w2v + 8192);
    const u32 wfv = opq(r2 * 512 + ((((lane >> 1) & 15) ^ (r2 & 7)) * 32) + (lane & 1) * 16), wfw = opq(wfv + 4096);
    const u32 wov = opq(rr8 * 512 + w4 * 128 + ((((lane >> 1) & 3) ^ (rr8 >> 1)) * 32) + (lane & 1) * 16), wow = opq(wov + 4096);
    u32 wor[4];
    static_for<0, 4>([&](auto T) { wor[T] = opq(m0 + (g >> 1) * 4096 + (4 * (g & 1) + q4) * 128 + ((T ^ (2 * (g & 1) + (q4 >> 1))) * 32) + (lane & 3) * 8); });
    const unsigned char* tp = (const unsigned char*)a.w2 + c2 * 16384 + n2 * 256 + (w4 & 1) * 128;
    const u32 tq = opq(lane < 16 ? (j * 32 + (w4 & 1) * 16 + lane) * 128 : OOB);
    const rsrc_t rt = mk(w4 < 2 ? a.wf : a.wo, 131072);
    const u32 jr = opq(LB + L_J + lane * 16);
    const u32 ov = opq((((bat3 * 250 + 2 * ploc) * 8 + l15) * 256 + w4 * 64 + (g & 1) * 16 + (g >> 1) * 8) * 2);
    if (ploc >= 126) mark(LB + L_KEY + 16 + w4 * 4, 0);
    const u32 tr = mt * 2 + (w4 >> 1);
    const unsigned char *tf = (const unsigned char*)a.wf + (j * 4 + w4) * 1024, *to = (const unsigned char*)a.wo + (j * 4 + w4) * 1024;
    if constexpr (ST == 0) { nap<SDLY>(); stouch_taps<7>(tp, tr); }
    static_assert(LOOK == 3, "waits below");
    wait_vm<5>();
    bar_raw();
    if constexpr (ST == 1) stouch_taps<7>(tp, tr);
    rounds<12, 131072, BARS>(m, so, pv, w1v, w1v + 16384, rw1);
    const rsrc_t rw2 = mk(a.w2, 15 * 131072), rwf = mk(a.wf, 131072), rwo = mk(a.wo, 131072);
    m = m0; so = 0;
    rounds<8, 131072, (1u << 14) | (1u << 15) | (1u << (15 + EXL))>(m, so, pv, w2v, w2w, rw2);
    stouch_taps<7>(tp + 8 * 131072, tr);
    if (ploc >= 126) {
      PAD(6);
      const u32 wgv = w1v - wbase + (bat3 * 2 + (pi & 1)) * 128;
      static_for<0, 2>([&](auto R) { dma16(m0 + 14 * 8192 + R * 4096, (r2 + 8 * R) * 512 + (((lane & 31) ^ (r2 + 8 * R)) * 16), mk(a.gp, 8192), 0); });
      static_for<0, 8>([&](auto R) { dma16(m0 + 10 * 8192 + R * 4096, wgv, mk(a.wg, 131072), R * 16384); });
    }
    if constexpr (NA > 8) rounds<NA - 8, 131072, 0>(m, so, pv, w2v, w2w, rw2);
    if constexpr (TG == 2) stouch_run<16>(tf);
    if constexpr (!TG) { dma4(LB + L_JUNK, tq, rt); wait_vm<1>(); } else wait_vm<0>();
    STAMP(4);
    bar_raw();
    if (ploc >= 126) {
      PAD(7);
      const u32 gr = LB + L_RING + 10 * 8192 + g * 1024 + q4 * 128 + ((w4 ^ ((q4 >> 1) + 2 * (g & 1))) * 32) + (lane & 3) * 8;
      v4u wa[8], gb[8];
      static_for<0, 8>([&](auto K) { gb[K] = lds128(LB + L_RING + 14 * 8192 + l15 * 512 + (((K * 4 + g) ^ l15) * 16)); });
      static_for<0, 8>([&](auto K) { wa[K] = ldstr2(gr + K * 4096, gr + K * 4096 + 512); });
      mark(LB + L_KEY + 16 + w4 * 4, 1);
      fence_sched();
      f32x4 d = {0, 0, 0, 0};
      static_for<0, 8>([&](auto K) { d = mfma(wa[K], gb[K], d); });
      const u32 go = WS_GE + (l15 >> 3) * 8192 + ((bat3 * 2 + (pi & 1)) * 4 + w4) * 512 + (l15 & 7) * 64 + g * 16;
      put(__builtin_bit_cast(v4u, d), rws, go, putck(go), *(const volatile LAS u32*)(size_t)(LB + L_KEY));
      marks_wait<1>(LB + L_KEY + 16 + (lane & 3) * 4);
    } else if constexpr (TG != 2) nap<GDLY>();
    if constexpr (TG == 1) dma4(LB + L_JUNK, tq, rt);
    if constexpr (TG == 2) stouch_run<16>(to);
    rounds<15 - NA, 131072, 0>(m, so, pv, w2v, w2w, rw2);
    if constexpr (TG == 3) dma4(LB + L_JUNK, tq, rt);
    if (!p3ok) {
      wait_vm<(TG == 3 ? 1 : 0)>();
      bar_raw();
      if (pi == 125 && w4 == 0) {
        u32 epoch;
        asm volatile("s_load_dword %0, %1, 0x0\n\ts_waitcnt lgkmcnt(0)" : "=s"(epoch) : "s"(a.ws) : "memory");
        epoch += 1;
        for (u32 spins = 0; spins < SPIN_CAP; ++spins) {
          v4u f4;
          asm volatile("buffer_load_dwordx4 %0, %1, %2, 0 offen sc1\n\ts_waitcnt vmcnt(0)" : "=v"(f4) : "v"(WS_FLAGS + (lane >> 2) * 128 + (lane & 3) * 16), "s"(rws) : "memory");
          if (__all(f4.x == epoch && f4.y == epoch && f4.z == epoch && f4.w == epoch)) break;
          __builtin_amdgcn_s_sleep(8);
        }
        if (lane == 0) __hip_atomic_store((GAS u32*)(a.ws + WS_EPOCH), epoch, __ATOMIC_RELAXED, __HIP_MEMORY_SCOPE_AGENT);
      }
      DUMP();
      return;
    }
    v2u WO[16][4];
    auto o_iss = [&](auto R) { dma16(m0 + L_O + (R & 1) * 8192, wov, rwo, R * 8192); dma16(m0 + L_O + (R & 1) * 8192 + 4096, wow, rwo, R * 8192); };
    auto o_rd = [&](auto R) { static_for<0, 4>([&](auto T) { WO[R][T] = ldstr(wor[T] + L_O + (R & 1) * 8192); }); };
    auto lds_done = [&]() { asm volatile("s_waitcnt lgkmcnt(0)" ::: "memory"); };
    static_for<0, 2>(o_iss);
    dma16(m0 + 15 * 8192, wfv, rwf, 15 * 8192); dma16(m0 + 15 * 8192 + 4096, wfw, rwf, 15 * 8192);
    if constexpr (OA == 4) { wait_vm<2>(); static_for<0, 2>(o_rd); lds_done(); static_for<2, 4>(o_iss); }
    else wait_vm<(TG == 3 ? 7 : 6)>();
    STAMP(5);
    bar_raw();
    m = m0; so = 0;
    if constexpr (OB > OA) {
      rounds<NA - 2, 8192, 0>(m, so, pv, wfv, wfw, rwf);
      static_for<OA - 2, OA>(o_rd);
      rounds<2, 8192, 0>(m, so, pv, wfv, wfw, rwf);
      lds_done(); static_for<OA, OB>(o_iss);
      wait_vm<2 * (OB - OA)>();
    } else {
      rounds<NA, 8192, 0>(m, so, pv, wfv, wfw, rwf);
      static_for<OA - 2, OA>(o_rd);
      wait_vm<0>();
    }
    STAMP(6);
    bar();
    dma16(m0 + L_J, lane * 16, rws, WS_GE + bat3 * 8192 + w4 * 1024); dma16(m0 + L_J + 4096, lane * 16, rws, WS_GE + bat3 * 8192 + w4 * 1024 + 4096);
    if (w4 == 0) dma16(LB + L_GC, lane < 32 ? lane * 16 : OOB, rws, WS_PL + ((WS_GE + bat3 * 8192) >> 4));
    rounds<15 - NA, 8192, 0>(m, so, pv, wfv, wfw, rwf);
    if constexpr (O0 > OB) { if constexpr (OB > OA) { static_for<OA, OB>(o_rd); lds_done(); } static_for<OB, O0>(o_iss); }
    wait_vm<0>();
    STAMP(7);
    bar_raw();
    if constexpr (O0 > OB) static_for<OB, O0>(o_rd); else static_for<OA, OB>(o_rd);
    nap<GDLY>();
    {
      auto iss = [&](auto R) {
        if constexpr (R >= O0 + 8) asm volatile("s_waitcnt lgkmcnt(8)" ::: "memory");
        dma16(m0 + ((R - O0) & 7) * 8192, wov, rwo, R * 8192); dma16(m0 + ((R - O0) & 7) * 8192 + 4096, wow, rwo, R * 8192);
      };
      PAD(21);
      static_for<O0, O0 + 3>(iss);
      static_for<O0, 16>([&](auto R) {
        if constexpr (R == O0 + NW) { STAMP(10); bar_raw(); }
        wait_vm<(R + 2 < 16 ? 4 : 2 * (15 - R))>();
        if constexpr (R + 3 < 16) iss(IC<R + 3>{});
        static_for<0, 4>([&](auto T) { WO[R][T] = ldstr(wor[T] + ((R - O0) & 7) * 8192); });
        if constexpr (R == 10) PAD(22);
        fence_sched();
      });
    }
    STAMP(8);
    bar_raw();
    STAMP(9);
    {
      PAD(23);
      f32x4 d[4] = {{0, 0, 0, 0}, {0, 0, 0, 0}, {0, 0, 0, 0}, {0, 0, 0, 0}};
      v4u b[8];
      static_for<4, 8>([&](auto K) { b[K] = lds128(jr + K * 1024); });
      fence_sched();
      static_for<0, 8>([&](auto I) {
        constexpr int K = (I + 4) & 7;
        static_for<0, 4>([&](auto T) { d[T] = mfma(v4u{WO[2 * K][T].x, WO[2 * K][T].y, WO[2 * K + 1][T].x, WO[2 * K + 1][T].y}, b[K], d[T]); });
        if constexpr (I < 4) b[I] = lds128(jr + I * 1024);
        fence_sched();
      });
      const rsrc_t ro = mk(a.out, 2048000);
      static_for<0, 2>([&](auto H) {
        const f32x4 d0 = d[2 * H], d1 = d[2 * H + 1];
        const auto r0 = __builtin_amdgcn_permlane16_swap(pk(d0.x, d0.y), pk(d1.x, d1.y), false, false);
        const auto r1 = __builtin_amdgcn_permlane16_swap(pk(d0.z, d0.w), pk(d1.z, d1.w), false, false);
        __builtin_amdgcn_raw_buffer_store_b128(v4u{r0[0], r1[0], r0[1], r1[1]}, ro, ov + H * 64, 0, 16);
      });
    }
    STAMP(14);
  } else {
    PAD(0);
    const u32 p1off = WS_P1 + k1 * 524288 + (2 * n1 + (w4 >> 1)) * 65536 + mt * 4096 + (w4 & 1) * 2048;
    if (lane < ((x8 & 3) == 3 ? 4 : 1)) {
      const u32 q = j * 8 + wv;
      const unsigned char* p = lane == 0 ? (const unsigned char*)a.w2 + 7 * 131072 + (c2 * 32 + mt * 2 + (w4 >> 1)) * 512 + n2 * 256 + (w4 & 1) * 128
                             : lane < 3 ? (const unsigned char*)a.wg + q * 512 + bat3 * 256 + (lane & 1) * 128 : (const unsigned char*)a.gp + (q & 63) * 128;
      asm volatile("s_mov_b32 m0, %1\n\ts_nop 0\n\tglobal_load_lds_dword %0, off" ::"v"(p), "s"(LB + L_JUNK) : "memory");
    }
    const u32 l15 = lane & 15, g = lane >> 4, q4 = l15 >> 2, p4 = lane & 3;
    const u32 w2r = opq(LB + L_RING + (8 * g + q4) * 256 + ((wv ^ (q4 | ((g & 1) << 2))) * 32) + p4 * 8), w2r2 = opq(w2r + 65536);
    u32 rho, rhoh, h1off[3], h1c, h1k, hc;
    {
      const u32 blk = wv >> 1, mm = (wv & 1) * 8 + q4;
      const u32 hb = WS_P1 + g * 524288 + c2 * 65536;
      rho = 8 + 4 * mm + 2 * (blk & 1);
      h1off[0] = opq(mloc * 64 + rho - 8 < 500 ? hb + mt * 4096 + wv * 512 + l15 * 16 : OOB);
      h1off[1] = opq(mloc * 64 + rho + 8 < 500 ? hb + mt * 4096 + wv * 512 + 256 + l15 * 16 : OOB);
      rho = opq(LB + L_X + (rho + (lane >> 5)) * 64 + (rho >> 2) * 32 + (blk >> 1) * 32 + p4 * 8 + (g & 1) * 4);
      const u32 nxt = l15 >> 3;
      rhoh = 72 * nxt + 4 * (q4 & 1) + 2 * (w4 & 1);
      const int hrow = (int)(mloc * 64 + rhoh) - 8;
      h1off[2] = opq((wv >= 4 && hrow >= 0 && hrow < 500) ? hb + (mt + 2 * nxt - 1) * 4096 + w4 * 1024 + (nxt ? 0 : 896) + (lane & 7) * 16 : OOB);
      rhoh = opq(LB + L_X + (rhoh + (lane >> 5)) * 64 + (rhoh >> 2) * 32 + (w4 >> 1) * 32 + p4 * 8 + (g & 1) * 4);
      const u32 sel = p4 == 0 ? h1off[0] : (p4 == 1 ? h1off[1] : (p4 == 2 ? h1off[2] : OOB));
      h1c = opq(ckoff(sel)); h1k = opq(sel == OOB ? 0 : ~0u);
    }
    const u32 ar = opq(LB + L_X + l15 * 288 + g * 16), p2v = opq(WS_P2 + c2 * 262144 + mt * 16384 + n2 * 8192 + wv * 1024 + l15 * 64 + g * 16), p2c = opq(putck(p2v));
    const u32 geoff = opq(WS_GE + bat3 * 8192 + wv * 1024 + (lane & 7) * 64 + g * 16);
    PAD(5);
    u32 wr[4];
    wr[0] = opq(LB + L_RING + (4 * g + q4) * 512 + (((2 * wv) ^ ((4 * g + q4) & 7)) * 32) + p4 * 8);
    wr[1] = opq(wr[0] ^ 32); wr[2] = opq(wr[0] + 65536); wr[3] = opq(wr[1] + 65536);
    const u32 h2off = opq(WS_P2 + (lane >> 3) * 262144 + (pi >> 4) * 16384 + (wv * 2 + ((lane >> 2) & 1)) * 1024 + (pi & 15) * 64 + p4 * 16), h2c = opq(ckoff(h2off));
    const u32 fw = opq(LB + L_F + (lane >> 5) * 576 + (wv * 32 + p4 * 8 + ((lane >> 2) & 1) * 4 + ((lane >> 3) & 3)) * 2);
    const u32 fr = opq(LB + L_F + (l15 >> 3) * 576 + g * 16), jw = opq(LB + L_J + lane * 16 + wv * 1024);
    if constexpr (W1E) warm<1>(wv);
    u32 epoch;
    asm volatile("s_waitcnt lgkmcnt(0)\n\ts_add_u32 %0, s98, 1" : "=s"(epoch)::"memory", "scc");
    const u32 key = epoch * 0x9E3779B1u | 1;
    h1k = opq(h1k & key);
    STAMP(1);
    {
      const u32 pw = wv >> 2;
      if (pw) __builtin_amdgcn_s_setprio(BPRIO);
      f32x4 acc[4] = {{0, 0, 0, 0}, {0, 0, 0, 0}, {0, 0, 0, 0}, {0, 0, 0, 0}};
      v4u G[2][11];
      v2u Wf[2][4];
      const u32 xr = opq(LB + L_X + l15 * 1056 + g * 16 + pw * 128);
      const u32 w1r = opq(LB + L_RING + pw * 8192 + g * 1024 + q4 * 128 + ((w4 ^ ((q4 >> 1) + 2 * (g & 1))) * 32) + p4 * 8), w1r2 = opq(w1r + 65536);
      const u32 p1v = opq(p1off + l15 * 64 + g * 16), p1c = opq(lane < 32 ? (p1off >> 4) + lane * 4 : OOB);
      const u32 ex = opq(LB + (w4 < 2 ? L_RING + 15 * 8192 : L_J - 8192) + w4 * 4096 + lane * 16);
      auto rdw1 = [&](auto S, auto M) { Wf[S & 1][M] = ldstr((S < 4 ? w1r : w1r2) + (S & 3) * 16384 + (M >> 1) * 4096 + (M & 1) * 512); };
      auto rdx = [&](auto C, auto U) { G[C][U] = lds128(xr + (U >> 2) * 1056 + (U & 3) * 256 + C * 64); };
      bar_raw();
      STAMP(2);
      static_for<0, 2>([&](auto C) { static_for<0, 4>([&](auto U) { rdx(C, U); }); });
      bar_raw();
      static_for<0, 4>([&](auto M) { rdw1(IC<0>{}, M); });
      asm volatile("s_nop 1" : "+v"(acc[0]), "+v"(acc[1]), "+v"(acc[2]), "+v"(acc[3]));
      fence_sched();
      f32x4 oth[4];
      auto step = [&](auto S) {
        if constexpr (S % 3 == 0) PAD(1 + S / 3);
        if constexpr (GRP && (S == 1 || S == 3 || (GRP == 1 && S == 5))) bar_raw();
        static_for<0, 8>([&](auto M) {
          constexpr bool late = !GRP || S == 0 || S == 6 || (GRP == 2 && S == 5);
          constexpr int C = M >> 2, J = M & 3, W0 = late ? BM + 1 : 0, X0 = late ? (BM < 2 ? 6 : 0) : 4;
          const v2u wa = Wf[S & 1][2 * C], wb = Wf[S & 1][2 * C + 1];
          mfma_ip(acc[J], v4u{wa.x, wa.y, wb.x, wb.y}, G[C][J + S]);
          fence_sched();
          if constexpr (S < 7 && late && M == BM) bar_raw();
          if constexpr (S == 7 && M == 4) bar_raw();
          if constexpr (S < 7 && M >= W0 && M < W0 + 4) rdw1(IC<S + 1>{}, IC<M - W0>{});
          if constexpr (S < 7 && M >= X0 && M < X0 + 2) rdx(IC<M - X0>{}, IC<S + 4>{});
          if constexpr (S == 7 && M >= 4) oth[M - 4] = *(const LAS f32x4*)(size_t)(ex + (M - 4) * 1024);
          fence_sched();
        });
      };
      static_for<0, 7>(step);
      asm volatile("s_nop 6" ::: "memory");
      fence_sched();
      if (pw) {
        static_for<0, 4>([&](auto J) { *(LAS f32x4*)(size_t)(ex + J * 1024) = acc[J]; });
        if (w4 == 0) mark(LB + L_KEY, key);
        bar();
        STAMP(3);
        __builtin_amdgcn_raw_buffer_store_b32(epoch, rws, tid == 512 ? WS_FLAGS + (bid >> 4) * 128 + (bid & 15) * 4 : OOB, 0, 16);
      } else {
        PAD(4);
        step(IC<7>{});
        asm volatile("s_nop 6" ::: "memory");
        fence_sched();
        STAMP(3);
        const v4u d0 = pack2(acc[0] + oth[0], acc[1] + oth[1]), d1 = pack2(acc[2] + oth[2], acc[3] + oth[3]);
        st_sc1(d0, rws, p1v);
        st_sc1(d1, rws, p1v + 1024);
        const auto r = __builtin_amdgcn_permlane16_swap(ck4(d0), ck4(d1), false, false);
        const u32 c = r[0] + r[1];
        const auto q = __builtin_amdgcn_permlane32_swap(c, c, false, false);
        __builtin_amdgcn_raw_buffer_store_b32(q[0] + q[1] + key, rws, p1c, WS_PL, 16);
      }
    }
    STAMP(4);
    v4u W2[15];
    auto rdw2 = [&](auto T) { const u32 ad = (T < 8 ? w2r : w2r2) + (T & 7) * 8192; W2[T] = ldstr2(ad, ad + 1024); };
    v4u hp[3];
    auto h1_load = [&]() {
      static_for<0, 2>([&](auto K) { hp[K] = ld16(rws, h1off[K]); });
      if (wv >= 4) hp[2] = ld16(rws, h1off[2]);
      hc = ldck(rws, h1c);
      mem_order();
    };
    v4u ge[2]; u32 gc[2];
    hp[2] = v4u{0, 0, 0, 0};
    nap<DLY1>();
    bar_raw();
    STAMP(5);
    h1_load();
    warm<(W1E ? 2 : 1)>(wv);
    static_for<0, NA>(rdw2);
    PAD(8);
    u32 tries = 0;
    auto h1_bad = [&]() { return __any(quadsum(ck4(hp[0]) + ck4(hp[1]) + ck4(hp[2]) + h1k - hc) != 0); };
    if (__builtin_expect(h1_bad(), 0)) {
      do { __builtin_amdgcn_s_sleep(2); h1_load(); ++tries; } while (h1_bad() && tries < SPIN_CAP);
    }
    STAMP(7);
    PAD(9);
    {
      const f32x2 s0 = red4(hp[0]), s1 = red4(hp[1]);
      *(LAS u32*)(size_t)(rho) = pk(gelu(s0.x), gelu(s0.y));
      *(LAS u32*)(size_t)(rho + 4 * 288) = pk(gelu(s1.x), gelu(s1.y));
      PAD(10);
      if (wv >= 4) {
        const f32x2 s2 = red4(hp[2]);
        *(LAS u32*)(size_t)(rhoh) = pk(gelu(s2.x), gelu(s2.y));
      }
    }
    STAMP(8);
    PAD(11);
    bar();
    STAMP(9);
    {
      f32x4 a0 = {0, 0, 0, 0}, a1 = {0, 0, 0, 0};
      v4u A[17];
      static_for<0, AW>([&](auto Q) { A[Q] = lds128(ar + ((Q + 1) >> 2) * 288 + ((Q + 1) & 3) * 64); });
      asm volatile("s_nop 1" : "+v"(a0), "+v"(a1));
      fence_sched();
      static_for<0, 15>([&](auto T) {
        mfma_ip(a0, W2[T], A[T]);
        mfma_ip(a1, W2[T], A[T + 2]);
        if constexpr (T + AW < 17) A[T + AW] = lds128(ar + ((T + AW + 1) >> 2) * 288 + ((T + AW + 1) & 3) * 64);
        if constexpr (T + NA < 15) rdw2(IC<T + NA>{});
        if constexpr (T == 8) PAD(12);
        fence_sched();
      });
      asm volatile("s_nop 7" ::: "memory");
      fence_sched();
      put(pack2(a0, a1), rws, p2v, p2c, key);
    }
    STAMP(10);
    PAD(13);
    if (!p3ok) { DUMP(); return; }
    bar_raw();
    v4u WF[8][2];
    auto rdwf = [&](auto K) { static_for<0, 2>([&](auto T) { const u32 ad = wr[(K >> 2) * 2 + T] + (K & 3) * 16384; WF[K][T] = ldstr2(ad, ad + 8192); }); };
    static_assert(NA >= 8 && NA <= 10, "slots 0..7 of w_f land before PE");
    static_for<0, 4>(rdwf);
    PAD(14);
    nap<DLY2>();
    PAD(15);
    bar();
    STAMP(11);
    {
      v4u fp = ld16(rws, h2off);
      u32 fc = ldck(rws, h2c);
      mem_order();
      if constexpr (!W1E) warm<2>(wv);
      static_for<4, 8>(rdwf);
      static_for<0, 2>([&](auto T) { ge[T] = lds128(LB + L_J + (geoff & 8191) + T * 512); gc[T] = *(const LAS u32*)(size_t)(LB + L_GC + ((ckoff(geoff) & 511) + T * 32)); });
    for (u32 spins = 0; __any(((groupsum(ck4(ge[0])) + key) ^ gc[0]) | ((groupsum(ck4(ge[1])) + key) ^ gc[1])) && spins < SPIN_CAP; ++spins) {
      __builtin_amdgcn_s_sleep(2);
      static_for<0, 2>([&](auto T) { ge[T] = ld16(rws, geoff + T * 512); gc[T] = ldck(rws, ckoff(geoff + T * 512)); });
      mem_order();
      tries += 65536;
    }
      PAD(16);
      if (__builtin_expect(__any(quadsum(ck4(fp)) + key != fc), 0)) {
        u32 spins = 0;
        do {
          __builtin_amdgcn_s_sleep(2);
          fp = ld16(rws, h2off);
          fc = ldck(rws, h2c);
          mem_order();
          tries += 256;
        } while (__any(quadsum(ck4(fp)) + key != fc) && ++spins < SPIN_CAP);
      }
      STAMP(12);
      PAD(17);
      const f32x2 s = red4(fp);
      const float snd = (lane & 8) ? s.x : s.y, kp = (lane & 8) ? s.y : s.x;
      const float tot = kp + asf(__builtin_amdgcn_update_dpp(0, (int)asu(snd), 0x128, 0xf, 0xf, false));
      *(LAS u16*)(size_t)(fw) = (u16)pk(gelu(tot), 0.f);
    }
    PAD(18);
    STAMP(14);
    bar();
    STAMP(6);
    {
      f32x4 d0 = __builtin_bit_cast(f32x4, ge[0]), d1 = __builtin_bit_cast(f32x4, ge[1]);
      v4u b[8];
      static_for<0, 8>([&](auto K) { b[K] = lds128(fr + K * 64); });
      fence_sched();
      PAD(19);
      static_for<0, 8>([&](auto K) { d0 = mfma(WF[K][0], b[K], d0); });
      fence_sched();
      float t0[4];
      static_for<0, 8>([&](auto K) {
        d1 = mfma(WF[K][1], b[K], d1);
        if constexpr (K >= 3 && K < 7) t0[K - 3] = tanh_(d0[K - 3]);
        fence_sched();
      });
      PAD(20);
      *(LAS v4u*)(size_t)(jw) = v4u{pk(t0[0], t0[1]), pk(t0[2], t0[3]), pk(tanh_(d1.x), tanh_(d1.y)), pk(tanh_(d1.z), tanh_(d1.w))};
    }
    STAMP(13);
    bar();
  }
  DUMP();
}

extern "C" void kernel_launch(void* const* d_in, const int* in_sizes, int n_in, void* d_out, int out_size, void* d_ws, size_t ws_size,
                              hipStream_t stream) {
  Args a{(const u16*)d_in[0], (const u16*)d_in[1], (const u16*)d_in[2], (const u16*)d_in[3], (const u16*)d_in[4],
         (const u16*)d_in[5], (const u16*)d_in[6], (u16*)d_out, (unsigned char*)d_ws};
  rnnt_fused<<<256, 768, 0, stream>>>(a);
}
```

```cpp
#define HPROBE 1
#include <hip/hip_runtime.h>
#include <hip/hip_bf16.h>
#include <type_traits>

typedef unsigned u32;
typedef unsigned short u16;
typedef unsigned long long u64;
typedef u32 v4u __attribute__((ext_vector_type(4)));
typedef u32 v2u __attribute__((ext_vector_type(2)));
typedef u64 v8q __attribute__((ext_vector_type(8)));
typedef float f32x4 __attribute__((ext_vector_type(4)));
typedef float f32x2 __attribute__((ext_vector_type(2)));
typedef short s16x4 __attribute__((ext_vector_type(4)));
typedef __bf16 bf16x8 __attribute__((ext_vector_type(8)));
typedef __bf16 bf16x2 __attribute__((ext_vector_type(2)));
typedef __amdgpu_buffer_rsrc_t rsrc_t;
#define GAS __attribute__((address_space(1)))
#define LAS __attribute__((address_space(3)))
#define DI __device__ __forceinline__
template <int N> using IC = std::integral_constant<int, N>;

constexpr u32 WS_EPOCH = 0;
constexpr u32 WS_FLAGS = 4096;
constexpr u32 WS_GE = 65536;
constexpr u32 WS_P1 = 1u << 20;
constexpr u32 WS_P2 = 3u << 20;
constexpr u32 WS_PL = 5u << 20;
constexpr u32 WS_BYTES = 6u << 20;
constexpr u32 OOB = 0x80000000u;
constexpr u32 SPIN_CAP = 100000;
constexpr u32 LSPIN_CAP = 4000000;
constexpr u32 L_RING = 0;
constexpr u32 L_KEY = 131072;
constexpr u32 L_JUNK = L_KEY + 64;
constexpr u32 L_F = L_JUNK + 256;
constexpr u32 L_X = L_F + 1152;
constexpr u32 L_GC = L_X;
constexpr u32 L_O = L_KEY + 8192;
constexpr u32 L_J = L_KEY + 24576;
constexpr u32 LDS_BYTES = L_J + 8192;
static_assert(L_X + 5760 <= L_O && L_X + 19008 <= L_J && LDS_BYTES <= 163840, "lds map");

#define LOOK 3
#define NA 8
#define OA 2
#define OB 2
#define O0 4
#define NW 3
#define SJ2 0
#define TG 1
#define EXL 2
#define GRP 0
#define BM 3
#define ST 0
#define TDLY 6
#define DLY1 6
#define DLY2 6
#define GDLY 4
#define SPRIO 3
#define BPRIO 1
#define AW 5
#define XAUX ""

template <int I, int N, class F> DI void static_for(F&& f) {
  if constexpr (I < N) { f(IC<I>{}); static_for<I + 1, N>(f); }
}
template <int N> DI void wait_vm() { asm volatile("s_waitcnt vmcnt(%0)" ::"i"(N) : "memory"); }
DI void bar() { asm volatile("s_waitcnt lgkmcnt(0)\n\ts_barrier" ::: "memory"); }
DI void bar_raw() { asm volatile("s_barrier" ::: "memory"); }
DI void fence_sched() { __builtin_amdgcn_sched_barrier(0); }
DI void mem_order() { asm volatile("" ::: "memory"); }
DI u32 opq(u32 x) { asm volatile("" : "+v"(x)); return x; }
template <int N> DI void nap() { if constexpr (N > 0) __builtin_amdgcn_s_sleep(N); }
DI void dma16(u32 lds, u32 voff, rsrc_t rs, u32 soff) {
  asm volatile("s_mov_b32 m0, %0\n\ts_nop 0\n\tbuffer_load_dwordx4 %1, %2, %3 offen lds" ::"s"(lds), "v"(voff), "s"(rs), "s"(soff) : "memory");
}
DI void dma16x(u32 lds, u32 voff, rsrc_t rs, u32 soff) {
  asm volatile("s_mov_b32 m0, %0\n\ts_nop 0\n\tbuffer_load_dwordx4 %1, %2, %3 offen" XAUX " lds" ::"s"(lds), "v"(voff), "s"(rs), "s"(soff) : "memory");
}
DI void dma4(u32 lds, u32 voff, rsrc_t rs) {
  asm volatile("s_mov_b32 m0, %0\n\ts_nop 0\n\tbuffer_load_dword %1, %2, 0 offen lds" ::"s"(lds), "v"(voff), "s"(rs) : "memory");
}
DI rsrc_t mk(const void* p, u32 bytes) { return __builtin_amdgcn_make_buffer_rsrc((void*)p, 0, (int)bytes, 0x00020000); }
DI v4u lds128(u32 a) { return *(const LAS v4u*)(size_t)a; }
DI v2u ldstr(u32 a) { return __builtin_bit_cast(v2u, __builtin_amdgcn_ds_read_tr16_b64_v4i16((LAS s16x4*)(size_t)a)); }
DI v4u ldstr2(u32 a0, u32 a1) { v2u x = ldstr(a0), y = ldstr(a1); return v4u{x.x, x.y, y.x, y.y}; }
DI f32x4 mfma(v4u a, v4u b, f32x4 c) {
  return __builtin_amdgcn_mfma_f32_16x16x32_bf16(__builtin_bit_cast(bf16x8, a), __builtin_bit_cast(bf16x8, b), c, 0, 0, 0);
}
DI void mfma_ip(f32x4& c, v4u a, v4u b) { asm volatile("v_mfma_f32_16x16x32_bf16 %0, %1, %2, %0" : "+v"(c) : "v"(a), "v"(b)); }
DI u32 pk(float a, float b) { f32x2 f = {a, b}; return __builtin_bit_cast(u32, __builtin_convertvector(f, bf16x2)); }
DI float asf(u32 x) { return __builtin_bit_cast(float, x); }
DI u32 asu(float x) { return __builtin_bit_cast(u32, x); }
DI float lo16(u32 x) { return asf(x << 16); }
DI float hi16(u32 x) { return asf(x & 0xFFFF0000u); }
DI float gelu(float x) {
  const float c0 = -2.0f * 0.7978845608028654f * 1.4426950408889634f, c1 = c0 * 0.044715f;
  float e = __builtin_amdgcn_exp2f(x * __builtin_fmaf(x * x, c1, c0));
  return x * __builtin_amdgcn_rcpf(1.0f + e);
}
DI float tanh_(float x) {
  float e = __builtin_amdgcn_exp2f(x * 2.8853900817779268f);
  return __builtin_fmaf(-2.0f, __builtin_amdgcn_rcpf(1.0f + e), 1.0f);
}
DI v4u pack2(f32x4 a, f32x4 b) { return v4u{pk(a.x, a.y), pk(a.z, a.w), pk(b.x, b.y), pk(b.z, b.w)}; }

DI void st_sc1(v4u v, rsrc_t rs, u32 off) { __builtin_amdgcn_raw_buffer_store_b128(v, rs, off, 0, 16); }
template <int AUX = 16> DI v4u ld16(rsrc_t rs, u32 off) { return __builtin_amdgcn_raw_buffer_load_b128(rs, off, 0, AUX); }
template <int AUX = 16> DI u32 ldck(rsrc_t rs, u32 off) { return __builtin_amdgcn_raw_buffer_load_b32(rs, off, WS_PL, AUX); }
DI u32 ckoff(u32 off) { return (off >> 4) & ~3u; }
DI u32 ck4(v4u d) { return (d.x + d.y + d.z) + d.w; }
DI u32 groupsum(u32 c) {
  const auto r = __builtin_amdgcn_permlane32_swap(c, c, false, false);
  c = r[0] + r[1];
  const auto q = __builtin_amdgcn_permlane16_swap(c, c, false, false);
  return q[0] + q[1];
}
DI u32 quadsum(u32 c) {
  c += __builtin_amdgcn_update_dpp(0, (int)c, 0xB1, 0xf, 0xf, false);
  c += __builtin_amdgcn_update_dpp(0, (int)c, 0x4E, 0xf, 0xf, false);
  return c;
}
DI u32 putck(u32 off) { return (off & 48) ? OOB : off >> 4; }
DI void put(v4u d, rsrc_t rs, u32 off, u32 coff, u32 key) {
  st_sc1(d, rs, off);
  fence_sched();
  __builtin_amdgcn_raw_buffer_store_b32(groupsum(ck4(d)) + key, rs, coff, WS_PL, 16);
}
DI f32x2 red4(v4u d) {
  const auto r0 = __builtin_amdgcn_permlane32_swap(d.x, d.z, false, false), r1 = __builtin_amdgcn_permlane32_swap(d.y, d.w, false, false);
  const float s0 = lo16(r0[0]) + lo16(r0[1]), s1 = hi16(r0[0]) + hi16(r0[1]), s2 = lo16(r1[0]) + lo16(r1[1]), s3 = hi16(r1[0]) + hi16(r1[1]);
  const auto q0 = __builtin_amdgcn_permlane16_swap(asu(s0), asu(s2), false, false);
  const auto q1 = __builtin_amdgcn_permlane16_swap(asu(s1), asu(s3), false, false);
  return f32x2{asf(q0[0]) + asf(q0[1]), asf(q1[0]) + asf(q1[1])};
}

DI void mark(u32 a, u32 v) { asm volatile("s_mov_b64 exec, 1\n\tds_write_b32 %0, %1\n\ts_mov_b64 exec, -1" ::"v"(a), "v"(v) : "memory"); }
template <u32 NEED> DI void marks_wait(u32 va) {
  u32 cnt, hv;
  asm volatile("s_mov_b32 %1, %4\n"
               "Lr%=:\n\tds_read_b32 %0, %2\n\ts_waitcnt lgkmcnt(0)\n\tv_cmp_gt_u32 vcc, %3, %0\n\ts_cbranch_vccz Ld%=\n\t"
               "s_sub_u32 %1, %1, 1\n\ts_cbranch_scc0 Lr%=\n"
               "Ld%=:"
               : "=&v"(hv), "=&s"(cnt) : "v"(va), "i"(NEED), "i"(LSPIN_CAP) : "memory", "scc", "vcc");
}
template <int N, u32 STEP, u32 BARS> DI void rounds(u32& m, u32& so, int& pv, u32 va, u32 vb, rsrc_t rs) {
  u32 cnt;
  asm volatile("s_mov_b32 %3, %7\n"
               "L%=:\n\ts_mov_b32 m0, %0\n\ts_add_u32 %0, %0, 0x1000\n\tbuffer_load_dwordx4 %4, %6, %1 offen lds\n\t"
               "s_mov_b32 m0, %0\n\ts_add_u32 %0, %0, 0x1000\n\tbuffer_load_dwordx4 %5, %6, %1 offen lds\n\t"
               "s_add_u32 %1, %1, %10\n\t.if %9\n\ts_add_i32 %2, %2, 1\n\ts_bitcmp0_b32 %9, %2\n\t.endif\n\ts_waitcnt vmcnt(%8)\n\t"
               ".if %9\n\ts_cbranch_scc1 Lq%=\n\ts_barrier\n"
               "Lq%=:\n\t.endif\n\ts_sub_u32 %3, %3, 1\n\ts_cbranch_scc0 L%="
               : "+s"(m), "+s"(so), "+s"(pv), "=&s"(cnt) : "v"(va), "v"(vb), "s"(rs), "i"(N - 1), "i"(2 * LOOK), "i"(BARS), "i"(STEP) : "memory", "scc");
}
DI void sl2(const unsigned char* p, u32 o) { asm volatile("s_load_dword s99, %0, %1\n\ts_load_dword s99, %0, %2" ::"s"(p), "s"(o), "s"(o + 64) : "memory", "s99"); }
template <int O> DI void sl1(const unsigned char* p) { asm volatile("s_load_dword s99, %0, %1" ::"s"(p), "i"(O) : "memory", "s99"); }
template <int N> DI void stouch_taps(const unsigned char* p, u32 r) {
  static_for<0, N>([&](auto T) { sl2(p, T * 131072 + ((r + 5 * T) & 31) * 512); });
}
template <int N> DI void stouch_run(const unsigned char* p) { static_for<0, N>([&](auto T) { sl1<T * 64>(p); }); }

#define PAD(k) asm volatile("PAD%c0:\n\ts_cbranch_execz RET%c1" ::"i"(k), "i"((k) / 8))
#define HOP "s_mov_b64 exec, 0\n\ts_getpc_b64 vcc\n\ts_add_u32 vcc_lo, vcc_lo, %0\n\ts_addc_u32 vcc_hi, vcc_hi, 0\n\ts_setpc_b64 vcc\n\t"
DI u32 warm0(u32 wv, const void* p) {
  u32 e, o = wv * 4 + 12;
  asm volatile("s_load_dword %1, %2, 0x0\n\t" HOP
               "s_branch PAD0\n\ts_branch PAD1\n\ts_branch PAD2\n\ts_branch PAD3\n\ts_branch PAD4\n\ts_branch PAD5\n\ts_branch PAD6\n\ts_branch PAD7\n"
               "RET0:\n\ts_mov_b64 exec, -1\n\ts_waitcnt lgkmcnt(0)"
               : "+s"(o), "=&s"(e) : "s"(p) : "memory", "scc", "vcc");
  return e;
}
template <int S> DI void warm(u32 wv) {
  asm volatile(HOP
               "s_branch PAD%c2\n\ts_branch PAD%c3\n\ts_branch PAD%c4\n\ts_branch PAD%c5\n\ts_branch PAD%c6\n\ts_branch PAD%c7\n\ts_branch PAD%c8\n\ts_branch PAD%c9\n"
               "RET%c1:\n\ts_mov_b64 exec, -1"
               :: "s"(wv * 4 + 12), "i"(S), "i"(8 * S), "i"(8 * S + 1), "i"(8 * S + 2), "i"(8 * S + 3), "i"(8 * S + 4), "i"(8 * S + 5), "i"(8 * S + 6), "i"(8 * S + 7) : "memory", "scc", "vcc");
}

DI u64 rtnow() { u64 t; asm volatile("s_memrealtime %0\n\ts_waitcnt lgkmcnt(0)" : "=s"(t)::"memory"); return t; }
#define STAMP(i) do { if constexpr ((i) == HPROBE) hp1 = rtnow(); } while (0)

struct Args { const u16 *x, *w1, *w2, *gp, *wf, *wg, *wo; u16* out; unsigned char* ws; };

__global__ void __launch_bounds__(768, 3) rnnt_fused(Args unused) {
  __shared__ __attribute__((aligned(16))) unsigned char smem[LDS_BYTES];
  const u32 LB = (u32)(size_t)(LAS unsigned char*)smem;
  const u32 tid = threadIdx.x;
  u64 hp0, hp1 = 0;
  asm volatile("s_memrealtime %0\n\ts_waitcnt lgkmcnt(0)" : "=s"(hp0)::"memory");
  v8q ka; u64 kws, pc; u32 clen;
  asm volatile("s_load_dwordx16 %0, %4, 0x0\n\ts_load_dwordx2 %1, %4, 0x40\n\ts_getpc_b64 %2\nLpc:\n\ts_mov_b32 %3, Lend-Lpc-4\n\t.subsection 1\nLend:\n\t.subsection 0" : "=&s"(ka), "=&s"(kws), "=&s"(pc), "=&s"(clen) : "s"(__builtin_amdgcn_kernarg_segment_ptr()) : "memory");
  asm volatile("s_mov_b32 m0, %1\n\ts_nop 0\n\tglobal_load_lds_dword %0, %2" :: "v"(min(((blockIdx.x >> 3) * 1536 + tid * 2) & 0x3f80, clen)), "s"(LB + L_JUNK), "s"(pc) : "memory");
  const u32 wq = __builtin_amdgcn_readfirstlane(tid >> 6), w4 = wq & 3, wv = wq - 4;
  const u32 bid = blockIdx.x, x8 = bid & 7, j = bid >> 3, mt = j >> 1, mloc = mt & 7, bat = mt >> 3;
  const u32 k1 = x8 >> 1, n1 = 2 * (x8 & 1) + (j & 1);
  u32 lane = tid & 63;
  const u32 wbase = k1 * 32768 + n1 * 128;
  u32 w1v = ((tid >> 3) & 31) * 512 + wbase + (((tid & 7) ^ (((tid >> 3) & 2) | ((tid >> 4) & 4))) * 16);
  u32 xv = ((tid >> 3) & 31) * 512 + (mloc * 65536 - 3584 + k1 * 128) + (tid & 7) * 16;
  u32 m = LB + L_RING + w4 * 1024, mx = LB + L_X + w4 * 1056, nx = min(mloc * 128 + 137, 1000u) * 512, xo = bat * 512000;
  u32 tv = lane < 14 ? (1 + lane) * 131072 + wbase + (mt * 4 + w4) * 512 : OOB;
  asm volatile("" : "+v"(w1v), "+v"(xv), "+v"(tv));
  asm volatile("s_waitcnt lgkmcnt(0)" : "+s"(ka), "+s"(kws), "+s"(m), "+s"(mx), "+s"(nx), "+s"(xo)::"memory");
  const Args a{(const u16*)ka[0], (const u16*)ka[1], (const u16*)ka[2], (const u16*)ka[3], (const u16*)ka[4], (const u16*)ka[5], (const u16*)ka[6], (u16*)ka[7], (unsigned char*)kws};
  const rsrc_t rw1 = mk(a.w1, 15 * 131072), rws = mk(a.ws, WS_BYTES);
  const u32 c2 = x8, n2 = j & 1;
  const u32 pi = x8 * 32 + j, ploc = pi & 127, bat3 = pi >> 7;
  const bool p3ok = ploc < 125;
#define DUMP()

  if (__builtin_expect(wq < 4, 1)) {
    {
      const rsrc_t rx = mk((const void*)(ka[0] + xo), nx);
      static_for<0, 4>([&](auto I) { dma16x(mx + I * 4224, xv + I * 16384, rx, 0); });
      if (w4 < 2) dma16x(mx + 4 * 4224, xv + 65536, rx, 0);
      dma16x(m, w1v, rw1, 0);
      dma16x(m + 4096, w1v, rw1, 16384);
    }
    dma4(LB + L_JUNK, tv, rw1);
    __builtin_amdgcn_s_setprio(SPRIO);

    const u32 m0 = m;
    m += 8192;
    u32 so = 131072;
    int pv = 1 - LOOK;
    constexpr u32 BARS = GRP ? 0x1114 : 0x1554;
    rounds<LOOK - 1, 131072, BARS>(m, so, pv, w1v, w1v + 16384, rw1);
    lane = opq(lane);
    const u32 g = lane >> 4, l15 = lane & 15, q4 = l15 >> 2, rr8 = lane >> 3, r2 = 2 * w4 + (lane >> 5);
    const u32 w2v = opq((c2 * 32 + w4 * 4 + g) * 512 + n2 * 256 + ((((lane >> 1) & 7) ^ (g | (((w4 >> 1) & 1) << 2))) * 32) + (lane & 1) * 16), w2w = opq(w2v + 8192);
    const u32 wfv = opq(r2 * 512 + ((((lane >> 1) & 15) ^ (r2 & 7)) * 32) + (lane & 1) * 16), wfw = opq(wfv + 4096);
    const u32 wov = opq(rr8 * 512 + w4 * 128 + ((((lane >> 1) & 3) ^ (rr8 >> 1)) * 32) + (lane & 1) * 16), wow = opq(wov + 4096);
    u32 wor[4];
    static_for<0, 4>([&](auto T) { wor[T] = opq(m0 + (g >> 1) * 4096 + (4 * (g & 1) + q4) * 128 + ((T ^ (2 * (g & 1) + (q4 >> 1))) * 32) + (lane & 3) * 8); });
    const unsigned char* tp = (const unsigned char*)a.w2 + c2 * 16384 + n2 * 256 + (w4 & 1) * 128;
    const u32 tq = opq(lane < 16 ? (j * 32 + (w4 & 1) * 16 + lane) * 128 : OOB);
    const rsrc_t rt = mk(w4 < 2 ? a.wf : a.wo, 131072);
    const u32 jr = opq(LB + L_J + lane * 16);
    const u32 ov = opq((((bat3 * 250 + 2 * ploc) * 8 + l15) * 256 + w4 * 64 + (g & 1) * 16 + (g >> 1) * 8) * 2);
    if (ploc >= 126) mark(LB + L_KEY + 16 + w4 * 4, 0);
    const u32 tr = mt * 2 + (w4 >> 1);
    const unsigned char *tf = (const unsigned char*)a.wf + (j * 4 + w4) * 1024, *to = (const unsigned char*)a.wo + (j * 4 + w4) * 1024;
    if constexpr (ST == 0) stouch_taps<7>(tp, tr);
    static_assert(LOOK == 3, "waits below");
    wait_vm<5>();
    bar_raw();
    if constexpr (ST == 1) stouch_taps<7>(tp, tr);
    rounds<12, 131072, BARS>(m, so, pv, w1v, w1v + 16384, rw1);
    const rsrc_t rw2 = mk(a.w2, 15 * 131072), rwf = mk(a.wf, 131072), rwo = mk(a.wo, 131072);
    m = m0; so = 0;
    rounds<8, 131072, (1u << 14) | (1u << 15) | (1u << (15 + EXL))>(m, so, pv, w2v, w2w, rw2);
    stouch_taps<7>(tp + 8 * 131072, tr);
    if (ploc >= 126) {
      PAD(6);
      const u32 wgv = w1v - wbase + (bat3 * 2 + (pi & 1)) * 128;
      static_for<0, 2>([&](auto R) { dma16(m0 + 14 * 8192 + R * 4096, (r2 + 8 * R) * 512 + (((lane & 31) ^ (r2 + 8 * R)) * 16), mk(a.gp, 8192), 0); });
      static_for<0, 8>([&](auto R) { dma16(m0 + 10 * 8192 + R * 4096, wgv, mk(a.wg, 131072), R * 16384); });
    }
    if constexpr (NA > 8) rounds<NA - 8, 131072, 0>(m, so, pv, w2v, w2w, rw2);
    if constexpr (TG == 2) stouch_run<16>(tf);
    if constexpr (!TG) { dma4(LB + L_JUNK, tq, rt); wait_vm<1>(); } else wait_vm<0>();

    bar_raw();
    if (ploc >= 126) {
      PAD(7);
      const u32 gr = LB + L_RING + 10 * 8192 + g * 1024 + q4 * 128 + ((w4 ^ ((q4 >> 1) + 2 * (g & 1))) * 32) + (lane & 3) * 8;
      v4u wa[8], gb[8];
      static_for<0, 8>([&](auto K) { gb[K] = lds128(LB + L_RING + 14 * 8192 + l15 * 512 + (((K * 4 + g) ^ l15) * 16)); });
      static_for<0, 8>([&](auto K) { wa[K] = ldstr2(gr + K * 4096, gr + K * 4096 + 512); });
      mark(LB + L_KEY + 16 + w4 * 4, 1);
      fence_sched();
      f32x4 d = {0, 0, 0, 0};
      static_for<0, 8>([&](auto K) { d = mfma(wa[K], gb[K], d); });
      const u32 go = WS_GE + (l15 >> 3) * 8192 + ((bat3 * 2 + (pi & 1)) * 4 + w4) * 512 + (l15 & 7) * 64 + g * 16;
      put(__builtin_bit_cast(v4u, d), rws, go, putck(go), *(const volatile LAS u32*)(size_t)(LB + L_KEY));
      marks_wait<1>(LB + L_KEY + 16 + (lane & 3) * 4);
    } else if constexpr (TG != 2) nap<GDLY>();
    if constexpr (TG == 1) dma4(LB + L_JUNK, tq, rt);
    if constexpr (TG == 2) stouch_run<16>(to);
    rounds<15 - NA, 131072, 0>(m, so, pv, w2v, w2w, rw2);
    if constexpr (TG == 3) dma4(LB + L_JUNK, tq, rt);
    if (!p3ok) {
      wait_vm<(TG == 3 ? 1 : 0)>();
      bar_raw();
      if (pi == 125 && w4 == 0) {
        u32 epoch;
        asm volatile("s_load_dword %0, %1, 0x0\n\ts_waitcnt lgkmcnt(0)" : "=s"(epoch) : "s"(a.ws) : "memory");
        epoch += 1;
        for (u32 spins = 0; spins < SPIN_CAP; ++spins) {
          v4u f4;
          asm volatile("buffer_load_dwordx4 %0, %1, %2, 0 offen sc1\n\ts_waitcnt vmcnt(0)" : "=v"(f4) : "v"(WS_FLAGS + (lane >> 2) * 128 + (lane & 3) * 16), "s"(rws) : "memory");
          if (__all(f4.x == epoch && f4.y == epoch && f4.z == epoch && f4.w == epoch)) break;
          __builtin_amdgcn_s_sleep(8);
        }
        if (lane == 0) __hip_atomic_store((GAS u32*)(a.ws + WS_EPOCH), epoch, __ATOMIC_RELAXED, __HIP_MEMORY_SCOPE_AGENT);
      }
      DUMP();
      return;
    }
    v2u WO[16][4];
    auto o_iss = [&](auto R) { dma16(m0 + L_O + (R & 1) * 8192, wov, rwo, R * 8192); dma16(m0 + L_O + (R & 1) * 8192 + 4096, wow, rwo, R * 8192); };
    auto o_rd = [&](auto R) { static_for<0, 4>([&](auto T) { WO[R][T] = ldstr(wor[T] + L_O + (R & 1) * 8192); }); };
    auto lds_done = [&]() { asm volatile("s_waitcnt lgkmcnt(0)" ::: "memory"); };
    static_for<0, 2>(o_iss);
    dma16(m0 + 15 * 8192, wfv, rwf, 15 * 8192); dma16(m0 + 15 * 8192 + 4096, wfw, rwf, 15 * 8192);
    if constexpr (OA == 4) { wait_vm<2>(); static_for<0, 2>(o_rd); lds_done(); static_for<2, 4>(o_iss); }
    else wait_vm<(TG == 3 ? 7 : 6)>();

    bar_raw();
    m = m0; so = 0;
    if constexpr (OB > OA) {
      rounds<NA - 2, 8192, 0>(m, so, pv, wfv, wfw, rwf);
      static_for<OA - 2, OA>(o_rd);
      rounds<2, 8192, 0>(m, so, pv, wfv, wfw, rwf);
      lds_done(); static_for<OA, OB>(o_iss);
      wait_vm<2 * (OB - OA)>();
    } else {
      rounds<NA, 8192, 0>(m, so, pv, wfv, wfw, rwf);
      static_for<OA - 2, OA>(o_rd);
      wait_vm<0>();
    }

    bar();
    dma16(m0 + L_J, lane * 16, rws, WS_GE + bat3 * 8192 + w4 * 1024); dma16(m0 + L_J + 4096, lane * 16, rws, WS_GE + bat3 * 8192 + w4 * 1024 + 4096);
    if (w4 == 0) dma16(LB + L_GC, lane < 32 ? lane * 16 : OOB, rws, WS_PL + ((WS_GE + bat3 * 8192) >> 4));
    rounds<15 - NA, 8192, 0>(m, so, pv, wfv, wfw, rwf);
    if constexpr (O0 > OB) { if constexpr (OB > OA) { static_for<OA, OB>(o_rd); lds_done(); } static_for<OB, O0>(o_iss); }
    wait_vm<0>();

    bar_raw();
    if constexpr (O0 > OB) static_for<OB, O0>(o_rd); else static_for<OA, OB>(o_rd);
    nap<GDLY>();
    {
      auto iss = [&](auto R) {
        if constexpr (R >= O0 + 8) asm volatile("s_waitcnt lgkmcnt(8)" ::: "memory");
        dma16(m0 + ((R - O0) & 7) * 8192, wov, rwo, R * 8192); dma16(m0 + ((R - O0) & 7) * 8192 + 4096, wow, rwo, R * 8192);
      };
      PAD(21);
      static_for<O0, O0 + 3>(iss);
      static_for<O0, 16>([&](auto R) {
        if constexpr (R == O0 + NW) {  bar_raw(); }
        wait_vm<(R + 2 < 16 ? 4 : 2 * (15 - R))>();
        if constexpr (R + 3 < 16) iss(IC<R + 3>{});
        static_for<0, 4>([&](auto T) { WO[R][T] = ldstr(wor[T] + ((R - O0) & 7) * 8192); });
        if constexpr (R == 10) PAD(22);
        fence_sched();
      });
    }

    bar_raw();

    {
      PAD(23);
      f32x4 d[4] = {{0, 0, 0, 0}, {0, 0, 0, 0}, {0, 0, 0, 0}, {0, 0, 0, 0}};
      v4u b[8];
      static_for<4, 8>([&](auto K) { b[K] = lds128(jr + K * 1024); });
      fence_sched();
      static_for<0, 8>([&](auto I) {
        constexpr int K = (I + 4) & 7;
        static_for<0, 4>([&](auto T) { d[T] = mfma(v4u{WO[2 * K][T].x, WO[2 * K][T].y, WO[2 * K + 1][T].x, WO[2 * K + 1][T].y}, b[K], d[T]); });
        if constexpr (I < 4) b[I] = lds128(jr + I * 1024);
        fence_sched();
      });
      const rsrc_t ro = mk(a.out, 2048000);
      static_for<0, 2>([&](auto H) {
        const f32x4 d0 = d[2 * H], d1 = d[2 * H + 1];
        const auto r0 = __builtin_amdgcn_permlane16_swap(pk(d0.x, d0.y), pk(d1.x, d1.y), false, false);
        const auto r1 = __builtin_amdgcn_permlane16_swap(pk(d0.z, d0.w), pk(d1.z, d1.w), false, false);
        __builtin_amdgcn_raw_buffer_store_b128(v4u{r0[0], r1[0], r0[1], r1[1]}, ro, ov + H * 64, 0, 16);
      });
    }

  } else {
    const u32 p1off = WS_P1 + k1 * 524288 + (2 * n1 + (w4 >> 1)) * 65536 + mt * 4096 + (w4 & 1) * 2048;
    if (lane < ((x8 & 3) == 3 ? 4 : 1)) {
      const u32 q = j * 8 + wv;
      const unsigned char* p = lane == 0 ? (const unsigned char*)a.w2 + 7 * 131072 + (c2 * 32 + mt * 2 + (w4 >> 1)) * 512 + n2 * 256 + (w4 & 1) * 128
                             : lane < 3 ? (const unsigned char*)a.wg + q * 512 + bat3 * 256 + (lane & 1) * 128 : (const unsigned char*)a.gp + (q & 63) * 128;
      asm volatile("s_mov_b32 m0, %1\n\ts_nop 0\n\tglobal_load_lds_dword %0, off" ::"v"(p), "s"(LB + L_JUNK) : "memory");
    }
    const u32 epoch = warm0(wv, a.ws) + 1, key = epoch * 0x9E3779B1u | 1;
    STAMP(1);
    const u32 l15 = lane & 15, g = lane >> 4, q4 = l15 >> 2, p4 = lane & 3;
    {
      PAD(0);
      const u32 pw = wv >> 2;
      if (pw) __builtin_amdgcn_s_setprio(BPRIO);
      f32x4 acc[4] = {{0, 0, 0, 0}, {0, 0, 0, 0}, {0, 0, 0, 0}, {0, 0, 0, 0}};
      v4u G[2][11];
      v2u Wf[2][4];
      const u32 xr = opq(LB + L_X + l15 * 1056 + g * 16 + pw * 128);
      const u32 w1r = opq(LB + L_RING + pw * 8192 + g * 1024 + q4 * 128 + ((w4 ^ ((q4 >> 1) + 2 * (g & 1))) * 32) + p4 * 8), w1r2 = opq(w1r + 65536);
      const u32 p1v = opq(p1off + l15 * 64 + g * 16), p1c = opq(lane < 32 ? (p1off >> 4) + lane * 4 : OOB);
      const u32 ex = opq(LB + (w4 < 2 ? L_RING + 15 * 8192 : L_J - 8192) + w4 * 4096 + lane * 16);
      auto rdw1 = [&](auto S, auto M) { Wf[S & 1][M] = ldstr((S < 4 ? w1r : w1r2) + (S & 3) * 16384 + (M >> 1) * 4096 + (M & 1) * 512); };
      auto rdx = [&](auto C, auto U) { G[C][U] = lds128(xr + (U >> 2) * 1056 + (U & 3) * 256 + C * 64); };
      bar_raw();
      STAMP(2);
      static_for<0, 2>([&](auto C) { static_for<0, 4>([&](auto U) { rdx(C, U); }); });
      bar_raw();
      static_for<0, 4>([&](auto M) { rdw1(IC<0>{}, M); });
      asm volatile("s_nop 1" : "+v"(acc[0]), "+v"(acc[1]), "+v"(acc[2]), "+v"(acc[3]));
      fence_sched();
      f32x4 oth[4];
      auto step = [&](auto S) {
        STAMP(20 + S);
        if constexpr (S % 3 == 0) PAD(1 + S / 3);
        if constexpr (GRP && (S == 1 || S == 3 || (GRP == 1 && S == 5))) bar_raw();
        static_for<0, 8>([&](auto M) {
          constexpr bool late = !GRP || S == 0 || S == 6 || (GRP == 2 && S == 5);
          constexpr int C = M >> 2, J = M & 3, W0 = late ? BM + 1 : 0, X0 = late ? (BM < 2 ? 6 : 0) : 4;
          const v2u wa = Wf[S & 1][2 * C], wb = Wf[S & 1][2 * C + 1];
          mfma_ip(acc[J], v4u{wa.x, wa.y, wb.x, wb.y}, G[C][J + S]);
          fence_sched();
          if constexpr (S < 7 && late && M == BM) bar_raw();
          if constexpr (S == 7 && M == 4) bar_raw();
          if constexpr (S < 7 && M >= W0 && M < W0 + 4) rdw1(IC<S + 1>{}, IC<M - W0>{});
          if constexpr (S < 7 && M >= X0 && M < X0 + 2) rdx(IC<M - X0>{}, IC<S + 4>{});
          if constexpr (S == 7 && M >= 4) oth[M - 4] = *(const LAS f32x4*)(size_t)(ex + (M - 4) * 1024);
          fence_sched();
        });
      };
      static_for<0, 7>(step);
      asm volatile("s_nop 6" ::: "memory");
      fence_sched();
      if (pw) {
        static_for<0, 4>([&](auto J) { *(LAS f32x4*)(size_t)(ex + J * 1024) = acc[J]; });
        if (w4 == 0) mark(LB + L_KEY, key);
        bar();
        STAMP(3);
        __builtin_amdgcn_raw_buffer_store_b32(epoch, rws, tid == 512 ? WS_FLAGS + (bid >> 4) * 128 + (bid & 15) * 4 : OOB, 0, 16);
      } else {
        PAD(4);
        step(IC<7>{});
        asm volatile("s_nop 6" ::: "memory");
        fence_sched();
        STAMP(3);
        const v4u d0 = pack2(acc[0] + oth[0], acc[1] + oth[1]), d1 = pack2(acc[2] + oth[2], acc[3] + oth[3]);
        st_sc1(d0, rws, p1v);
        st_sc1(d1, rws, p1v + 1024);
        const auto r = __builtin_amdgcn_permlane16_swap(ck4(d0), ck4(d1), false, false);
        const u32 c = r[0] + r[1];
        const auto q = __builtin_amdgcn_permlane32_swap(c, c, false, false);
        __builtin_amdgcn_raw_buffer_store_b32(q[0] + q[1] + key, rws, p1c, WS_PL, 16);
      }
    }
    STAMP(4);
    PAD(5);
    v4u W2[15];
    const u32 w2r = opq(LB + L_RING + (8 * g + q4) * 256 + ((wv ^ (q4 | ((g & 1) << 2))) * 32) + p4 * 8), w2r2 = opq(w2r + 65536);
    auto rdw2 = [&](auto T) { const u32 ad = (T < 8 ? w2r : w2r2) + (T & 7) * 8192; W2[T] = ldstr2(ad, ad + 1024); };
    v4u hp[3];
    u32 rho, rhoh, h1off[3], h1c, h1k, hc;
    {
      const u32 blk = wv >> 1, mm = (wv & 1) * 8 + q4;
      const u32 hb = WS_P1 + g * 524288 + c2 * 65536;
      rho = 8 + 4 * mm + 2 * (blk & 1);
      h1off[0] = opq(mloc * 64 + rho - 8 < 500 ? hb + mt * 4096 + wv * 512 + l15 * 16 : OOB);
      h1off[1] = opq(mloc * 64 + rho + 8 < 500 ? hb + mt * 4096 + wv * 512 + 256 + l15 * 16 : OOB);
      rho = opq(LB + L_X + (rho + (lane >> 5)) * 64 + (rho >> 2) * 32 + (blk >> 1) * 32 + p4 * 8 + (g & 1) * 4);
      const u32 nxt = l15 >> 3;
      rhoh = 72 * nxt + 4 * (q4 & 1) + 2 * (w4 & 1);
      const int hrow = (int)(mloc * 64 + rhoh) - 8;
      h1off[2] = opq((wv >= 4 && hrow >= 0 && hrow < 500) ? hb + (mt + 2 * nxt - 1) * 4096 + w4 * 1024 + (nxt ? 0 : 896) + (lane & 7) * 16 : OOB);
      rhoh = opq(LB + L_X + (rhoh + (lane >> 5)) * 64 + (rhoh >> 2) * 32 + (w4 >> 1) * 32 + p4 * 8 + (g & 1) * 4);
      const u32 sel = p4 == 0 ? h1off[0] : (p4 == 1 ? h1off[1] : (p4 == 2 ? h1off[2] : OOB));
      h1c = opq(ckoff(sel)); h1k = opq(sel == OOB ? 0 : key);
    }
    const u32 ar = opq(LB + L_X + l15 * 288 + g * 16), p2v = opq(WS_P2 + c2 * 262144 + mt * 16384 + n2 * 8192 + wv * 1024 + l15 * 64 + g * 16), p2c = opq(putck(p2v));
    auto h1_load = [&]() {
      static_for<0, 2>([&](auto K) { hp[K] = ld16(rws, h1off[K]); });
      if (wv >= 4) hp[2] = ld16(rws, h1off[2]);
      hc = ldck(rws, h1c);
      mem_order();
    };
    v4u ge[2]; u32 gc[2];
    const u32 geoff = opq(WS_GE + bat3 * 8192 + wv * 1024 + (lane & 7) * 64 + g * 16);
    hp[2] = v4u{0, 0, 0, 0};
    nap<DLY1>();
    bar_raw();
    STAMP(5);
    h1_load();
    warm<1>(wv);
    static_for<0, NA>(rdw2);
    PAD(8);
    u32 tries = 0;
    auto h1_bad = [&]() { return __any(quadsum(ck4(hp[0]) + ck4(hp[1]) + ck4(hp[2]) + h1k - hc) != 0); };
    if (__builtin_expect(h1_bad(), 0)) {
      do { __builtin_amdgcn_s_sleep(2); h1_load(); ++tries; } while (h1_bad() && tries < SPIN_CAP);
    }
    STAMP(7);
    PAD(9);
    {
      const f32x2 s0 = red4(hp[0]), s1 = red4(hp[1]);
      *(LAS u32*)(size_t)(rho) = pk(gelu(s0.x), gelu(s0.y));
      *(LAS u32*)(size_t)(rho + 4 * 288) = pk(gelu(s1.x), gelu(s1.y));
      PAD(10);
      if (wv >= 4) {
        const f32x2 s2 = red4(hp[2]);
        *(LAS u32*)(size_t)(rhoh) = pk(gelu(s2.x), gelu(s2.y));
      }
    }
    STAMP(8);
    PAD(11);
    bar();
    STAMP(9);
    {
      f32x4 a0 = {0, 0, 0, 0}, a1 = {0, 0, 0, 0};
      v4u A[17];
      static_for<0, AW>([&](auto Q) { A[Q] = lds128(ar + ((Q + 1) >> 2) * 288 + ((Q + 1) & 3) * 64); });
      asm volatile("s_nop 1" : "+v"(a0), "+v"(a1));
      fence_sched();
      static_for<0, 15>([&](auto T) {
        mfma_ip(a0, W2[T], A[T]);
        mfma_ip(a1, W2[T], A[T + 2]);
        if constexpr (T + AW < 17) A[T + AW] = lds128(ar + ((T + AW + 1) >> 2) * 288 + ((T + AW + 1) & 3) * 64);
        if constexpr (T + NA < 15) rdw2(IC<T + NA>{});
        if constexpr (T == 8) PAD(12);
        fence_sched();
      });
      asm volatile("s_nop 7" ::: "memory");
      fence_sched();
      put(pack2(a0, a1), rws, p2v, p2c, key);
    }
    STAMP(10);
    PAD(13);
    if (!p3ok) { DUMP(); return; }
    bar_raw();
    v4u WF[8][2];
    u32 wr[4];
    wr[0] = opq(LB + L_RING + (4 * g + q4) * 512 + (((2 * wv) ^ ((4 * g + q4) & 7)) * 32) + p4 * 8);
    wr[1] = opq(wr[0] ^ 32); wr[2] = opq(wr[0] + 65536); wr[3] = opq(wr[1] + 65536);
    auto rdwf = [&](auto K) { static_for<0, 2>([&](auto T) { const u32 ad = wr[(K >> 2) * 2 + T] + (K & 3) * 16384; WF[K][T] = ldstr2(ad, ad + 8192); }); };
    static_assert(NA >= 8 && NA <= 10, "slots 0..7 of w_f land before PE");
    static_for<0, 4>(rdwf);
    PAD(14);
    const u32 h2off = opq(WS_P2 + (lane >> 3) * 262144 + (pi >> 4) * 16384 + (wv * 2 + ((lane >> 2) & 1)) * 1024 + (pi & 15) * 64 + p4 * 16), h2c = opq(ckoff(h2off));
    const u32 fw = opq(LB + L_F + (lane >> 5) * 576 + (wv * 32 + p4 * 8 + ((lane >> 2) & 1) * 4 + ((lane >> 3) & 3)) * 2);
    const u32 fr = opq(LB + L_F + (l15 >> 3) * 576 + g * 16), jw = opq(LB + L_J + lane * 16 + wv * 1024);
    nap<DLY2>();
    PAD(15);
    bar();
    STAMP(11);
    {
      v4u fp = ld16(rws, h2off);
      u32 fc = ldck(rws, h2c);
      mem_order();
      warm<2>(wv);
      static_for<4, 8>(rdwf);
      static_for<0, 2>([&](auto T) { ge[T] = lds128(LB + L_J + (geoff & 8191) + T * 512); gc[T] = *(const LAS u32*)(size_t)(LB + L_GC + ((ckoff(geoff) & 511) + T * 32)); });
    for (u32 spins = 0; __any(((groupsum(ck4(ge[0])) + key) ^ gc[0]) | ((groupsum(ck4(ge[1])) + key) ^ gc[1])) && spins < SPIN_CAP; ++spins) {
      __builtin_amdgcn_s_sleep(2);
      static_for<0, 2>([&](auto T) { ge[T] = ld16(rws, geoff + T * 512); gc[T] = ldck(rws, ckoff(geoff + T * 512)); });
      mem_order();
      tries += 65536;
    }
      PAD(16);
      if (__builtin_expect(__any(quadsum(ck4(fp)) + key != fc), 0)) {
        u32 spins = 0;
        do {
          __builtin_amdgcn_s_sleep(2);
          fp = ld16(rws, h2off);
          fc = ldck(rws, h2c);
          mem_order();
          tries += 256;
        } while (__any(quadsum(ck4(fp)) + key != fc) && ++spins < SPIN_CAP);
      }
      STAMP(12);
      PAD(17);
      const f32x2 s = red4(fp);
      const float snd = (lane & 8) ? s.x : s.y, kp = (lane & 8) ? s.y : s.x;
      const float tot = kp + asf(__builtin_amdgcn_update_dpp(0, (int)asu(snd), 0x128, 0xf, 0xf, false));
      *(LAS u16*)(size_t)(fw) = (u16)pk(gelu(tot), 0.f);
    }
    PAD(18);
    STAMP(14);
    bar();
    STAMP(6);
    {
      f32x4 d0 = __builtin_bit_cast(f32x4, ge[0]), d1 = __builtin_bit_cast(f32x4, ge[1]);
      v4u b[8];
      static_for<0, 8>([&](auto K) { b[K] = lds128(fr + K * 64); });
      fence_sched();
      PAD(19);
      static_for<0, 8>([&](auto K) { d0 = mfma(WF[K][0], b[K], d0); });
      fence_sched();
      float t0[4];
      static_for<0, 8>([&](auto K) {
        d1 = mfma(WF[K][1], b[K], d1);
        if constexpr (K >= 3 && K < 7) t0[K - 3] = tanh_(d0[K - 3]);
        fence_sched();
      });
      PAD(20);
      *(LAS v4u*)(size_t)(jw) = v4u{pk(t0[0], t0[1]), pk(t0[2], t0[3]), pk(tanh_(d1.x), tanh_(d1.y)), pk(tanh_(d1.z), tanh_(d1.w))};
    }
    STAMP(13);
    bar();
    if (wq == 4 && bid == 100) { const u64 dl = hp0 + 3000 + 4 * (hp1 - hp0); for (u32 s = 0; s < 1000000 && __builtin_amdgcn_s_memrealtime() < dl; ++s) __builtin_amdgcn_s_sleep(1); }
  }
  DUMP();
}

extern "C" void kernel_launch(void* const* d_in, const int* in_sizes, int n_in, void* d_out, int out_size, void* d_ws, size_t ws_size,
                              hipStream_t stream) {
  Args a{(const u16*)d_in[0], (const u16*)d_in[1], (const u16*)d_in[2], (const u16*)d_in[3], (const u16*)d_in[4],
         (const u16*)d_in[5], (const u16*)d_in[6], (u16*)d_out, (unsigned char*)d_ws};
  rnnt_fused<<<256, 768, 0, stream>>>(a);
}
```

```cpp
#include <hip/hip_runtime.h>
#include <hip/hip_bf16.h>
#include <type_traits>

typedef unsigned u32;
typedef unsigned short u16;
typedef unsigned long long u64;
typedef u32 v4u __attribute__((ext_vector_type(4)));
typedef u32 v2u __attribute__((ext_vector_type(2)));
typedef u64 v8q __attribute__((ext_vector_type(8)));
typedef float f32x4 __attribute__((ext_vector_type(4)));
typedef float f32x2 __attribute__((ext_vector_type(2)));
typedef short s16x4 __attribute__((ext_vector_type(4)));
typedef __bf16 bf16x8 __attribute__((ext_vector_type(8)));
typedef __bf16 bf16x2 __attribute__((ext_vector_type(2)));
typedef __amdgpu_buffer_rsrc_t rsrc_t;
#define GAS __attribute__((address_space(1)))
#define LAS __attribute__((address_space(3)))
#define DI __device__ __forceinline__
template <int N> using IC = std::integral_constant<int, N>;

constexpr u32 WS_EPOCH = 0;
constexpr u32 WS_FLAGS = 4096;
constexpr u32 WS_GE = 65536;
constexpr u32 WS_P1 = 1u << 20;
constexpr u32 WS_P2 = 3u << 20;
constexpr u32 WS_PL = 5u << 20;
constexpr u32 WS_BYTES = 6u << 20;
constexpr u32 OOB = 0x80000000u;
constexpr u32 SPIN_CAP = 100000;
constexpr u32 LSPIN_CAP = 4000000;
constexpr u32 L_RING = 0;
constexpr u32 L_KEY = 131072;
constexpr u32 L_JUNK = L_KEY + 64;
constexpr u32 L_F = L_JUNK + 256;
constexpr u32 L_X = L_F + 1152;
constexpr u32 L_GC = L_X;
constexpr u32 L_O = L_KEY + 8192;
constexpr u32 L_J = L_KEY + 24576;
constexpr u32 LDS_BYTES = L_J + 8192;
static_assert(L_X + 5760 <= L_O && L_X + 19008 <= L_J && LDS_BYTES <= 163840, "lds map");

#define LOOK 3
#define NA 8
#define OA 2
#define OB 2
#define O0 4
#define NW 3
#define SJ2 0
#define TG 1
#define EXL 0
#define EDLY 10
#define GR 5
#define GRP 0
#define BM 3
#define ST 0
#define TDLY 6
#define DLY1 6
#define DLY2 6
#define GDLY 4
#define SPRIO 3
#define BPRIO 0
#define AW 5
#define XAUX ""

template <int I, int N, class F> DI void static_for(F&& f) {
  if constexpr (I < N) { f(IC<I>{}); static_for<I + 1, N>(f); }
}
template <int N> DI void wait_vm() { asm volatile("s_waitcnt vmcnt(%0)" ::"i"(N) : "memory"); }
DI void bar() { asm volatile("s_waitcnt lgkmcnt(0)\n\ts_barrier" ::: "memory"); }
DI void bar_raw() { asm volatile("s_barrier" ::: "memory"); }
DI void fence_sched() { __builtin_amdgcn_sched_barrier(0); }
DI void mem_order() { asm volatile("" ::: "memory"); }
DI u32 opq(u32 x) { asm volatile("" : "+v"(x)); return x; }
template <int N> DI void nap() { if constexpr (N > 0) __builtin_amdgcn_s_sleep(N); }
DI void dma16(u32 lds, u32 voff, rsrc_t rs, u32 soff) {
  asm volatile("s_mov_b32 m0, %0\n\ts_nop 0\n\tbuffer_load_dwordx4 %1, %2, %3 offen lds" ::"s"(lds), "v"(voff), "s"(rs), "s"(soff) : "memory");
}
DI void dma16x(u32 lds, u32 voff, rsrc_t rs, u32 soff) {
  asm volatile("s_mov_b32 m0, %0\n\ts_nop 0\n\tbuffer_load_dwordx4 %1, %2, %3 offen" XAUX " lds" ::"s"(lds), "v"(voff), "s"(rs), "s"(soff) : "memory");
}
DI void dma4(u32 lds, u32 voff, rsrc_t rs) {
  asm volatile("s_mov_b32 m0, %0\n\ts_nop 0\n\tbuffer_load_dword %1, %2, 0 offen lds" ::"s"(lds), "v"(voff), "s"(rs) : "memory");
}
DI rsrc_t mk(const void* p, u32 bytes) { return __builtin_amdgcn_make_buffer_rsrc((void*)p, 0, (int)bytes, 0x00020000); }
DI v4u lds128(u32 a) { return *(const LAS v4u*)(size_t)a; }
DI v2u ldstr(u32 a) { return __builtin_bit_cast(v2u, __builtin_amdgcn_ds_read_tr16_b64_v4i16((LAS s16x4*)(size_t)a)); }
DI v4u ldstr2(u32 a0, u32 a1) { v2u x = ldstr(a0), y = ldstr(a1); return v4u{x.x, x.y, y.x, y.y}; }
DI f32x4 mfma(v4u a, v4u b, f32x4 c) {
  return __builtin_amdgcn_mfma_f32_16x16x32_bf16(__builtin_bit_cast(bf16x8, a), __builtin_bit_cast(bf16x8, b), c, 0, 0, 0);
}
DI void mfma_ip(f32x4& c, v4u a, v4u b) { asm volatile("v_mfma_f32_16x16x32_bf16 %0, %1, %2, %0" : "+v"(c) : "v"(a), "v"(b)); }
DI u32 pk(float a, float b) { f32x2 f = {a, b}; return __builtin_bit_cast(u32, __builtin_convertvector(f, bf16x2)); }
DI float asf(u32 x) { return __builtin_bit_cast(float, x); }
DI u32 asu(float x) { return __builtin_bit_cast(u32, x); }
DI float lo16(u32 x) { return asf(x << 16); }
DI float hi16(u32 x) { return asf(x & 0xFFFF0000u); }
DI float gelu(float x) {
  const float c0 = -2.0f * 0.7978845608028654f * 1.4426950408889634f, c1 = c0 * 0.044715f;
  float e = __builtin_amdgcn_exp2f(x * __builtin_fmaf(x * x, c1, c0));
  return x * __builtin_amdgcn_rcpf(1.0f + e);
}
DI float tanh_(float x) {
  float e = __builtin_amdgcn_exp2f(x * 2.8853900817779268f);
  return __builtin_fmaf(-2.0f, __builtin_amdgcn_rcpf(1.0f + e), 1.0f);
}
DI v4u pack2(f32x4 a, f32x4 b) { return v4u{pk(a.x, a.y), pk(a.z, a.w), pk(b.x, b.y), pk(b.z, b.w)}; }

DI void st_sc1(v4u v, rsrc_t rs, u32 off) { __builtin_amdgcn_raw_buffer_store_b128(v, rs, off, 0, 16); }
template <int AUX = 16> DI v4u ld16(rsrc_t rs, u32 off) { return __builtin_amdgcn_raw_buffer_load_b128(rs, off, 0, AUX); }
template <int AUX = 16> DI u32 ldck(rsrc_t rs, u32 off) { return __builtin_amdgcn_raw_buffer_load_b32(rs, off, WS_PL, AUX); }
DI u32 ckoff(u32 off) { return (off >> 4) & ~3u; }
DI u32 ck4(v4u d) { return (d.x + d.y + d.z) + d.w; }
DI u32 groupsum(u32 c) {
  const auto r = __builtin_amdgcn_permlane32_swap(c, c, false, false);
  c = r[0] + r[1];
  const auto q = __builtin_amdgcn_permlane16_swap(c, c, false, false);
  return q[0] + q[1];
}
DI u32 quadsum(u32 c) {
  c += __builtin_amdgcn_update_dpp(0, (int)c, 0xB1, 0xf, 0xf, false);
  c += __builtin_amdgcn_update_dpp(0, (int)c, 0x4E, 0xf, 0xf, false);
  return c;
}
DI u32 putck(u32 off) { return (off & 48) ? OOB : off >> 4; }
DI void put(v4u d, rsrc_t rs, u32 off, u32 coff, u32 key) {
  st_sc1(d, rs, off);
  fence_sched();
  __builtin_amdgcn_raw_buffer_store_b32(groupsum(ck4(d)) + key, rs, coff, WS_PL, 16);
}
DI v4u sel(u32 e) {
  const u32 one = (e & 1) ? 0x3F800000u : 0x3F80u, d = e >> 1;
  return v4u{d == 0 ? one : 0, d == 1 ? one : 0, d == 2 ? one : 0, d == 3 ? one : 0};
}
DI v4u opq4(v4u x) { asm volatile("" : "+v"(x)); return x; }

DI void mark(u32 a, u32 v) { asm volatile("s_mov_b64 exec, 1\n\tds_write_b32 %0, %1\n\ts_mov_b64 exec, -1" ::"v"(a), "v"(v) : "memory"); }
template <u32 NEED> DI void marks_wait(u32 va) {
  u32 cnt, hv;
  asm volatile("s_mov_b32 %1, %4\n"
               "Lr%=:\n\tds_read_b32 %0, %2\n\ts_waitcnt lgkmcnt(0)\n\tv_cmp_gt_u32 vcc, %3, %0\n\ts_cbranch_vccz Ld%=\n\t"
               "s_sub_u32 %1, %1, 1\n\ts_cbranch_scc0 Lr%=\n"
               "Ld%=:"
               : "=&v"(hv), "=&s"(cnt) : "v"(va), "i"(NEED), "i"(LSPIN_CAP) : "memory", "scc", "vcc");
}
template <int N, u32 STEP, u32 BARS> DI void rounds(u32& m, u32& so, int& pv, u32 va, u32 vb, rsrc_t rs) {
  u32 cnt;
  asm volatile("s_mov_b32 %3, %7\n"
               "L%=:\n\ts_mov_b32 m0, %0\n\ts_add_u32 %0, %0, 0x1000\n\tbuffer_load_dwordx4 %4, %6, %1 offen lds\n\t"
               "s_mov_b32 m0, %0\n\ts_add_u32 %0, %0, 0x1000\n\tbuffer_load_dwordx4 %5, %6, %1 offen lds\n\t"
               "s_add_u32 %1, %1, %10\n\t.if %9\n\ts_add_i32 %2, %2, 1\n\ts_bitcmp0_b32 %9, %2\n\t.endif\n\ts_waitcnt vmcnt(%8)\n\t"
               ".if %9\n\ts_cbranch_scc1 Lq%=\n\ts_barrier\n"
               "Lq%=:\n\t.endif\n\ts_sub_u32 %3, %3, 1\n\ts_cbranch_scc0 L%="
               : "+s"(m), "+s"(so), "+s"(pv), "=&s"(cnt) : "v"(va), "v"(vb), "s"(rs), "i"(N - 1), "i"(2 * LOOK), "i"(BARS), "i"(STEP) : "memory", "scc");
}
#define PAD(k) asm volatile("PAD%c0:\n\ts_cbranch_execz RET%c1" ::"i"(k), "i"((k) / 8))
#define HOP "s_mov_b64 exec, 0\n\ts_getpc_b64 vcc\n\ts_add_u32 vcc_lo, vcc_lo, %0\n\ts_addc_u32 vcc_hi, vcc_hi, 0\n\ts_setpc_b64 vcc\n\t"
DI u32 warm0(u32 wv, const void* p) {
  u32 e, o = wv * 4 + 12;
  asm volatile("s_load_dword %1, %2, 0x0\n\t" HOP
               "s_branch PAD0\n\ts_branch PAD1\n\ts_branch PAD2\n\ts_branch PAD3\n\ts_branch PAD4\n\ts_branch PAD5\n\ts_branch PAD6\n\ts_branch PAD7\n"
               "RET0:\n\ts_mov_b64 exec, -1\n\ts_waitcnt lgkmcnt(0)"
               : "+s"(o), "=&s"(e) : "s"(p) : "memory", "scc", "vcc");
  return e;
}
template <int S> DI void warm(u32 wv) {
  asm volatile(HOP
               "s_branch PAD%c2\n\ts_branch PAD%c3\n\ts_branch PAD%c4\n\ts_branch PAD%c5\n\ts_branch PAD%c6\n\ts_branch PAD%c7\n\ts_branch PAD%c8\n\ts_branch PAD%c9\n"
               "RET%c1:\n\ts_mov_b64 exec, -1"
               :: "s"(wv * 4 + 12), "i"(S), "i"(8 * S), "i"(8 * S + 1), "i"(8 * S + 2), "i"(8 * S + 3), "i"(8 * S + 4), "i"(8 * S + 5), "i"(8 * S + 6), "i"(8 * S + 7) : "memory", "scc", "vcc");
}

#define STAMP(i)

struct Args { const u16 *x, *w1, *w2, *gp, *wf, *wg, *wo; u16* out; unsigned char* ws; };

__global__ void __launch_bounds__(768, 3) rnnt_fused(Args unused) {
  __shared__ __attribute__((aligned(16))) unsigned char smem[LDS_BYTES];
  const u32 LB = (u32)(size_t)(LAS unsigned char*)smem;
  const u32 tid = threadIdx.x;
  v8q ka; u64 kws, pc; u32 clen;
  asm volatile("s_load_dwordx16 %0, %4, 0x0\n\ts_load_dwordx2 %1, %4, 0x40\n\ts_getpc_b64 %2\nLpc:\n\ts_mov_b32 %3, Lend-Lpc-4\n\t.subsection 1\nLend:\n\t.subsection 0" : "=&s"(ka), "=&s"(kws), "=&s"(pc), "=&s"(clen) : "s"(__builtin_amdgcn_kernarg_segment_ptr()) : "memory");
  asm volatile("s_mov_b32 m0, %1\n\ts_nop 0\n\tglobal_load_lds_dword %0, %2" :: "v"(min(((blockIdx.x >> 3) * 1536 + tid * 2) & 0x3f80, clen)), "s"(LB + L_JUNK), "s"(pc) : "memory");
  const u32 wq = __builtin_amdgcn_readfirstlane(tid >> 6), w4 = wq & 3, wv = wq - 4;
  const u32 bid = blockIdx.x, x8 = bid & 7, j = bid >> 3, mt = j >> 1, mloc = mt & 7, bat = mt >> 3;
  const u32 k1 = x8 >> 1, n1 = 2 * (x8 & 1) + (j & 1);
  u32 lane = tid & 63;
  const u32 wbase = k1 * 32768 + n1 * 128;
  u32 w1v = ((tid >> 3) & 31) * 512 + wbase + (((tid & 7) ^ (((tid >> 3) & 2) | ((tid >> 4) & 4))) * 16);
  u32 xv = ((tid >> 3) & 31) * 512 + (mloc * 65536 - 3584 + k1 * 128) + (tid & 7) * 16;
  u32 m = LB + L_RING + w4 * 1024, mx = LB + L_X + w4 * 1056, nx = min(mloc * 128 + 137, 1000u) * 512, xo = bat * 512000;
  u32 tv = lane < 14 ? (1 + lane) * 131072 + wbase + (mt * 4 + w4) * 512 : OOB;
  asm volatile("" : "+v"(w1v), "+v"(xv), "+v"(tv));
  asm volatile("s_waitcnt lgkmcnt(0)" : "+s"(ka), "+s"(kws), "+s"(m), "+s"(mx), "+s"(nx), "+s"(xo)::"memory");
  const Args a{(const u16*)ka[0], (const u16*)ka[1], (const u16*)ka[2], (const u16*)ka[3], (const u16*)ka[4], (const u16*)ka[5], (const u16*)ka[6], (u16*)ka[7], (unsigned char*)kws};
  const rsrc_t rw1 = mk(a.w1, 15 * 131072), rws = mk(a.ws, WS_BYTES);
  const u32 c2 = x8, n2 = j & 1;
  const u32 pi = x8 * 32 + j, ploc = pi & 127, bat3 = pi >> 7;
  const bool p3ok = ploc < 125;
#define DUMP()

  if (__builtin_expect(wq < 4, 1)) {
    {
      const rsrc_t rx = mk((const void*)(ka[0] + xo), nx);
      static_for<0, 4>([&](auto I) { dma16x(mx + I * 4224, xv + I * 16384, rx, 0); });
      if (w4 < 2) dma16x(mx + 4 * 4224, xv + 65536, rx, 0);
      dma16x(m, w1v, rw1, 0);
      dma16x(m + 4096, w1v, rw1, 16384);
    }
    dma4(LB + L_JUNK, tv, rw1);
    __builtin_amdgcn_s_setprio(SPRIO);
    STAMP(1);
    const u32 m0 = m;
    m += 8192;
    u32 so = 131072;
    int pv = 1 - LOOK;
    constexpr u32 BARS = GRP ? 0x1114 : 0x1554;
    rounds<LOOK - 1, 131072, BARS>(m, so, pv, w1v, w1v + 16384, rw1);
    lane = opq(lane);
    const u32 g = lane >> 4, l15 = lane & 15, q4 = l15 >> 2, rr8 = lane >> 3, r2 = 2 * w4 + (lane >> 5);
    const u32 w2v = opq((c2 * 32 + w4 * 4 + g) * 512 + n2 * 256 + ((((lane >> 1) & 7) ^ (g | (((w4 >> 1) & 1) << 2))) * 32) + (lane & 1) * 16), w2w = opq(w2v + 8192);
    const u32 wfv = opq(r2 * 512 + ((((lane >> 1) & 15) ^ (r2 & 7)) * 32) + (lane & 1) * 16), wfw = opq(wfv + 4096);
    const u32 wov = opq(rr8 * 512 + w4 * 128 + ((((lane >> 1) & 3) ^ (rr8 >> 1)) * 32) + (lane & 1) * 16), wow = opq(wov + 4096);
    u32 wor[4];
    static_for<0, 4>([&](auto T) { wor[T] = opq(m0 + (g >> 1) * 4096 + (4 * (g & 1) + q4) * 128 + ((T ^ (2 * (g & 1) + (q4 >> 1))) * 32) + (lane & 3) * 8); });
    u32 tq;
    {
      const u32 ln = mt * 60 + w4 * 30 + lane;
      const u32 t2 = lane < 30 ? (ln >> 6) * 131072 + (c2 * 32 + ((ln >> 1) & 31)) * 512 + n2 * 256 + (ln & 1) * 128 : OOB;
      tq = opq(w4 < 2 ? t2 : (lane < 32 ? (j * 32 + lane) * 128 : OOB));
    }
    const rsrc_t rt = mk(w4 < 2 ? a.w2 : (w4 == 2 ? a.wo : a.wf), w4 < 2 ? 15 * 131072 : 131072);
    const u32 jr = opq(LB + L_J + lane * 16);
    const u32 ov = opq((((bat3 * 250 + 2 * ploc) * 8 + l15) * 256 + w4 * 64 + (g & 1) * 16 + (g >> 1) * 8) * 2);
    if (ploc >= 126) mark(LB + L_KEY + 16 + w4 * 4, 0);
    static_assert(LOOK == 3, "waits below");
    wait_vm<5>();
    bar_raw();
    rounds<12, 131072, BARS>(m, so, pv, w1v, w1v + 16384, rw1);
    const rsrc_t rw2 = mk(a.w2, 15 * 131072), rwf = mk(a.wf, 131072), rwo = mk(a.wo, 131072);
    m = m0; so = 0;
    dma4(LB + L_JUNK, tq, rt);
    wait_vm<3>();
    bar_raw();
    wait_vm<1>();
    bar_raw();
    wait_vm<0>();
    nap<EDLY>();
    rounds<NA, 131072, 0>(m, so, pv, w2v, w2w, rw2);
    wait_vm<0>();
    STAMP(4);
    bar_raw();
    nap<GDLY>();
    rounds<15 - NA, 131072, 0>(m, so, pv, w2v, w2w, rw2);
    if (!p3ok) {
      if (ploc >= 126) {
        PAD(6);
        auto at = [](int R) constexpr { return R < 2 ? 15 * 8192 + R * 4096 : L_O + (R - 2) * 4096; };
        static_assert(L_J == L_O + 16384, "24 KB in a row");
        const u32 wgv = w1v - wbase + (bat3 * 2 + (pi & 1)) * 128;
        static_for<0, 6>([&](auto R) { dma16(m0 + at(R), wgv, mk(a.wg, 131072), R * 16384); });
        wait_vm<6>();
        bar_raw();
        static_for<6, 8>([&](auto R) { dma16(m0 + at(R), wgv, mk(a.wg, 131072), R * 16384); });
        static_for<0, 2>([&](auto R) { dma16(m0 + R * 4096, (r2 + 8 * R) * 512 + (((lane & 31) ^ (r2 + 8 * R)) * 16), mk(a.gp, 8192), 0); });
        wait_vm<0>();
        mark(LB + L_KEY + 16 + w4 * 4, 1);
        marks_wait<1>(LB + L_KEY + 16 + (lane & 3) * 4);
        PAD(7);
        const u32 gr = LB + L_RING + g * 1024 + q4 * 128 + ((w4 ^ ((q4 >> 1) + 2 * (g & 1))) * 32) + (lane & 3) * 8;
        v4u wa[8], gb[8];
        static_for<0, 8>([&](auto K) { gb[K] = lds128(LB + L_RING + l15 * 512 + (((K * 4 + g) ^ l15) * 16)); });
        static_for<0, 8>([&](auto K) { wa[K] = ldstr2(gr + at(K), gr + at(K) + 512); });
        fence_sched();
        f32x4 d = {0, 0, 0, 0};
        static_for<0, 8>([&](auto K) { d = mfma(wa[K], gb[K], d); });
        const u32 go = WS_GE + (l15 >> 3) * 8192 + ((bat3 * 2 + (pi & 1)) * 4 + w4) * 512 + (l15 & 7) * 64 + g * 16;
        put(__builtin_bit_cast(v4u, d), rws, go, putck(go), *(const volatile LAS u32*)(size_t)(LB + L_KEY));
        DUMP();
        return;
      }
      wait_vm<0>();
      bar_raw();
      if (pi == 125 && w4 == 0) {
        u32 epoch;
        asm volatile("s_load_dword %0, %1, 0x0\n\ts_waitcnt lgkmcnt(0)" : "=s"(epoch) : "s"(a.ws) : "memory");
        epoch += 1;
        for (u32 spins = 0; spins < SPIN_CAP; ++spins) {
          v4u f4;
          asm volatile("buffer_load_dwordx4 %0, %1, %2, 0 offen sc1\n\ts_waitcnt vmcnt(0)" : "=v"(f4) : "v"(WS_FLAGS + (lane >> 2) * 128 + (lane & 3) * 16), "s"(rws) : "memory");
          if (__all(f4.x == epoch && f4.y == epoch && f4.z == epoch && f4.w == epoch)) break;
          __builtin_amdgcn_s_sleep(8);
        }
        if (lane == 0) __hip_atomic_store((GAS u32*)(a.ws + WS_EPOCH), epoch, __ATOMIC_RELAXED, __HIP_MEMORY_SCOPE_AGENT);
      }
      DUMP();
      return;
    }
    v2u WO[16][4];
    auto o_iss = [&](auto R) { dma16(m0 + L_O + (R & 1) * 8192, wov, rwo, R * 8192); dma16(m0 + L_O + (R & 1) * 8192 + 4096, wow, rwo, R * 8192); };
    auto o_rd = [&](auto R) { static_for<0, 4>([&](auto T) { WO[R][T] = ldstr(wor[T] + L_O + (R & 1) * 8192); }); };
    auto lds_done = [&]() { asm volatile("s_waitcnt lgkmcnt(0)" ::: "memory"); };
    static_for<0, 2>(o_iss);
    dma16(m0 + 15 * 8192, wfv, rwf, 15 * 8192); dma16(m0 + 15 * 8192 + 4096, wfw, rwf, 15 * 8192);
    if constexpr (OA == 4) { wait_vm<2>(); static_for<0, 2>(o_rd); lds_done(); static_for<2, 4>(o_iss); }
    else wait_vm<6>();
    STAMP(5);
    bar_raw();
    m = m0; so = 0;
    if constexpr (OB > OA) {
      rounds<NA - 2, 8192, 0>(m, so, pv, wfv, wfw, rwf);
      static_for<OA - 2, OA>(o_rd);
      rounds<2, 8192, 0>(m, so, pv, wfv, wfw, rwf);
      lds_done(); static_for<OA, OB>(o_iss);
      wait_vm<2 * (OB - OA)>();
    } else {
      rounds<NA, 8192, 0>(m, so, pv, wfv, wfw, rwf);
      static_for<OA - 2, OA>(o_rd);
      wait_vm<0>();
    }
    STAMP(6);
    bar();
    rounds<GR, 8192, 0>(m, so, pv, wfv, wfw, rwf);
    dma16(m0 + L_J, lane * 16, rws, WS_GE + bat3 * 8192 + w4 * 1024); dma16(m0 + L_J + 4096, lane * 16, rws, WS_GE + bat3 * 8192 + w4 * 1024 + 4096);
    if (w4 == 0) dma16(LB + L_GC, lane < 32 ? lane * 16 : OOB, rws, WS_PL + ((WS_GE + bat3 * 8192) >> 4));
    if constexpr (15 - NA - GR > 0) rounds<15 - NA - GR, 8192, 0>(m, so, pv, wfv, wfw, rwf);
    if constexpr (O0 > OB) { if constexpr (OB > OA) { static_for<OA, OB>(o_rd); lds_done(); } static_for<OB, O0>(o_iss); }
    wait_vm<0>();
    STAMP(7);
    bar_raw();
    if constexpr (O0 > OB) static_for<OB, O0>(o_rd); else static_for<OA, OB>(o_rd);
    nap<GDLY>();
    {
      auto iss = [&](auto R) {
        if constexpr (R >= O0 + 8) asm volatile("s_waitcnt lgkmcnt(8)" ::: "memory");
        dma16(m0 + ((R - O0) & 7) * 8192, wov, rwo, R * 8192); dma16(m0 + ((R - O0) & 7) * 8192 + 4096, wow, rwo, R * 8192);
      };
      PAD(21);
      static_for<O0, O0 + 3>(iss);
      static_for<O0, 16>([&](auto R) {
        if constexpr (R == O0 + NW) { STAMP(10); bar_raw(); }
        wait_vm<(R + 2 < 16 ? 4 : 2 * (15 - R))>();
        if constexpr (R + 3 < 16) iss(IC<R + 3>{});
        static_for<0, 4>([&](auto T) { WO[R][T] = ldstr(wor[T] + ((R - O0) & 7) * 8192); });
        if constexpr (R == 10) PAD(22);
        fence_sched();
      });
    }
    STAMP(8);
    bar_raw();
    STAMP(9);
    {
      PAD(23);
      f32x4 d[4] = {{0, 0, 0, 0}, {0, 0, 0, 0}, {0, 0, 0, 0}, {0, 0, 0, 0}};
      v4u b[8];
      static_for<4, 8>([&](auto K) { b[K] = lds128(jr + K * 1024); });
      fence_sched();
      static_for<0, 8>([&](auto I) {
        constexpr int K = (I + 4) & 7;
        static_for<0, 4>([&](auto T) { d[T] = mfma(v4u{WO[2 * K][T].x, WO[2 * K][T].y, WO[2 * K + 1][T].x, WO[2 * K + 1][T].y}, b[K], d[T]); });
        if constexpr (I < 4) b[I] = lds128(jr + I * 1024);
        fence_sched();
      });
      const rsrc_t ro = mk(a.out, 2048000);
      static_for<0, 2>([&](auto H) {
        const f32x4 d0 = d[2 * H], d1 = d[2 * H + 1];
        const auto r0 = __builtin_amdgcn_permlane16_swap(pk(d0.x, d0.y), pk(d1.x, d1.y), false, false);
        const auto r1 = __builtin_amdgcn_permlane16_swap(pk(d0.z, d0.w), pk(d1.z, d1.w), false, false);
        __builtin_amdgcn_raw_buffer_store_b128(v4u{r0[0], r1[0], r0[1], r1[1]}, ro, ov + H * 64, 0, 16);
      });
    }
    STAMP(14);
  } else {
    const u32 p1off = WS_P1 + k1 * 524288 + (2 * n1 + (w4 >> 1)) * 65536 + mt * 4096 + (w4 & 1) * 2048;
    if ((x8 & 3) == 3 && lane < 3) {
      const u32 q = j * 8 + wv;
      const unsigned char* p = lane < 2 ? (const unsigned char*)a.wg + q * 512 + bat3 * 256 + lane * 128 : (const unsigned char*)a.gp + (q & 63) * 128;
      asm volatile("s_mov_b32 m0, %1\n\ts_nop 0\n\tglobal_load_lds_dword %0, off" ::"v"(p), "s"(LB + L_JUNK) : "memory");
    }
    const u32 epoch = warm0(wv, a.ws) + 1, key = epoch * 0x9E3779B1u | 1;
    STAMP(1);
    const u32 l15 = lane & 15, g = lane >> 4, q4 = l15 >> 2, p4 = lane & 3;
    {
      PAD(0);
      const u32 pw = wv >> 2;
      if (pw) __builtin_amdgcn_s_setprio(BPRIO);
      f32x4 acc[4] = {{0, 0, 0, 0}, {0, 0, 0, 0}, {0, 0, 0, 0}, {0, 0, 0, 0}};
      v4u G[2][11];
      v2u Wf[2][4];
      const u32 xr = opq(LB + L_X + l15 * 1056 + g * 16 + pw * 128);
      const u32 w1r = opq(LB + L_RING + pw * 8192 + g * 1024 + q4 * 128 + ((w4 ^ ((q4 >> 1) + 2 * (g & 1))) * 32) + p4 * 8), w1r2 = opq(w1r + 65536);
      const u32 p1v = opq(p1off + l15 * 64 + g * 16), p1c = opq(lane < 32 ? (p1off >> 4) + lane * 4 : OOB);
      const u32 ex = opq(LB + (w4 < 2 ? L_RING + 15 * 8192 : L_J - 8192) + w4 * 4096 + lane * 16);
      auto rdw1 = [&](auto S, auto M) { Wf[S & 1][M] = ldstr((S < 4 ? w1r : w1r2) + (S & 3) * 16384 + (M >> 1) * 4096 + (M & 1) * 512); };
      auto rdx = [&](auto C, auto U) { G[C][U] = lds128(xr + (U >> 2) * 1056 + (U & 3) * 256 + C * 64); };
      bar_raw();
      STAMP(2);
      static_for<0, 2>([&](auto C) { static_for<0, 4>([&](auto U) { rdx(C, U); }); });
      bar_raw();
      static_for<0, 4>([&](auto M) { rdw1(IC<0>{}, M); });
      asm volatile("s_nop 1" : "+v"(acc[0]), "+v"(acc[1]), "+v"(acc[2]), "+v"(acc[3]));
      fence_sched();
      f32x4 oth[4];
      auto step = [&](auto S) {
        if constexpr (S % 3 == 0) PAD(1 + S / 3);
        if constexpr (GRP && (S == 1 || S == 3 || (GRP == 1 && S == 5))) bar_raw();
        static_for<0, 8>([&](auto M) {
          constexpr bool late = !GRP || S == 0 || S == 6 || (GRP == 2 && S == 5);
          constexpr int C = M >> 2, J = M & 3, W0 = late ? BM + 1 : 0, X0 = late ? (BM < 2 ? 6 : 0) : 4;
          const v2u wa = Wf[S & 1][2 * C], wb = Wf[S & 1][2 * C + 1];
          mfma_ip(acc[J], v4u{wa.x, wa.y, wb.x, wb.y}, G[C][J + S]);
          fence_sched();
          if constexpr (S < 6 && late && M == BM) bar_raw();
          if constexpr (S < 6 && M >= W0 && M < W0 + 4) rdw1(IC<S + 1>{}, IC<M - W0>{});
          if constexpr (S < 7 && M >= X0 && M < X0 + 2) rdx(IC<M - X0>{}, IC<S + 4>{});
          if constexpr (S == 7 && M < 4) oth[M] = *(const LAS f32x4*)(size_t)(ex + M * 1024);
          fence_sched();
        });
      };
      static_for<0, 7>(step);
      asm volatile("s_nop 7" ::: "memory");
      fence_sched();
      if (pw) {
        static_for<0, 4>([&](auto J) { *(LAS f32x4*)(size_t)(ex + J * 1024) = acc[J]; });
        if (w4 == 0) mark(LB + L_KEY, key);
        bar();
        STAMP(3);
        __builtin_amdgcn_raw_buffer_store_b32(epoch, rws, tid == 512 ? WS_FLAGS + (bid >> 4) * 128 + (bid & 15) * 4 : OOB, 0, 16);
      } else {
        PAD(4);
        bar_raw();
        static_for<0, 4>([&](auto M) { rdw1(IC<7>{}, M); });
        fence_sched();
        step(IC<7>{});
        asm volatile("s_nop 7" ::: "memory");
        fence_sched();
        STAMP(3);
        const v4u d0 = pack2(acc[0] + oth[0], acc[1] + oth[1]), d1 = pack2(acc[2] + oth[2], acc[3] + oth[3]);
        st_sc1(d0, rws, p1v);
        st_sc1(d1, rws, p1v + 1024);
        const auto r = __builtin_amdgcn_permlane16_swap(ck4(d0), ck4(d1), false, false);
        const u32 c = r[0] + r[1];
        const auto q = __builtin_amdgcn_permlane32_swap(c, c, false, false);
        __builtin_amdgcn_raw_buffer_store_b32(q[0] + q[1] + key, rws, p1c, WS_PL, 16);
      }
    }
    STAMP(4);
    PAD(5);
    v4u W2[15];
    const u32 w2r = opq(LB + L_RING + (8 * g + q4) * 256 + ((wv ^ (q4 | ((g & 1) << 2))) * 32) + p4 * 8), w2r2 = opq(w2r + 65536);
    auto rdw2 = [&](auto T) { const u32 ad = (T < 8 ? w2r : w2r2) + (T & 7) * 8192; W2[T] = ldstr2(ad, ad + 1024); };
    v4u hp[3];
    u32 rho, rhoh, h1off[3], h1c, h1k, hc;
    {
      const u32 blk = wv >> 1, mm = (wv & 1) * 8 + q4;
      const u32 hb = WS_P1 + g * 524288 + c2 * 65536;
      rho = 8 + 4 * mm + 2 * (blk & 1);
      h1off[0] = opq(mloc * 64 + rho - 8 < 500 ? hb + mt * 4096 + wv * 512 + l15 * 16 : OOB);
      h1off[1] = opq(mloc * 64 + rho + 8 < 500 ? hb + mt * 4096 + wv * 512 + 256 + l15 * 16 : OOB);
      rho = opq(LB + L_X + (rho + (g & 1)) * 64 + (rho >> 2) * 32 + (lane >> 5) * 4 * 288 + (blk >> 1) * 32 + p4 * 8);
      const u32 nxt = l15 >> 3;
      rhoh = 72 * nxt + 4 * (q4 & 1) + 2 * (w4 & 1);
      const int hrow = (int)(mloc * 64 + rhoh) - 8;
      h1off[2] = opq((wv >= 4 && hrow >= 0 && hrow < 500) ? hb + (mt + 2 * nxt - 1) * 4096 + w4 * 1024 + (nxt ? 0 : 896) + (lane & 7) * 16 : OOB);
      rhoh = opq(LB + L_X + (rhoh + (lane >> 5)) * 64 + (rhoh >> 2) * 32 + (w4 >> 1) * 32 + p4 * 8 + (g & 1) * 4);
      const u32 sel = p4 == 0 ? h1off[0] : (p4 == 1 ? h1off[1] : (p4 == 2 ? h1off[2] : OOB));
      h1c = opq(ckoff(sel)); h1k = opq(sel == OOB ? 0 : key);
    }
    const u32 ar = opq(LB + L_X + l15 * 288 + g * 16), p2v = opq(WS_P2 + c2 * 262144 + mt * 16384 + n2 * 8192 + wv * 1024 + l15 * 64 + g * 16), p2c = opq(putck(p2v));
    const v4u selA = opq4(sel(l15)), selB = opq4(sel(l15 - 8)), selH = opq4(sel((l15 & 2) ? 8 : 2 * q4 + (l15 & 1)));
    auto h1_load = [&]() {
      static_for<0, 2>([&](auto K) { hp[K] = ld16(rws, h1off[K]); });
      if (wv >= 4) hp[2] = ld16(rws, h1off[2]);
      hc = ldck(rws, h1c);
      mem_order();
    };
    v4u ge[2]; u32 gc[2];
    const u32 geoff = opq(WS_GE + bat3 * 8192 + wv * 1024 + (lane & 7) * 64 + g * 16);
    hp[2] = v4u{0, 0, 0, 0};
    nap<DLY1>();
    bar_raw();
    STAMP(5);
    h1_load();
    warm<1>(wv);
    static_for<0, NA>(rdw2);
    PAD(8);
    u32 tries = 0;
    auto h1_bad = [&]() { return __any(quadsum(ck4(hp[0]) + ck4(hp[1]) + ck4(hp[2]) + h1k - hc) != 0); };
    if (__builtin_expect(h1_bad(), 0)) {
      do { __builtin_amdgcn_s_sleep(2); h1_load(); ++tries; } while (h1_bad() && tries < SPIN_CAP);
    }
    STAMP(7);
    PAD(9);
    {
      const f32x4 z = {0, 0, 0, 0}, s0 = mfma(selB, hp[1], mfma(selA, hp[0], z)), s2 = mfma(selH, hp[2], z);
      *(LAS v2u*)(size_t)(rho) = v2u{pk(gelu(s0.x), gelu(s0.y)), pk(gelu(s0.z), gelu(s0.w))};
      PAD(10);
      if (wv >= 4) *(LAS u32*)(size_t)(rhoh) = pk(gelu(s2.x), gelu(s2.y));
    }
    STAMP(8);
    PAD(11);
    bar();
    STAMP(9);
    {
      f32x4 a0 = {0, 0, 0, 0}, a1 = {0, 0, 0, 0};
      v4u A[17];
      static_for<0, AW>([&](auto Q) { A[Q] = lds128(ar + ((Q + 1) >> 2) * 288 + ((Q + 1) & 3) * 64); });
      asm volatile("s_nop 1" : "+v"(a0), "+v"(a1));
      fence_sched();
      static_for<0, 15>([&](auto T) {
        mfma_ip(a0, W2[T], A[T]);
        mfma_ip(a1, W2[T], A[T + 2]);
        if constexpr (T + AW < 17) A[T + AW] = lds128(ar + ((T + AW + 1) >> 2) * 288 + ((T + AW + 1) & 3) * 64);
        if constexpr (T + NA < 15) rdw2(IC<T + NA>{});
        if constexpr (T == 8) PAD(12);
        fence_sched();
      });
      asm volatile("s_nop 7" ::: "memory");
      fence_sched();
      put(pack2(a0, a1), rws, p2v, p2c, key);
    }
    STAMP(10);
    PAD(13);
    if (!p3ok) { DUMP(); return; }
    bar_raw();
    v4u WF[8][2];
    u32 wr[4];
    wr[0] = opq(LB + L_RING + (4 * g + q4) * 512 + (((2 * wv) ^ ((4 * g + q4) & 7)) * 32) + p4 * 8);
    wr[1] = opq(wr[0] ^ 32); wr[2] = opq(wr[0] + 65536); wr[3] = opq(wr[1] + 65536);
    auto rdwf = [&](auto K) { static_for<0, 2>([&](auto T) { const u32 ad = wr[(K >> 2) * 2 + T] + (K & 3) * 16384; WF[K][T] = ldstr2(ad, ad + 8192); }); };
    static_assert(NA >= 8 && NA <= 12, "slots 0..7 of w_f land before PE");
    static_for<0, 4>(rdwf);
    PAD(14);
    const u32 h2off = opq(WS_P2 + (lane >> 3) * 262144 + (pi >> 4) * 16384 + (wv * 2 + ((lane >> 2) & 1)) * 1024 + (pi & 15) * 64 + p4 * 16), h2c = opq(ckoff(h2off));
    const u32 fw = opq(LB + L_F + (lane >> 5) * 576 + (wv * 32 + p4 * 8 + ((lane >> 2) & 1) * 4 + ((lane >> 3) & 3)) * 2);
    const u32 fr = opq(LB + L_F + (l15 >> 3) * 576 + g * 16), jw = opq(LB + L_J + lane * 16 + wv * 1024);
    nap<DLY2>();
    PAD(15);
    bar();
    STAMP(11);
    {
      v4u fp = ld16(rws, h2off);
      u32 fc = ldck(rws, h2c);
      mem_order();
      warm<2>(wv);
      static_for<4, 8>(rdwf);
      static_for<0, 2>([&](auto T) { ge[T] = lds128(LB + L_J + (geoff & 8191) + T * 512); gc[T] = *(const LAS u32*)(size_t)(LB + L_GC + ((ckoff(geoff) & 511) + T * 32)); });
    for (u32 spins = 0; __any(((groupsum(ck4(ge[0])) + key) ^ gc[0]) | ((groupsum(ck4(ge[1])) + key) ^ gc[1])) && spins < SPIN_CAP; ++spins) {
      __builtin_amdgcn_s_sleep(2);
      static_for<0, 2>([&](auto T) { ge[T] = ld16(rws, geoff + T * 512); gc[T] = ldck(rws, ckoff(geoff + T * 512)); });
      mem_order();
      tries += 65536;
    }
      PAD(16);
      if (__builtin_expect(__any(quadsum(ck4(fp)) + key != fc), 0)) {
        u32 spins = 0;
        do {
          __builtin_amdgcn_s_sleep(2);
          fp = ld16(rws, h2off);
          fc = ldck(rws, h2c);
          mem_order();
          tries += 256;
        } while (__any(quadsum(ck4(fp)) + key != fc) && ++spins < SPIN_CAP);
      }
      STAMP(12);
      PAD(17);
      const f32x4 s = mfma(selH, fp, f32x4{0, 0, 0, 0});
      const float snd = (lane & 8) ? s.x : s.y, kp = (lane & 8) ? s.y : s.x;
      const float tot = kp + asf(__builtin_amdgcn_update_dpp(0, (int)asu(snd), 0x128, 0xf, 0xf, false));
      *(LAS u16*)(size_t)(fw) = (u16)pk(gelu(tot), 0.f);
    }
    PAD(18);
    STAMP(14);
    bar();
    STAMP(6);
    {
      f32x4 d0 = __builtin_bit_cast(f32x4, ge[0]), d1 = __builtin_bit_cast(f32x4, ge[1]);
      v4u b[8];
      static_for<0, 8>([&](auto K) { b[K] = lds128(fr + K * 64); });
      fence_sched();
      PAD(19);
      static_for<0, 8>([&](auto K) { d0 = mfma(WF[K][0], b[K], d0); });
      fence_sched();
      float t0[4];
      static_for<0, 8>([&](auto K) {
        d1 = mfma(WF[K][1], b[K], d1);
        if constexpr (K >= 3 && K < 7) t0[K - 3] = tanh_(d0[K - 3]);
        fence_sched();
      });
      PAD(20);
      *(LAS v4u*)(size_t)(jw) = v4u{pk(t0[0], t0[1]), pk(t0[2], t0[3]), pk(tanh_(d1.x), tanh_(d1.y)), pk(tanh_(d1.z), tanh_(d1.w))};
    }
    STAMP(13);
    bar();
  }
  DUMP();
}

extern "C" void kernel_launch(void* const* d_in, const int* in_sizes, int n_in, void* d_out, int out_size, void* d_ws, size_t ws_size,
                              hipStream_t stream) {
  Args a{(const u16*)d_in[0], (const u16*)d_in[1], (const u16*)d_in[2], (const u16*)d_in[3], (const u16*)d_in[4],
         (const u16*)d_in[5], (const u16*)d_in[6], (u16*)d_out, (unsigned char*)d_ws};
  rnnt_fused<<<256, 768, 0, stream>>>(a);
}
```

```cpp
#define BPRIO 0
#define EDLY 10
#include <hip/hip_runtime.h>
#include <hip/hip_bf16.h>
#include <type_traits>

typedef unsigned u32;
typedef unsigned short u16;
typedef unsigned long long u64;
typedef u32 v4u __attribute__((ext_vector_type(4)));
typedef u32 v2u __attribute__((ext_vector_type(2)));
typedef u64 v8q __attribute__((ext_vector_type(8)));
typedef float f32x4 __attribute__((ext_vector_type(4)));
typedef float f32x2 __attribute__((ext_vector_type(2)));
typedef short s16x4 __attribute__((ext_vector_type(4)));
typedef __bf16 bf16x8 __attribute__((ext_vector_type(8)));
typedef __bf16 bf16x2 __attribute__((ext_vector_type(2)));
typedef __amdgpu_buffer_rsrc_t rsrc_t;
#define GAS __attribute__((address_space(1)))
#define LAS __attribute__((address_space(3)))
#define DI __device__ __forceinline__
template <int N> using IC = std::integral_constant<int, N>;

constexpr u32 WS_EPOCH = 0;
constexpr u32 WS_FLAGS = 4096;
constexpr u32 WS_GE = 65536;
constexpr u32 WS_P1 = 1u << 20;
constexpr u32 WS_P2 = 3u << 20;
constexpr u32 WS_PL = 5u << 20;
constexpr u32 WS_BYTES = 6u << 20;
constexpr u32 OOB = 0x80000000u;
constexpr u32 SPIN_CAP = 100000;
constexpr u32 LSPIN_CAP = 4000000;
constexpr u32 L_RING = 0;
constexpr u32 L_KEY = 131072;
constexpr u32 L_JUNK = L_KEY + 64;
constexpr u32 L_F = L_JUNK + 256;
constexpr u32 L_X = L_F + 1152;
constexpr u32 L_GC = L_X;
constexpr u32 L_O = L_KEY + 8192;
constexpr u32 L_J = L_KEY + 24576;
constexpr u32 LDS_BYTES = L_J + 8192;
static_assert(L_X + 5760 <= L_O && L_X + 19008 <= L_J && LDS_BYTES <= 163840, "lds map");

#define LOOK 3
#define NA 8
#define OA 2
#define OB 2
#define O0 4
#define NW 3
#define SJ2 0
#define TG 1
#define EXL 0
#define GRP 0
#define BM 3
#define ST 0
#define TDLY 6
#define DLY1 6
#define DLY2 6
#define GDLY 4
#define SPRIO 3
#define AW 5
#define XAUX ""

template <int I, int N, class F> DI void static_for(F&& f) {
  if constexpr (I < N) { f(IC<I>{}); static_for<I + 1, N>(f); }
}
template <int N> DI void wait_vm() { asm volatile("s_waitcnt vmcnt(%0)" ::"i"(N) : "memory"); }
DI void bar() { asm volatile("s_waitcnt lgkmcnt(0)\n\ts_barrier" ::: "memory"); }
DI void bar_raw() { asm volatile("s_barrier" ::: "memory"); }
DI void fence_sched() { __builtin_amdgcn_sched_barrier(0); }
DI void mem_order() { asm volatile("" ::: "memory"); }
DI u32 opq(u32 x) { asm volatile("" : "+v"(x)); return x; }
template <int N> DI void nap() { if constexpr (N > 0) __builtin_amdgcn_s_sleep(N); }
DI void dma16(u32 lds, u32 voff, rsrc_t rs, u32 soff) {
  asm volatile("s_mov_b32 m0, %0\n\ts_nop 0\n\tbuffer_load_dwordx4 %1, %2, %3 offen lds" ::"s"(lds), "v"(voff), "s"(rs), "s"(soff) : "memory");
}
DI void dma16x(u32 lds, u32 voff, rsrc_t rs, u32 soff) {
  asm volatile("s_mov_b32 m0, %0\n\ts_nop 0\n\tbuffer_load_dwordx4 %1, %2, %3 offen" XAUX " lds" ::"s"(lds), "v"(voff), "s"(rs), "s"(soff) : "memory");
}
DI void dma4(u32 lds, u32 voff, rsrc_t rs) {
  asm volatile("s_mov_b32 m0, %0\n\ts_nop 0\n\tbuffer_load_dword %1, %2, 0 offen lds" ::"s"(lds), "v"(voff), "s"(rs) : "memory");
}
DI rsrc_t mk(const void* p, u32 bytes) { return __builtin_amdgcn_make_buffer_rsrc((void*)p, 0, (int)bytes, 0x00020000); }
DI v4u lds128(u32 a) { return *(const LAS v4u*)(size_t)a; }
DI v2u ldstr(u32 a) { return __builtin_bit_cast(v2u, __builtin_amdgcn_ds_read_tr16_b64_v4i16((LAS s16x4*)(size_t)a)); }
DI v4u ldstr2(u32 a0, u32 a1) { v2u x = ldstr(a0), y = ldstr(a1); return v4u{x.x, x.y, y.x, y.y}; }
DI f32x4 mfma(v4u a, v4u b, f32x4 c) {
  return __builtin_amdgcn_mfma_f32_16x16x32_bf16(__builtin_bit_cast(bf16x8, a), __builtin_bit_cast(bf16x8, b), c, 0, 0, 0);
}
DI void mfma_ip(f32x4& c, v4u a, v4u b) { asm volatile("v_mfma_f32_16x16x32_bf16 %0, %1, %2, %0" : "+v"(c) : "v"(a), "v"(b)); }
DI u32 pk(float a, float b) { f32x2 f = {a, b}; return __builtin_bit_cast(u32, __builtin_convertvector(f, bf16x2)); }
DI float asf(u32 x) { return __builtin_bit_cast(float, x); }
DI u32 asu(float x) { return __builtin_bit_cast(u32, x); }
DI float lo16(u32 x) { return asf(x << 16); }
DI float hi16(u32 x) { return asf(x & 0xFFFF0000u); }
DI float gelu(float x) {
  const float c0 = -2.0f * 0.7978845608028654f * 1.4426950408889634f, c1 = c0 * 0.044715f;
  float e = __builtin_amdgcn_exp2f(x * __builtin_fmaf(x * x, c1, c0));
  return x * __builtin_amdgcn_rcpf(1.0f + e);
}
DI float tanh_(float x) {
  float e = __builtin_amdgcn_exp2f(x * 2.8853900817779268f);
  return __builtin_fmaf(-2.0f, __builtin_amdgcn_rcpf(1.0f + e), 1.0f);
}
DI v4u pack2(f32x4 a, f32x4 b) { return v4u{pk(a.x, a.y), pk(a.z, a.w), pk(b.x, b.y), pk(b.z, b.w)}; }

DI void st_sc1(v4u v, rsrc_t rs, u32 off) { __builtin_amdgcn_raw_buffer_store_b128(v, rs, off, 0, 16); }
template <int AUX = 16> DI v4u ld16(rsrc_t rs, u32 off) { return __builtin_amdgcn_raw_buffer_load_b128(rs, off, 0, AUX); }
template <int AUX = 16> DI u32 ldck(rsrc_t rs, u32 off) { return __builtin_amdgcn_raw_buffer_load_b32(rs, off, WS_PL, AUX); }
DI u32 ckoff(u32 off) { return (off >> 4) & ~3u; }
DI u32 ck4(v4u d) { return (d.x + d.y + d.z) + d.w; }
DI u32 groupsum(u32 c) {
  const auto r = __builtin_amdgcn_permlane32_swap(c, c, false, false);
  c = r[0] + r[1];
  const auto q = __builtin_amdgcn_permlane16_swap(c, c, false, false);
  return q[0] + q[1];
}
DI u32 quadsum(u32 c) {
  c += __builtin_amdgcn_update_dpp(0, (int)c, 0xB1, 0xf, 0xf, false);
  c += __builtin_amdgcn_update_dpp(0, (int)c, 0x4E, 0xf, 0xf, false);
  return c;
}
DI u32 putck(u32 off) { return (off & 48) ? OOB : off >> 4; }
DI void put(v4u d, rsrc_t rs, u32 off, u32 coff, u32 key) {
  st_sc1(d, rs, off);
  fence_sched();
  __builtin_amdgcn_raw_buffer_store_b32(groupsum(ck4(d)) + key, rs, coff, WS_PL, 16);
}
DI v4u sel(u32 e) {
  const u32 one = (e & 1) ? 0x3F800000u : 0x3F80u, d = e >> 1;
  return v4u{d == 0 ? one : 0, d == 1 ? one : 0, d == 2 ? one : 0, d == 3 ? one : 0};
}
DI v4u opq4(v4u x) { asm volatile("" : "+v"(x)); return x; }

DI void mark(u32 a, u32 v) { asm volatile("s_mov_b64 exec, 1\n\tds_write_b32 %0, %1\n\ts_mov_b64 exec, -1" ::"v"(a), "v"(v) : "memory"); }
template <u32 NEED> DI void marks_wait(u32 va) {
  u32 cnt, hv;
  asm volatile("s_mov_b32 %1, %4\n"
               "Lr%=:\n\tds_read_b32 %0, %2\n\ts_waitcnt lgkmcnt(0)\n\tv_cmp_gt_u32 vcc, %3, %0\n\ts_cbranch_vccz Ld%=\n\t"
               "s_sub_u32 %1, %1, 1\n\ts_cbranch_scc0 Lr%=\n"
               "Ld%=:"
               : "=&v"(hv), "=&s"(cnt) : "v"(va), "i"(NEED), "i"(LSPIN_CAP) : "memory", "scc", "vcc");
}
template <int N, u32 STEP, u32 BARS> DI void rounds(u32& m, u32& so, int& pv, u32 va, u32 vb, rsrc_t rs) {
  u32 cnt;
  asm volatile("s_mov_b32 %3, %7\n"
               "L%=:\n\ts_mov_b32 m0, %0\n\ts_add_u32 %0, %0, 0x1000\n\tbuffer_load_dwordx4 %4, %6, %1 offen lds\n\t"
               "s_mov_b32 m0, %0\n\ts_add_u32 %0, %0, 0x1000\n\tbuffer_load_dwordx4 %5, %6, %1 offen lds\n\t"
               "s_add_u32 %1, %1, %10\n\t.if %9\n\ts_add_i32 %2, %2, 1\n\ts_bitcmp0_b32 %9, %2\n\t.endif\n\ts_waitcnt vmcnt(%8)\n\t"
               ".if %9\n\ts_cbranch_scc1 Lq%=\n\ts_barrier\n"
               "Lq%=:\n\t.endif\n\ts_sub_u32 %3, %3, 1\n\ts_cbranch_scc0 L%="
               : "+s"(m), "+s"(so), "+s"(pv), "=&s"(cnt) : "v"(va), "v"(vb), "s"(rs), "i"(N - 1), "i"(2 * LOOK), "i"(BARS), "i"(STEP) : "memory", "scc");
}
#define PAD(k) asm volatile("PAD%c0:\n\ts_cbranch_execz RET%c1" ::"i"(k), "i"((k) / 8))
#define HOP "s_mov_b64 exec, 0\n\ts_getpc_b64 vcc\n\ts_add_u32 vcc_lo, vcc_lo, %0\n\ts_addc_u32 vcc_hi, vcc_hi, 0\n\ts_setpc_b64 vcc\n\t"
DI u32 warm0(u32 wv, const void* p) {
  u32 e, o = wv * 4 + 12;
  asm volatile("s_load_dword %1, %2, 0x0\n\t" HOP
               "s_branch PAD0\n\ts_branch PAD1\n\ts_branch PAD2\n\ts_branch PAD3\n\ts_branch PAD4\n\ts_branch PAD5\n\ts_branch PAD6\n\ts_branch PAD7\n"
               "RET0:\n\ts_mov_b64 exec, -1\n\ts_waitcnt lgkmcnt(0)"
               : "+s"(o), "=&s"(e) : "s"(p) : "memory", "scc", "vcc");
  return e;
}
template <int S> DI void warm(u32 wv) {
  asm volatile(HOP
               "s_branch PAD%c2\n\ts_branch PAD%c3\n\ts_branch PAD%c4\n\ts_branch PAD%c5\n\ts_branch PAD%c6\n\ts_branch PAD%c7\n\ts_branch PAD%c8\n\ts_branch PAD%c9\n"
               "RET%c1:\n\ts_mov_b64 exec, -1"
               :: "s"(wv * 4 + 12), "i"(S), "i"(8 * S), "i"(8 * S + 1), "i"(8 * S + 2), "i"(8 * S + 3), "i"(8 * S + 4), "i"(8 * S + 5), "i"(8 * S + 6), "i"(8 * S + 7) : "memory", "scc", "vcc");
}

#define STAMP(i)

struct Args { const u16 *x, *w1, *w2, *gp, *wf, *wg, *wo; u16* out; unsigned char* ws; };

__global__ void __launch_bounds__(768, 3) rnnt_fused(Args unused) {
  __shared__ __attribute__((aligned(16))) unsigned char smem[LDS_BYTES];
  const u32 LB = (u32)(size_t)(LAS unsigned char*)smem;
  const u32 tid = threadIdx.x;
  v8q ka; u64 kws, pc; u32 clen;
  asm volatile("s_load_dwordx16 %0, %4, 0x0\n\ts_load_dwordx2 %1, %4, 0x40\n\ts_getpc_b64 %2\nLpc:\n\ts_mov_b32 %3, Lend-Lpc-4\n\t.subsection 1\nLend:\n\t.subsection 0" : "=&s"(ka), "=&s"(kws), "=&s"(pc), "=&s"(clen) : "s"(__builtin_amdgcn_kernarg_segment_ptr()) : "memory");
  asm volatile("s_mov_b32 m0, %1\n\ts_nop 0\n\tglobal_load_lds_dword %0, %2" :: "v"(min(((blockIdx.x >> 3) * 1536 + tid * 2) & 0x3f80, clen)), "s"(LB + L_JUNK), "s"(pc) : "memory");
  const u32 wq = __builtin_amdgcn_readfirstlane(tid >> 6), w4 = wq & 3, wv = wq - 4;
  const u32 bid = blockIdx.x, x8 = bid & 7, j = bid >> 3, mt = j >> 1, mloc = mt & 7, bat = mt >> 3;
  const u32 k1 = x8 >> 1, n1 = 2 * (x8 & 1) + (j & 1);
  u32 lane = tid & 63;
  const u32 wbase = k1 * 32768 + n1 * 128;
  u32 w1v = ((tid >> 3) & 31) * 512 + wbase + (((tid & 7) ^ (((tid >> 3) & 2) | ((tid >> 4) & 4))) * 16);
  u32 xv = ((tid >> 3) & 31) * 512 + (mloc * 65536 - 3584 + k1 * 128) + (tid & 7) * 16;
  u32 m = LB + L_RING + w4 * 1024, mx = LB + L_X + w4 * 1056, nx = min(mloc * 128 + 137, 1000u) * 512, xo = bat * 512000;
  u32 tv = lane < 14 ? (1 + lane) * 131072 + wbase + (mt * 4 + w4) * 512 : OOB;
  asm volatile("" : "+v"(w1v), "+v"(xv), "+v"(tv));
  asm volatile("s_waitcnt lgkmcnt(0)" : "+s"(ka), "+s"(kws), "+s"(m), "+s"(mx), "+s"(nx), "+s"(xo)::"memory");
  const Args a{(const u16*)ka[0], (const u16*)ka[1], (const u16*)ka[2], (const u16*)ka[3], (const u16*)ka[4], (const u16*)ka[5], (const u16*)ka[6], (u16*)ka[7], (unsigned char*)kws};
  const rsrc_t rw1 = mk(a.w1, 15 * 131072), rws = mk(a.ws, WS_BYTES);
  const u32 c2 = x8, n2 = j & 1;
  const u32 pi = x8 * 32 + j, ploc = pi & 127, bat3 = pi >> 7;
  const bool p3ok = ploc < 125;
#define DUMP()

  if (__builtin_expect(wq < 4, 1)) {
    {
      const rsrc_t rx = mk((const void*)(ka[0] + xo), nx);
      static_for<0, 4>([&](auto I) { dma16x(mx + I * 4224, xv + I * 16384, rx, 0); });
      if (w4 < 2) dma16x(mx + 4 * 4224, xv + 65536, rx, 0);
      dma16x(m, w1v, rw1, 0);
      dma16x(m + 4096, w1v, rw1, 16384);
    }
    dma4(LB + L_JUNK, tv, rw1);
    __builtin_amdgcn_s_setprio(SPRIO);
    STAMP(1);
    const u32 m0 = m;
    m += 8192;
    u32 so = 131072;
    int pv = 1 - LOOK;
    constexpr u32 BARS = GRP ? 0x1114 : 0x1554;
    rounds<LOOK - 1, 131072, BARS>(m, so, pv, w1v, w1v + 16384, rw1);
    lane = opq(lane);
    const u32 g = lane >> 4, l15 = lane & 15, q4 = l15 >> 2, rr8 = lane >> 3, r2 = 2 * w4 + (lane >> 5);
    const u32 w2v = opq((c2 * 32 + w4 * 4 + g) * 512 + n2 * 256 + ((((lane >> 1) & 7) ^ (g | (((w4 >> 1) & 1) << 2))) * 32) + (lane & 1) * 16), w2w = opq(w2v + 8192);
    const u32 wfv = opq(r2 * 512 + ((((lane >> 1) & 15) ^ (r2 & 7)) * 32) + (lane & 1) * 16), wfw = opq(wfv + 4096);
    const u32 wov = opq(rr8 * 512 + w4 * 128 + ((((lane >> 1) & 3) ^ (rr8 >> 1)) * 32) + (lane & 1) * 16), wow = opq(wov + 4096);
    u32 wor[4];
    static_for<0, 4>([&](auto T) { wor[T] = opq(m0 + (g >> 1) * 4096 + (4 * (g & 1) + q4) * 128 + ((T ^ (2 * (g & 1) + (q4 >> 1))) * 32) + (lane & 3) * 8); });
    u32 tq;
    {
      const u32 ln = mt * 60 + w4 * 30 + lane;
      const u32 t2 = lane < 30 ? (ln >> 6) * 131072 + (c2 * 32 + ((ln >> 1) & 31)) * 512 + n2 * 256 + (ln & 1) * 128 : OOB;
      tq = opq(w4 < 2 ? t2 : (lane < 32 ? (j * 32 + lane) * 128 : OOB));
    }
    const rsrc_t rt = mk(w4 < 2 ? a.w2 : (w4 == 2 ? a.wo : a.wf), w4 < 2 ? 15 * 131072 : 131072);
    const u32 jr = opq(LB + L_J + lane * 16);
    const u32 ov = opq((((bat3 * 250 + 2 * ploc) * 8 + l15) * 256 + w4 * 64 + (g & 1) * 16 + (g >> 1) * 8) * 2);
    if (ploc >= 126) mark(LB + L_KEY + 16 + w4 * 4, 0);
    static_assert(LOOK == 3, "waits below");
    wait_vm<5>();
    bar_raw();
    rounds<12, 131072, BARS>(m, so, pv, w1v, w1v + 16384, rw1);
    const rsrc_t rw2 = mk(a.w2, 15 * 131072), rwf = mk(a.wf, 131072), rwo = mk(a.wo, 131072);
    m = m0; so = 0;
    dma4(LB + L_JUNK, tq, rt);
    wait_vm<3>();
    bar_raw();
    wait_vm<1>();
    bar_raw();
    wait_vm<0>();
    nap<EDLY>();
    if (ploc >= 126) {
      PAD(6);
      const u32 wgv = w1v - wbase + (bat3 * 2 + (pi & 1)) * 128;
      static_for<0, 2>([&](auto R) { dma16(m0 + 13 * 8192 + R * 4096, (r2 + 8 * R) * 512 + (((lane & 31) ^ (r2 + 8 * R)) * 16), mk(a.gp, 8192), 0); });
      static_for<0, 8>([&](auto R) { dma16(m0 + 9 * 8192 + R * 4096, wgv, mk(a.wg, 131072), R * 16384); });
    }
    rounds<NA, 131072, 0>(m, so, pv, w2v, w2w, rw2);
    wait_vm<0>();
    STAMP(4);
    bar_raw();
    if (ploc >= 126) {
      PAD(7);
      const u32 gr = LB + L_RING + 9 * 8192 + g * 1024 + q4 * 128 + ((w4 ^ ((q4 >> 1) + 2 * (g & 1))) * 32) + (lane & 3) * 8;
      v4u wa[8], gb[8];
      static_for<0, 8>([&](auto K) { gb[K] = lds128(LB + L_RING + 13 * 8192 + l15 * 512 + (((K * 4 + g) ^ l15) * 16)); });
      static_for<0, 8>([&](auto K) { wa[K] = ldstr2(gr + K * 4096, gr + K * 4096 + 512); });
      mark(LB + L_KEY + 16 + w4 * 4, 1);
      fence_sched();
      f32x4 d = {0, 0, 0, 0};
      static_for<0, 8>([&](auto K) { d = mfma(wa[K], gb[K], d); });
      const u32 go = WS_GE + (l15 >> 3) * 8192 + ((bat3 * 2 + (pi & 1)) * 4 + w4) * 512 + (l15 & 7) * 64 + g * 16;
      put(__builtin_bit_cast(v4u, d), rws, go, putck(go), *(const volatile LAS u32*)(size_t)(LB + L_KEY));
      marks_wait<1>(LB + L_KEY + 16 + (lane & 3) * 4);
    } else nap<GDLY>();
    rounds<15 - NA, 131072, 0>(m, so, pv, w2v, w2w, rw2);
    if (!p3ok) {
      wait_vm<0>();
      bar_raw();
      if (pi == 125 && w4 == 0) {
        u32 epoch;
        asm volatile("s_load_dword %0, %1, 0x0\n\ts_waitcnt lgkmcnt(0)" : "=s"(epoch) : "s"(a.ws) : "memory");
        epoch += 1;
        for (u32 spins = 0; spins < SPIN_CAP; ++spins) {
          v4u f4;
          asm volatile("buffer_load_dwordx4 %0, %1, %2, 0 offen sc1\n\ts_waitcnt vmcnt(0)" : "=v"(f4) : "v"(WS_FLAGS + (lane >> 2) * 128 + (lane & 3) * 16), "s"(rws) : "memory");
          if (__all(f4.x == epoch && f4.y == epoch && f4.z == epoch && f4.w == epoch)) break;
          __builtin_amdgcn_s_sleep(8);
        }
        if (lane == 0) __hip_atomic_store((GAS u32*)(a.ws + WS_EPOCH), epoch, __ATOMIC_RELAXED, __HIP_MEMORY_SCOPE_AGENT);
      }
      DUMP();
      return;
    }
    v2u WO[16][4];
    auto o_iss = [&](auto R) { dma16(m0 + L_O + (R & 1) * 8192, wov, rwo, R * 8192); dma16(m0 + L_O + (R & 1) * 8192 + 4096, wow, rwo, R * 8192); };
    auto o_rd = [&](auto R) { static_for<0, 4>([&](auto T) { WO[R][T] = ldstr(wor[T] + L_O + (R & 1) * 8192); }); };
    auto lds_done = [&]() { asm volatile("s_waitcnt lgkmcnt(0)" ::: "memory"); };
    static_for<0, 2>(o_iss);
    dma16(m0 + 15 * 8192, wfv, rwf, 15 * 8192); dma16(m0 + 15 * 8192 + 4096, wfw, rwf, 15 * 8192);
    if constexpr (OA == 4) { wait_vm<2>(); static_for<0, 2>(o_rd); lds_done(); static_for<2, 4>(o_iss); }
    else wait_vm<6>();
    STAMP(5);
    bar_raw();
    m = m0; so = 0;
    if constexpr (OB > OA) {
      rounds<NA - 2, 8192, 0>(m, so, pv, wfv, wfw, rwf);
      static_for<OA - 2, OA>(o_rd);
      rounds<2, 8192, 0>(m, so, pv, wfv, wfw, rwf);
      lds_done(); static_for<OA, OB>(o_iss);
      wait_vm<2 * (OB - OA)>();
    } else {
      rounds<NA, 8192, 0>(m, so, pv, wfv, wfw, rwf);
      static_for<OA - 2, OA>(o_rd);
      wait_vm<0>();
    }
    STAMP(6);
    bar();
    dma16(m0 + L_J, lane * 16, rws, WS_GE + bat3 * 8192 + w4 * 1024); dma16(m0 + L_J + 4096, lane * 16, rws, WS_GE + bat3 * 8192 + w4 * 1024 + 4096);
    if (w4 == 0) dma16(LB + L_GC, lane < 32 ? lane * 16 : OOB, rws, WS_PL + ((WS_GE + bat3 * 8192) >> 4));
    rounds<15 - NA, 8192, 0>(m, so, pv, wfv, wfw, rwf);
    if constexpr (O0 > OB) { if constexpr (OB > OA) { static_for<OA, OB>(o_rd); lds_done(); } static_for<OB, O0>(o_iss); }
    wait_vm<0>();
    STAMP(7);
    bar_raw();
    if constexpr (O0 > OB) static_for<OB, O0>(o_rd); else static_for<OA, OB>(o_rd);
    nap<GDLY>();
    {
      auto iss = [&](auto R) {
        if constexpr (R >= O0 + 8) asm volatile("s_waitcnt lgkmcnt(8)" ::: "memory");
        dma16(m0 + ((R - O0) & 7) * 8192, wov, rwo, R * 8192); dma16(m0 + ((R - O0) & 7) * 8192 + 4096, wow, rwo, R * 8192);
      };
      PAD(21);
      static_for<O0, O0 + 3>(iss);
      static_for<O0, 16>([&](auto R) {
        if constexpr (R == O0 + NW) { STAMP(10); bar_raw(); }
        wait_vm<(R + 2 < 16 ? 4 : 2 * (15 - R))>();
        if constexpr (R + 3 < 16) iss(IC<R + 3>{});
        static_for<0, 4>([&](auto T) { WO[R][T] = ldstr(wor[T] + ((R - O0) & 7) * 8192); });
        if constexpr (R == 10) PAD(22);
        fence_sched();
      });
    }
    STAMP(8);
    bar_raw();
    STAMP(9);
    {
      PAD(23);
      f32x4 d[4] = {{0, 0, 0, 0}, {0, 0, 0, 0}, {0, 0, 0, 0}, {0, 0, 0, 0}};
      v4u b[8];
      static_for<4, 8>([&](auto K) { b[K] = lds128(jr + K * 1024); });
      fence_sched();
      static_for<0, 8>([&](auto I) {
        constexpr int K = (I + 4) & 7;
        static_for<0, 4>([&](auto T) { d[T] = mfma(v4u{WO[2 * K][T].x, WO[2 * K][T].y, WO[2 * K + 1][T].x, WO[2 * K + 1][T].y}, b[K], d[T]); });
        if constexpr (I < 4) b[I] = lds128(jr + I * 1024);
        fence_sched();
      });
      const rsrc_t ro = mk(a.out, 2048000);
      static_for<0, 2>([&](auto H) {
        const f32x4 d0 = d[2 * H], d1 = d[2 * H + 1];
        const auto r0 = __builtin_amdgcn_permlane16_swap(pk(d0.x, d0.y), pk(d1.x, d1.y), false, false);
        const auto r1 = __builtin_amdgcn_permlane16_swap(pk(d0.z, d0.w), pk(d1.z, d1.w), false, false);
        __builtin_amdgcn_raw_buffer_store_b128(v4u{r0[0], r1[0], r0[1], r1[1]}, ro, ov + H * 64, 0, 16);
      });
    }
    STAMP(14);
  } else {
    const u32 p1off = WS_P1 + k1 * 524288 + (2 * n1 + (w4 >> 1)) * 65536 + mt * 4096 + (w4 & 1) * 2048;
    if ((x8 & 3) == 3 && lane < 3) {
      const u32 q = j * 8 + wv;
      const unsigned char* p = lane < 2 ? (const unsigned char*)a.wg + q * 512 + bat3 * 256 + lane * 128 : (const unsigned char*)a.gp + (q & 63) * 128;
      asm volatile("s_mov_b32 m0, %1\n\ts_nop 0\n\tglobal_load_lds_dword %0, off" ::"v"(p), "s"(LB + L_JUNK) : "memory");
    }
    const u32 epoch = warm0(wv, a.ws) + 1, key = epoch * 0x9E3779B1u | 1;
    STAMP(1);
    const u32 l15 = lane & 15, g = lane >> 4, q4 = l15 >> 2, p4 = lane & 3;
    {
      PAD(0);
      const u32 pw = wv >> 2;
      if (pw) __builtin_amdgcn_s_setprio(BPRIO);
      f32x4 acc[4] = {{0, 0, 0, 0}, {0, 0, 0, 0}, {0, 0, 0, 0}, {0, 0, 0, 0}};
      v4u G[2][11];
      v2u Wf[2][4];
      const u32 xr = opq(LB + L_X + l15 * 1056 + g * 16 + pw * 128);
      const u32 w1r = opq(LB + L_RING + pw * 8192 + g * 1024 + q4 * 128 + ((w4 ^ ((q4 >> 1) + 2 * (g & 1))) * 32) + p4 * 8), w1r2 = opq(w1r + 65536);
      const u32 p1v = opq(p1off + l15 * 64 + g * 16), p1c = opq(lane < 32 ? (p1off >> 4) + lane * 4 : OOB);
      const u32 ex = opq(LB + (w4 < 2 ? L_RING + 15 * 8192 : L_J - 8192) + w4 * 4096 + lane * 16);
      auto rdw1 = [&](auto S, auto M) { Wf[S & 1][M] = ldstr((S < 4 ? w1r : w1r2) + (S & 3) * 16384 + (M >> 1) * 4096 + (M & 1) * 512); };
      auto rdx = [&](auto C, auto U) { G[C][U] = lds128(xr + (U >> 2) * 1056 + (U & 3) * 256 + C * 64); };
      bar_raw();
      STAMP(2);
      static_for<0, 2>([&](auto C) { static_for<0, 4>([&](auto U) { rdx(C, U); }); });
      bar_raw();
      static_for<0, 4>([&](auto M) { rdw1(IC<0>{}, M); });
      asm volatile("s_nop 1" : "+v"(acc[0]), "+v"(acc[1]), "+v"(acc[2]), "+v"(acc[3]));
      fence_sched();
      f32x4 oth[4];
      auto step = [&](auto S) {
        if constexpr (S % 3 == 0) PAD(1 + S / 3);
        if constexpr (GRP && (S == 1 || S == 3 || (GRP == 1 && S == 5))) bar_raw();
        static_for<0, 8>([&](auto M) {
          constexpr bool late = !GRP || S == 0 || S == 6 || (GRP == 2 && S == 5);
          constexpr int C = M >> 2, J = M & 3, W0 = late ? BM + 1 : 0, X0 = late ? (BM < 2 ? 6 : 0) : 4;
          const v2u wa = Wf[S & 1][2 * C], wb = Wf[S & 1][2 * C + 1];
          mfma_ip(acc[J], v4u{wa.x, wa.y, wb.x, wb.y}, G[C][J + S]);
          fence_sched();
          if constexpr (S < 6 && late && M == BM) bar_raw();
          if constexpr (S < 6 && M >= W0 && M < W0 + 4) rdw1(IC<S + 1>{}, IC<M - W0>{});
          if constexpr (S < 7 && M >= X0 && M < X0 + 2) rdx(IC<M - X0>{}, IC<S + 4>{});
          if constexpr (S == 7 && M < 4) oth[M] = *(const LAS f32x4*)(size_t)(ex + M * 1024);
          fence_sched();
        });
      };
      static_for<0, 7>(step);
      asm volatile("s_nop 7" ::: "memory");
      fence_sched();
      if (pw) {
        static_for<0, 4>([&](auto J) { *(LAS f32x4*)(size_t)(ex + J * 1024) = acc[J]; });
        if (w4 == 0) mark(LB + L_KEY, key);
        bar();
        STAMP(3);
        __builtin_amdgcn_raw_buffer_store_b32(epoch, rws, tid == 512 ? WS_FLAGS + (bid >> 4) * 128 + (bid & 15) * 4 : OOB, 0, 16);
      } else {
        PAD(4);
        bar_raw();
        static_for<0, 4>([&](auto M) { rdw1(IC<7>{}, M); });
        fence_sched();
        step(IC<7>{});
        asm volatile("s_nop 7" ::: "memory");
        fence_sched();
        STAMP(3);
        const v4u d0 = pack2(acc[0] + oth[0], acc[1] + oth[1]), d1 = pack2(acc[2] + oth[2], acc[3] + oth[3]);
        st_sc1(d0, rws, p1v);
        st_sc1(d1, rws, p1v + 1024);
        const auto r = __builtin_amdgcn_permlane16_swap(ck4(d0), ck4(d1), false, false);
        const u32 c = r[0] + r[1];
        const auto q = __builtin_amdgcn_permlane32_swap(c, c, false, false);
        __builtin_amdgcn_raw_buffer_store_b32(q[0] + q[1] + key, rws, p1c, WS_PL, 16);
      }
    }
    STAMP(4);
    PAD(5);
    v4u W2[15];
    const u32 w2r = opq(LB + L_RING + (8 * g + q4) * 256 + ((wv ^ (q4 | ((g & 1) << 2))) * 32) + p4 * 8), w2r2 = opq(w2r + 65536);
    auto rdw2 = [&](auto T) { const u32 ad = (T < 8 ? w2r : w2r2) + (T & 7) * 8192; W2[T] = ldstr2(ad, ad + 1024); };
    v4u hp[3];
    u32 rho, rhoh, h1off[3], h1c, h1k, hc;
    {
      const u32 blk = wv >> 1, mm = (wv & 1) * 8 + q4;
      const u32 hb = WS_P1 + g * 524288 + c2 * 65536;
      rho = 8 + 4 * mm + 2 * (blk & 1);
      h1off[0] = opq(mloc * 64 + rho - 8 < 500 ? hb + mt * 4096 + wv * 512 + l15 * 16 : OOB);
      h1off[1] = opq(mloc * 64 + rho + 8 < 500 ? hb + mt * 4096 + wv * 512 + 256 + l15 * 16 : OOB);
      rho = opq(LB + L_X + (rho + (g & 1)) * 64 + (rho >> 2) * 32 + (lane >> 5) * 4 * 288 + (blk >> 1) * 32 + p4 * 8);
      const u32 nxt = l15 >> 3;
      rhoh = 72 * nxt + 4 * (q4 & 1) + 2 * (w4 & 1);
      const int hrow = (int)(mloc * 64 + rhoh) - 8;
      h1off[2] = opq((wv >= 4 && hrow >= 0 && hrow < 500) ? hb + (mt + 2 * nxt - 1) * 4096 + w4 * 1024 + (nxt ? 0 : 896) + (lane & 7) * 16 : OOB);
      rhoh = opq(LB + L_X + (rhoh + (lane >> 5)) * 64 + (rhoh >> 2) * 32 + (w4 >> 1) * 32 + p4 * 8 + (g & 1) * 4);
      const u32 sel = p4 == 0 ? h1off[0] : (p4 == 1 ? h1off[1] : (p4 == 2 ? h1off[2] : OOB));
      h1c = opq(ckoff(sel)); h1k = opq(sel == OOB ? 0 : key);
    }
    const u32 ar = opq(LB + L_X + l15 * 288 + g * 16), p2v = opq(WS_P2 + c2 * 262144 + mt * 16384 + n2 * 8192 + wv * 1024 + l15 * 64 + g * 16), p2c = opq(putck(p2v));
    const v4u selA = opq4(sel(l15)), selB = opq4(sel(l15 - 8)), selH = opq4(sel((l15 & 2) ? 8 : 2 * q4 + (l15 & 1)));
    auto h1_load = [&]() {
      static_for<0, 2>([&](auto K) { hp[K] = ld16(rws, h1off[K]); });
      if (wv >= 4) hp[2] = ld16(rws, h1off[2]);
      hc = ldck(rws, h1c);
      mem_order();
    };
    v4u ge[2]; u32 gc[2];
    const u32 geoff = opq(WS_GE + bat3 * 8192 + wv * 1024 + (lane & 7) * 64 + g * 16);
    hp[2] = v4u{0, 0, 0, 0};
    nap<DLY1>();
    bar_raw();
    STAMP(5);
    h1_load();
    warm<1>(wv);
    static_for<0, NA>(rdw2);
    PAD(8);
    u32 tries = 0;
    auto h1_bad = [&]() { return __any(quadsum(ck4(hp[0]) + ck4(hp[1]) + ck4(hp[2]) + h1k - hc) != 0); };
    if (__builtin_expect(h1_bad(), 0)) {
      do { __builtin_amdgcn_s_sleep(2); h1_load(); ++tries; } while (h1_bad() && tries < SPIN_CAP);
    }
    STAMP(7);
    PAD(9);
    {
      const f32x4 z = {0, 0, 0, 0}, s0 = mfma(selB, hp[1], mfma(selA, hp[0], z)), s2 = mfma(selH, hp[2], z);
      *(LAS v2u*)(size_t)(rho) = v2u{pk(gelu(s0.x), gelu(s0.y)), pk(gelu(s0.z), gelu(s0.w))};
      PAD(10);
      if (wv >= 4) *(LAS u32*)(size_t)(rhoh) = pk(gelu(s2.x), gelu(s2.y));
    }
    STAMP(8);
    PAD(11);
    bar();
    STAMP(9);
    {
      f32x4 a0 = {0, 0, 0, 0}, a1 = {0, 0, 0, 0};
      v4u A[17];
      static_for<0, AW>([&](auto Q) { A[Q] = lds128(ar + ((Q + 1) >> 2) * 288 + ((Q + 1) & 3) * 64); });
      asm volatile("s_nop 1" : "+v"(a0), "+v"(a1));
      fence_sched();
      static_for<0, 15>([&](auto T) {
        mfma_ip(a0, W2[T], A[T]);
        mfma_ip(a1, W2[T], A[T + 2]);
        if constexpr (T + AW < 17) A[T + AW] = lds128(ar + ((T + AW + 1) >> 2) * 288 + ((T + AW + 1) & 3) * 64);
        if constexpr (T + NA < 15) rdw2(IC<T + NA>{});
        if constexpr (T == 8) PAD(12);
        fence_sched();
      });
      asm volatile("s_nop 7" ::: "memory");
      fence_sched();
      put(pack2(a0, a1), rws, p2v, p2c, key);
    }
    STAMP(10);
    PAD(13);
    if (!p3ok) { DUMP(); return; }
    bar_raw();
    v4u WF[8][2];
    u32 wr[4];
    wr[0] = opq(LB + L_RING + (4 * g + q4) * 512 + (((2 * wv) ^ ((4 * g + q4) & 7)) * 32) + p4 * 8);
    wr[1] = opq(wr[0] ^ 32); wr[2] = opq(wr[0] + 65536); wr[3] = opq(wr[1] + 65536);
    auto rdwf = [&](auto K) { static_for<0, 2>([&](auto T) { const u32 ad = wr[(K >> 2) * 2 + T] + (K & 3) * 16384; WF[K][T] = ldstr2(ad, ad + 8192); }); };
    static_assert(NA >= 8 && NA <= 9, "slots 0..7 of w_f land before PE");
    static_for<0, 4>(rdwf);
    PAD(14);
    const u32 h2off = opq(WS_P2 + (lane >> 3) * 262144 + (pi >> 4) * 16384 + (wv * 2 + ((lane >> 2) & 1)) * 1024 + (pi & 15) * 64 + p4 * 16), h2c = opq(ckoff(h2off));
    const u32 fw = opq(LB + L_F + (lane >> 5) * 576 + (wv * 32 + p4 * 8 + ((lane >> 2) & 1) * 4 + ((lane >> 3) & 3)) * 2);
    const u32 fr = opq(LB + L_F + (l15 >> 3) * 576 + g * 16), jw = opq(LB + L_J + lane * 16 + wv * 1024);
    nap<DLY2>();
    PAD(15);
    bar();
    STAMP(11);
    {
      v4u fp = ld16(rws, h2off);
      u32 fc = ldck(rws, h2c);
      mem_order();
      warm<2>(wv);
      static_for<4, 8>(rdwf);
      static_for<0, 2>([&](auto T) { ge[T] = lds128(LB + L_J + (geoff & 8191) + T * 512); gc[T] = *(const LAS u32*)(size_t)(LB + L_GC + ((ckoff(geoff) & 511) + T * 32)); });
    for (u32 spins = 0; __any(((groupsum(ck4(ge[0])) + key) ^ gc[0]) | ((groupsum(ck4(ge[1])) + key) ^ gc[1])) && spins < SPIN_CAP; ++spins) {
      __builtin_amdgcn_s_sleep(2);
      static_for<0, 2>([&](auto T) { ge[T] = ld16(rws, geoff + T * 512); gc[T] = ldck(rws, ckoff(geoff + T * 512)); });
      mem_order();
      tries += 65536;
    }
      PAD(16);
      if (__builtin_expect(__any(quadsum(ck4(fp)) + key != fc), 0)) {
        u32 spins = 0;
        do {
          __builtin_amdgcn_s_sleep(2);
          fp = ld16(rws, h2off);
          fc = ldck(rws, h2c);
          mem_order();
          tries += 256;
        } while (__any(quadsum(ck4(fp)) + key != fc) && ++spins < SPIN_CAP);
      }
      STAMP(12);
      PAD(17);
      const f32x4 s = mfma(selH, fp, f32x4{0, 0, 0, 0});
      const float snd = (lane & 8) ? s.x : s.y, kp = (lane & 8) ? s.y : s.x;
      const float tot = kp + asf(__builtin_amdgcn_update_dpp(0, (int)asu(snd), 0x128, 0xf, 0xf, false));
      *(LAS u16*)(size_t)(fw) = (u16)pk(gelu(tot), 0.f);
    }
    PAD(18);
    STAMP(14);
    bar();
    STAMP(6);
    {
      f32x4 d0 = __builtin_bit_cast(f32x4, ge[0]), d1 = __builtin_bit_cast(f32x4, ge[1]);
      v4u b[8];
      static_for<0, 8>([&](auto K) { b[K] = lds128(fr + K * 64); });
      fence_sched();
      PAD(19);
      static_for<0, 8>([&](auto K) { d0 = mfma(WF[K][0], b[K], d0); });
      fence_sched();
      float t0[4];
      static_for<0, 8>([&](auto K) {
        d1 = mfma(WF[K][1], b[K], d1);
        if constexpr (K >= 3 && K < 7) t0[K - 3] = tanh_(d0[K - 3]);
        fence_sched();
      });
      PAD(20);
      *(LAS v4u*)(size_t)(jw) = v4u{pk(t0[0], t0[1]), pk(t0[2], t0[3]), pk(tanh_(d1.x), tanh_(d1.y)), pk(tanh_(d1.z), tanh_(d1.w))};
    }
    STAMP(13);
    bar();
  }
  DUMP();
}

extern "C" void kernel_launch(void* const* d_in, const int* in_sizes, int n_in, void* d_out, int out_size, void* d_ws, size_t ws_size,
                              hipStream_t stream) {
  Args a{(const u16*)d_in[0], (const u16*)d_in[1], (const u16*)d_in[2], (const u16*)d_in[3], (const u16*)d_in[4],
         (const u16*)d_in[5], (const u16*)d_in[6], (u16*)d_out, (unsigned char*)d_ws};
  rnnt_fused<<<256, 768, 0, stream>>>(a);
}
```
